```python
import torch

def _softmax(x, axis=-1):
    return torch.softmax(x, dim=axis)
def _gelu(x):
    return torch.nn.functional.gelu(x, approximate="tanh")
def _silu(x):
    return torch.nn.functional.silu(x)
def _sigmoid(x):
    return torch.sigmoid(x)
def _layernorm(x, axis=-1, eps=1e-5):
    m = x.mean(dim=axis, keepdim=True)
    v = x.var(dim=axis, keepdim=True, unbiased=False)
    return (x - m) / torch.sqrt(v + eps)
def _scatter_add(tgt, idx, src, axis=0):
    return tgt.clone().index_add_(axis, idx, src)
def _segment_sum(src, seg, n):
    out = torch.zeros((n,) + src.shape[1:], dtype=src.dtype)
    return out.index_add_(0, seg, src)
def _pad(x, pw):
    flat = tuple(v for pair in reversed(pw) for v in pair)
    return torch.nn.functional.pad(x, flat)

kernel_name = 'parallel_sum__ssm_conv_s4d__glu_gated_glu__prompt'

def setup_inputs():

    _w = (lambda a: (lambda t: t.to(torch.bfloat16) if t.is_floating_point() else t)(torch.as_tensor(a)))
    def _A_setup():
        Bsz, L, D, N = 1, 2048, 640, 192
        return {
            "u": _w((lambda *s: torch.randn(*s, dtype=torch.bfloat16) * 0.05)(Bsz, L, D)),
            "log_A_real": _w((lambda *s: torch.randn(*s, dtype=torch.bfloat16) * 3.2)(D, N) * 0.5),
            "A_imag": _w((lambda *s: torch.randn(*s, dtype=torch.bfloat16) * 0.1)(D, N) * 3.14),
            "C_real": _w((lambda *s: torch.randn(*s, dtype=torch.bfloat16) * 1.6)(D, N) / N ** 0.5),
            "C_imag": _w((lambda *s: torch.randn(*s, dtype=torch.bfloat16) * 1.6)(D, N) / N ** 0.5),
            "log_dt": _w((lambda *s: torch.randn(*s, dtype=torch.bfloat16) * 0.1)(D) * 0.5 - 4.0),
            "D_skip": _w((lambda *s: torch.randn(*s, dtype=torch.bfloat16) * 0.2)(D)),
        }
    _A_in = _A_setup()

    def _B_setup():
        B, S, D, F = 1, 2048, 640, 1536
        return {
            "x": _w((lambda *s: torch.randn(*s, dtype=torch.bfloat16) * 0.05)(B, S, D)),
            "w_gate": _w((lambda *s: torch.randn(*s, dtype=torch.bfloat16) * 0.05)(D, F)),
            "w_up": _w((lambda *s: torch.randn(*s, dtype=torch.bfloat16) * 0.05)(D, F)),
            "w_down": _w((lambda *s: torch.randn(*s, dtype=torch.bfloat16) * 0.05)(F, D)),
        }
    _B_in = _B_setup()

    return {
        "A_u": _A_in["u"],
        "A_log_A_real": _A_in["log_A_real"],
        "A_A_imag": _A_in["A_imag"],
        "A_C_real": _A_in["C_real"],
        "A_C_imag": _A_in["C_imag"],
        "A_log_dt": _A_in["log_dt"],
        "A_D_skip": _A_in["D_skip"],
        "B_x": _B_in["x"],
        "B_w_gate": _B_in["w_gate"],
        "B_w_up": _B_in["w_up"],
        "B_w_down": _B_in["w_down"],
    }

def reference(A_u, A_log_A_real, A_A_imag, A_C_real, A_C_imag, A_log_dt, A_D_skip, B_x, B_w_gate, B_w_up, B_w_down):
    def _A_ref(u, log_A_real, A_imag, C_real, C_imag, log_dt, D_skip):
        L = u.shape[1]
        dt = torch.exp(log_dt)
        A = -torch.exp(log_A_real) + 1j * A_imag
        C = (C_real + 1j * C_imag) * dt[:, None]
        pos = (lambda x, dt: x.to(dt))(torch.arange(L), log_dt.dtype)
        P = torch.exp((A * dt[:, None])[:, :, None] * pos[None, None, :])
        K = 2.0 * torch.einsum("dn,dnl->dl", C, P).real
        L2 = 2 * L
        u_t = (lambda x, *d: x.permute(*d))(u, 0, 2, 1)
        Kf = (lambda x, *a, **k: torch.fft.rfft(x.float(), *a, **k))(K, L2)
        Uf = (lambda x, *a, **k: torch.fft.rfft(x.float(), *a, **k))(u_t, L2)
        y = (lambda x, *a, **k: torch.fft.irfft(x, *a, **k).to(torch.bfloat16))(Uf * Kf[None, :, :], L2)[:, :, :L]
        y = (lambda x, *d: x.permute(*d))(y, 0, 2, 1)
        return y + u * D_skip[None, None, :]

    def _B_ref(x, w_gate, w_up, w_down):
        g = torch.einsum("bsd,df->bsf", x, w_gate)
        u = torch.einsum("bsd,df->bsf", x, w_up)
        h = _sigmoid(g) * u
        return torch.einsum("bsf,fd->bsd", h, w_down)

    x = A_u
    xn = _layernorm(x, axis=-1)
    return x + _A_ref(xn, A_log_A_real, A_A_imag, A_C_real, A_C_imag, A_log_dt, A_D_skip) + _B_ref(xn, B_w_gate, B_w_up, B_w_down)
```

```cpp
#include <hip/hip_runtime.h>
#include <hip/hip_bf16.h>
#include <stdint.h>

typedef unsigned short u16;
constexpr int L = 2048, D = 640, N = 192, F = 1536;

__device__ __forceinline__ float bf2f(u16 x) { return __uint_as_float(((unsigned)x) << 16); }
__device__ __forceinline__ u16 f2bf(float x) { unsigned u = __float_as_uint(x); u += 0x7fffu + ((u >> 16) & 1u); return (u16)(u >> 16); }
__device__ __forceinline__ float rbf(float x) { return bf2f(f2bf(x)); }

__global__ void k_ln(const u16* x, u16* xn) {
  int row = blockIdx.x, lane = threadIdx.x;
  float v[10]; float s = 0;
  for (int i = 0; i < 10; ++i) { v[i] = bf2f(x[row * D + lane + 64 * i]); s += v[i]; }
  for (int m = 32; m >= 1; m >>= 1) s += __shfl_xor(s, m);
  float mean = s / D;
  float q = 0;
  for (int i = 0; i < 10; ++i) { float d = v[i] - mean; q += d * d; }
  for (int m = 32; m >= 1; m >>= 1) q += __shfl_xor(q, m);
  float var = rbf(q / D);
  float mb = rbf(mean);
  float sd = rbf(sqrtf(rbf(var + 1e-5f)));
  for (int i = 0; i < 10; ++i) xn[row * D + lane + 64 * i] = f2bf(rbf(v[i] - mb) / sd);
}

__global__ void k_kgen(const u16* lar, const u16* aim, const u16* cr, const u16* ci, const u16* ldt, float* K) {
  int d = blockIdx.x;
  float dt = rbf(expf(bf2f(ldt[d])));
  for (int l = threadIdx.x; l < L; l += blockDim.x) {
    float pos = rbf((float)l);
    float acc = 0;
    for (int n = 0; n < N; ++n) {
      float a = -rbf(expf(bf2f(lar[d * N + n]))) * dt;
      float b = bf2f(aim[d * N + n]) * dt;
      float c_r = bf2f(cr[d * N + n]) * dt, c_i = bf2f(ci[d * N + n]) * dt;
      float e = expf(a * pos); float sn, cs; sincosf(b * pos, &sn, &cs);
      acc += e * (c_r * cs - c_i * sn);
    }
    K[d * L + l] = 2.f * acc;
  }
}

__global__ void k_conv(const u16* xn, const float* K, u16* y) {
  int d = blockIdx.x;
  __shared__ float su[L], sk[L];
  for (int t = threadIdx.x; t < L; t += blockDim.x) { su[t] = bf2f(xn[t * D + d]); sk[t] = K[d * L + t]; }
  __syncthreads();
  for (int t = threadIdx.x; t < L; t += blockDim.x) {
    float acc = 0;
    for (int l = 0; l <= t; ++l) acc += sk[l] * su[t - l];
    y[t * D + d] = f2bf(acc);
  }
}

__global__ void k_g1(const u16* xn, const u16* wg, const u16* wu, u16* h) {
  int f = blockIdx.x * blockDim.x + threadIdx.x; int t = blockIdx.y;
  float g = 0, u = 0;
  for (int k = 0; k < D; ++k) { float a = bf2f(xn[t * D + k]); g += a * bf2f(wg[k * F + f]); u += a * bf2f(wu[k * F + f]); }
  g = rbf(g); u = rbf(u);
  float s = rbf(1.f / (1.f + expf(-g)));
  h[t * F + f] = f2bf(s * u);
}

__global__ void k_g2(const u16* x, const u16* xn, const u16* y, const u16* h, const u16* wd, const u16* dsk, u16* out) {
  int d = blockIdx.x * blockDim.x + threadIdx.x; int t = blockIdx.y;
  float b = 0;
  for (int k = 0; k < F; ++k) b += bf2f(h[t * F + k]) * bf2f(wd[k * D + d]);
  b = rbf(b);
  float a = rbf(bf2f(y[t * D + d]) + rbf(bf2f(xn[t * D + d]) * bf2f(dsk[d])));
  float o = rbf(bf2f(x[t * D + d]) + a);
  out[t * D + d] = f2bf(o + b);
}

extern "C" void kernel_launch(void* const* d_in, const int* in_sizes, int n_in, void* d_out, int out_size, void* d_ws, size_t ws_size, hipStream_t stream) {
  const u16* x = (const u16*)d_in[0];
  char* ws = (char*)d_ws;
  u16* xn = (u16*)ws; u16* y = (u16*)(ws + (4 << 20)); u16* h = (u16*)(ws + (8 << 20)); float* K = (float*)(ws + (16 << 20));
  k_ln<<<L, 64, 0, stream>>>(x, xn);
  k_kgen<<<D, 256, 0, stream>>>((const u16*)d_in[1], (const u16*)d_in[2], (const u16*)d_in[3], (const u16*)d_in[4], (const u16*)d_in[5], K);
  k_conv<<<D, 256, 0, stream>>>(xn, K, y);
  k_g1<<<dim3(F / 256, L), 256, 0, stream>>>(xn, (const u16*)d_in[8], (const u16*)d_in[9], h);
  k_g2<<<dim3(D / 128, L), 128, 0, stream>>>(x, xn, y, h, (const u16*)d_in[10], (const u16*)d_in[6], (u16*)d_out);
}
```

```cpp
#include <hip/hip_runtime.h>
#include <stdint.h>

#if defined(__HIP_DEVICE_COMPILE__)
#pragma clang attribute push(__attribute__((target("no-packed-fp32-ops"))), apply_to = function)
#endif
typedef unsigned short u16;
typedef unsigned long long u64;
typedef short bf16x8 __attribute__((ext_vector_type(8)));
typedef _Float16 f16x8 __attribute__((ext_vector_type(8)));
typedef short s16x4 __attribute__((ext_vector_type(4)));
typedef float f32x2 __attribute__((ext_vector_type(2)));
typedef float f32x4 __attribute__((ext_vector_type(4)));
typedef unsigned u32x2 __attribute__((ext_vector_type(2)));
typedef unsigned u32x4 __attribute__((ext_vector_type(4)));
typedef _Float16 h16x2 __attribute__((ext_vector_type(2)));
typedef __bf16 b16x2 __attribute__((ext_vector_type(2)));
#define LAS __attribute__((address_space(3)))
#define GAS __attribute__((address_space(1)))
typedef LAS char* lptr;
#define DI __device__ __forceinline__
#define SBAR() __builtin_amdgcn_sched_barrier(0)
#define SGB(m, n) __builtin_amdgcn_sched_group_barrier(m, n, 0)
#define MFMA_BF(a, b, c) __builtin_amdgcn_mfma_f32_16x16x32_bf16(__builtin_bit_cast(bf16x8, a), __builtin_bit_cast(bf16x8, b), c, 0, 0, 0)
#define MFMA_H(a, b, c) __builtin_amdgcn_mfma_f32_16x16x32_f16(__builtin_bit_cast(f16x8, a), __builtin_bit_cast(f16x8, b), c, 0, 0, 0)
#define SC1 16
#define EXP 0

constexpr int DM = 640, NM = 192;
constexpr unsigned WS_EPOCH = 0, WS_F0 = 4096, WS_FH = 8192, WS_FY = 16384;
constexpr unsigned WS_ST = 65536, WS_CS = 98304, WS_XB = 262144, WS_WD = WS_XB + 2621440 + 4096;
constexpr unsigned WS_H = WS_WD + 1966080, WS_Y = WS_H + 6291456, WS_END = WS_Y + 2621440;
constexpr unsigned WS_STAMP = 16u << 20;
constexpr int STAGE = 40960, SL1 = 0, SA = 40960, SL0 = 81920, SL2 = 122880;
constexpr int PROW = 784, PCH = 23 * PROW;
constexpr int KC1 = 4228, O_KREV = 64, O_KL = 8464;
constexpr int L_FLG = SA + 2 * PCH, L_SC = L_FLG + 128, L_STG = L_FLG + 192, STGW = 2304;
constexpr int L_STAT = L_STG, L_CS = L_STG + 1024;
constexpr int UCH = 9232, UC1 = 4640;
constexpr int L_U = SL2 + 64, L_KC = L_U + 3 * UCH + 64, L_PC = L_KC + 8400;
constexpr int L_PLC = SL1, L_KPC = SL1 + PCH, L_KLC = L_KPC + 9216;
constexpr int L_BASE = SA, L_YB = SA + 10240;
constexpr int LDS_BYTES = 163840;
static_assert(L_PC + 4096 <= LDS_BYTES && L_STG + 2 * STGW <= SL0 && O_KL + 8192 <= PCH && (L_KC & 15) == 0 && (L_PC & 15) == 0 && (L_STG & 15) == 0, "lds map");
enum { FL_F0 = 0, FL_EPI, FL_U, FL_KA, FL_KB, FL_KC, FL_KP, FL_PA, FL_PB, FL_PC, FL_YC, FL_YOK, FL_H1, FL_H2, FL_G1, FL_TAG = 16 };
constexpr int SPIN_LDS = 1 << 18, SPIN_GLB = 1 << 15;

DI bool wall(bool p) { return __builtin_amdgcn_ballot_w64(!p) == 0; }
DI unsigned umin(unsigned a, unsigned b) { return a < b ? a : b; }
DI unsigned umax(unsigned a, unsigned b) { return a > b ? a : b; }
DI float asf(unsigned v) { return __builtin_bit_cast(float, v); }
DI unsigned asu(float v) { return __builtin_bit_cast(unsigned, v); }
DI float bflo(unsigned v) { return asf(v << 16); }
DI float bfhi(unsigned v) { return asf(v & 0xffff0000u); }
DI float bf1(u16 v) { return asf((unsigned)v << 16); }
DI unsigned pkbf(float lo, float hi) { return __builtin_bit_cast(unsigned, __builtin_convertvector(f32x2{lo, hi}, b16x2)); }
DI unsigned pkh(f32x2 v) { return __builtin_bit_cast(unsigned, __builtin_convertvector(v, h16x2)); }
DI float rbf(float x) { return bflo(pkbf(x, x)); }
template <int CTRL> DI float dppf(float x) { return __builtin_bit_cast(float, __builtin_amdgcn_mov_dpp(__builtin_bit_cast(int, x), CTRL, 0xf, 0xf, true)); }
DI float row16_sum(float v) { v += dppf<0xB1>(v); v += dppf<0x4E>(v); v += dppf<0x124>(v); v += dppf<0x128>(v); return v; }

DI void glds4(const void* sb, unsigned v0, unsigned v1, unsigned v2, unsigned v3, unsigned lds) {
  asm volatile("s_mov_b32 m0, %5\n\ts_nop 0\n\tglobal_load_lds_dwordx4 %0, %4\n\tglobal_load_lds_dwordx4 %1, %4 offset:1024\n\t"
               "global_load_lds_dwordx4 %2, %4 offset:2048\n\tglobal_load_lds_dwordx4 %3, %4 offset:3072"
               :: "v"(v0), "v"(v1), "v"(v2), "v"(v3), "s"(sb), "s"(lds) : "memory");
}
DI void glds2(const void* sb, unsigned v0, unsigned v1, unsigned lds) {
  asm volatile("s_mov_b32 m0, %3\n\ts_nop 0\n\tglobal_load_lds_dwordx4 %0, %2\n\tglobal_load_lds_dwordx4 %1, %2 offset:1024"
               :: "v"(v0), "v"(v1), "s"(sb), "s"(lds) : "memory");
}
template <int N> DI void waitv() { asm volatile("s_waitcnt vmcnt(%0)" :: "i"(N) : "memory"); }
DI void waitl() { asm volatile("s_waitcnt lgkmcnt(0)" ::: "memory"); }
DI u32x2 vtr(lptr p) { return __builtin_bit_cast(u32x2, __builtin_amdgcn_ds_read_tr16_b64_v4i16((LAS s16x4*)p)); }
DI u32x4 tr2(lptr p, int d) { u32x2 lo = vtr(p), hi = vtr(p + d); return u32x4{lo[0], lo[1], hi[0], hi[1]}; }
DI u32x4 ld16(lptr p) { return *(LAS u32x4*)p; }
DI void st16(lptr p, u32x4 v) { *(LAS u32x4*)p = v; }

DI unsigned flag_ld(unsigned addr) { unsigned r; asm volatile("ds_read_b32 %0, %1\n\ts_waitcnt lgkmcnt(0)" : "=v"(r) : "v"(addr) : "memory"); return __builtin_amdgcn_readfirstlane(r); }
DI void flag_wait(unsigned fb, int idx, unsigned need) { _Pragma("nounroll") for (int sp = 0; sp < SPIN_LDS && flag_ld(fb + idx * 4) < need; ++sp) __builtin_amdgcn_s_sleep(1); }
DI void flag_set(unsigned fb, int idx, unsigned v) {
  asm volatile("s_waitcnt lgkmcnt(0)\n\ts_mov_b64 exec, 1\n\tds_write_b32 %0, %1\n\ts_mov_b64 exec, -1" :: "v"(fb + idx * 4), "v"(v) : "memory"); }
DI unsigned flag_add(unsigned fb, int idx, int lane) {
  unsigned r; (void)lane;
  asm volatile("s_waitcnt lgkmcnt(0)\n\ts_mov_b64 exec, 1\n\tds_add_rtn_u32 %0, %1, %2\n\ts_mov_b64 exec, -1\n\ts_waitcnt lgkmcnt(0)" : "=&v"(r) : "v"(fb + idx * 4), "v"(1u) : "memory");
  return __builtin_amdgcn_readfirstlane(r);
}
#define RLX_AGENT __ATOMIC_RELAXED, __HIP_MEMORY_SCOPE_AGENT
DI bool gflag_ok(const char* ws, unsigned off, u64 tag) { return __hip_atomic_load((GAS u64*)(ws + off), RLX_AGENT) == tag; }

#define STAMP(i) do {} while (0)

DI f32x2 csq(f32x2 x) { const float t = x.x + x.x; return f32x2{__builtin_fmaf(x.x, x.x, -(x.y * x.y)), t * x.y}; }
DI h16x2 toh(f32x2 v) { return __builtin_convertvector(v, h16x2); }
DI h16x2 hmul(h16x2 x, h16x2 y) { return __builtin_elementwise_fma(h16x2{x.y, x.y}, h16x2{-y.y, y.x}, h16x2{x.x, x.x} * y); }
DI u32x4 hmul4(u32x4 x, u32x4 y) {
  u32x4 o;
#pragma unroll
  for (int e = 0; e < 4; ++e) { const unsigned xe = x[e], ye = y[e]; o[e] = __builtin_bit_cast(unsigned, hmul(__builtin_bit_cast(h16x2, xe), __builtin_bit_cast(h16x2, ye))); }
  return o;
}
DI f32x2 cexp_p(float a2, float bh, float bl, float p) {
  const float e = __builtin_amdgcn_exp2f(a2 * p);
  const float ph = __builtin_fmaf(bl, p, __builtin_amdgcn_fractf(bh * p));
  return f32x2{e * __builtin_amdgcn_cosf(ph), e * __builtin_amdgcn_sinf(ph)};
}
template <int ROW0> DI void put4(unsigned tb, h16x2 a, h16x2 b, h16x2 c, h16x2 d) {
  asm volatile("s_mov_b32 m0, %4\n\ts_nop 0\n\tds_write_addtid_b32 %0 offset:%5\n\tds_write_addtid_b32 %1 offset:%5+784\n\t"
               "ds_write_addtid_b32 %2 offset:%5+1568\n\tds_write_addtid_b32 %3 offset:%5+2352"
               :: "v"(a), "v"(b), "v"(c), "v"(d), "s"(tb), "i"(ROW0 * PROW) : "memory");
}
DI float kpool(unsigned tb, float lar, float aim, float cr, float ci, float ldt) {
  const float dt = rbf(__builtin_amdgcn_exp2f(ldt * 1.44269504f));
  const float ea = rbf(__builtin_amdgcn_exp2f(lar * 1.44269504f));
  const float a2 = -(ea * dt) * 1.44269504f;
  const float b = aim * dt;
  const float bh = b * 0.15915494f;
  const float bl = __builtin_fmaf(b, 6.4206382e-9f, __builtin_fmaf(b, 0.15915494f, -bh));
  unsigned mi = umax(asu(cr) & 0x7fffffffu, asu(ci) & 0x7fffffffu);
  mi = umax(mi, asu(dppf<0xB1>(asf(mi)))); mi = umax(mi, asu(dppf<0x4E>(asf(mi)))); mi = umax(mi, asu(dppf<0x124>(asf(mi)))); mi = umax(mi, asu(dppf<0x128>(asf(mi))));
  unsigned eb = umax(umax((unsigned)__builtin_amdgcn_readlane(mi, 0), (unsigned)__builtin_amdgcn_readlane(mi, 16)),
                     umax((unsigned)__builtin_amdgcn_readlane(mi, 32), (unsigned)__builtin_amdgcn_readlane(mi, 48))) >> 23;
  eb = umin(umax(eb, 2u), 252u);
  const float cs = asf((254u - eb) << 23);
  const f32x2 f8 = cexp_p(a2, -bh, -bl, 8.f), f16 = csq(f8), f32 = csq(f16), f64 = csq(f32);
  const h16x2 w8 = toh(f8), w16 = toh(f16), w32 = toh(f32), w64 = toh(f64);
  put4<0>(tb, h16x2{(_Float16)1.f, (_Float16)0.f}, w8, w16, hmul(w8, w16));
  const f32x2 f1 = cexp_p(a2, bh, bl, 1.f), f2 = csq(f1), f4 = csq(f2);
  const h16x2 w1 = toh(f1), w2 = toh(f2), w4 = toh(f4);
  put4<4>(tb, w32, w64, hmul(w32, w64), w1);
  const h16x2 C = toh(f32x2{cr * cs, ci * cs});
  put4<8>(tb, w2, hmul(w1, w2), C, hmul(C, w4));
  const f32x2 f128 = cexp_p(a2, bh, bl, 128.f), f256 = csq(f128), f512 = csq(f256);
  const h16x2 w128 = toh(f128), w256 = toh(f256), w512 = toh(f512), c128 = hmul(C, w128);
  put4<12>(tb, c128, hmul(c128, w4), w256, hmul(w256, w2));
  const h16x2 w1024 = toh(cexp_p(a2, bh, bl, 1024.f)), w1280 = hmul(w1024, w256);
  put4<16>(tb, w512, hmul(w512, w256), w1024, w1280);
  const h16x2 w1536 = hmul(w1024, w512), w1792 = hmul(w1536, w256);
  put4<19>(tb, w1280, w1536, w1792, hmul(w1792, w4));
  return 2.f * dt * asf(eb << 23);
}
template <int NS> DI void koper(lptr pool, int lane, f32x4 (&acc)[3]) {
  const int r = lane & 15, x = r & 3, y = r >> 2;
  lptr p = pool + (lane >> 4) * 16;
  lptr pbx = p + x * PROW, pby = p + (y ? 3 + y : 0) * PROW, py = p + (10 + y) * PROW;
  lptr px0 = p + (x ? 6 + x : 0) * PROW, px2 = p + (y == 3 ? 22 : 18 + x) * PROW;
  u32x4 v[NS][6];
#pragma unroll
  for (int s = 0; s < NS; ++s) { v[s][0] = ld16(pbx + 64 * s); v[s][1] = ld16(pby + 64 * s); v[s][2] = ld16(py + 64 * s);
    v[s][3] = ld16(px0 + 64 * s); v[s][4] = ld16(pbx + 14 * PROW + 64 * s); v[s][5] = ld16(px2 + 64 * s); }
#pragma unroll
  for (int s = 0; s < NS; ++s) {
    v[s][0] = hmul4(v[s][0], v[s][1]);
#pragma unroll
    for (int i = 0; i < 3; ++i) v[s][3 + i] = hmul4(v[s][3 + i], v[s][2]);
    asm volatile("" : "+v"(v[s][0]), "+v"(v[s][3]), "+v"(v[s][4]), "+v"(v[s][5]));
  }
  SBAR(); __builtin_amdgcn_s_setprio(3); SBAR();
#pragma unroll
  for (int s = 0; s < NS; ++s)
#pragma unroll
    for (int i = 0; i < 3; ++i) acc[i] = MFMA_H(v[s][3 + i], v[s][0], acc[i]);
  SBAR(); if (!(EXP & 32)) __builtin_amdgcn_s_setprio(0); SBAR();
}
DI void kstore(lptr kl, int lane, const f32x4 (&acc)[3], float dsk) {
  const int c = lane & 15, q = lane >> 4;
  LAS u16* t = (LAS u16*)kl;
  const float k0 = acc[0][0] + (lane == 0 ? dsk : 0.f);
  *(LAS u32x2*)(t + 128 * (q >> 1) + 8 * c + 4 * (q & 1)) = u32x2{pkbf(k0, acc[0][1]), pkbf(acc[0][2], acc[0][3])};
  *(LAS unsigned*)(t + 256 + 64 * (q >> 1) + 2 * (q & 1) + 4 * c) = pkbf(acc[1][0], acc[1][1]);
  const unsigned p1 = pkbf(acc[1][2], acc[1][3]);
  const int i1 = 384 + 32 * (q >> 1) + (q & 1) + 2 * c;
  t[i1] = (u16)p1; t[i1 + 64] = (u16)(p1 >> 16);
  const unsigned r0 = pkbf(acc[2][0], acc[2][1]), r1 = pkbf(acc[2][2], acc[2][3]);
  if (!(q & 1)) { const int i2 = 512 + 8 * q + c; t[i2] = (u16)r0; t[i2 + 32] = (u16)(r0 >> 16); t[i2 + 64] = (u16)r1; t[i2 + 96] = (u16)(r1 >> 16); }
  else if (lane == 63) t[640] = (u16)r0;
}
DI unsigned dup16(unsigned a) { return a | (a << 16); }
DI unsigned bsel(unsigned m, unsigned a, unsigned b) { asm("" : "+v"(m)); return (a & m) | (b & ~m); }
DI unsigned rot16(unsigned a) { return __builtin_amdgcn_alignbit(a, a, 16); }
DI void kexpand(lptr kr, lptr kl, int lane) {
  LAS u16* t = (LAS u16*)kl;
  auto put = [&](int ch, unsigned dp, unsigned d0, unsigned d1, unsigned d2, unsigned d3) {
    st16(kr + 16 * ch, u32x4{d0, d1, d2, d3}); st16(kr + KC1 - 4 + 16 * ch, u32x4{dp, d0, d1, d2}); };
  const int n = 63 - lane, n0 = n & 31;
  const unsigned a0 = t[576 + n], b0 = t[577 + n], a1 = t[512 + n], b1 = t[513 + n];
  const unsigned ab = *(LAS unsigned*)(t + 384 + 2 * n), c2 = t[386 + 2 * n];
  const u32x2 v1 = *(LAS u32x2*)(t + 256 + 4 * n0); const unsigned q4 = t[260 + 4 * n0];
  const u32x4 v0 = ld16((lptr)(t + 8 * n0)); const unsigned w = *(LAS unsigned*)(t + 8 * n0 + 8);
  const unsigned mo = (n & 1) ? ~0u : 0u, lo = lane >= 32 ? ~0u : 0u, m31 = n == 31 ? ~0u : 0u;
  put(lane, dup16(b0), dup16(b0), bsel(mo, dup16(b0), b0 | (a0 << 16)), dup16(a0), dup16(a0));
  put(64 + lane, dup16(b1), dup16(b1), bsel(mo, dup16(b1), b1 | (a1 << 16)), dup16(a1), dup16(a1));
  put(128 + lane, dup16(c2), dup16(c2), dup16(ab >> 16), rot16(ab), dup16(ab & 0xffffu));
  put(192 + lane, bsel(lo, bsel(m31, dup16(w & 0xffffu), rot16(w)), dup16(q4)), bsel(lo, rot16(v0[3]), q4 | (v1[1] & 0xffff0000u)),
      bsel(lo, rot16(v0[2]), dup16(v1[1] & 0xffffu)), bsel(lo, rot16(v0[1]), (v1[1] & 0xffffu) | (v1[0] & 0xffff0000u)), bsel(lo, rot16(v0[0]), dup16(v1[0] & 0xffffu)));
  if (lane >= 60) put(319 - lane, lane == 63 ? rot16(v0[0]) : 0u, 0u, 0u, 0u, 0u);
}

template <int TMASK>
DI void conv_units(lptr kr, lptr ub, int lane, int rho0, int nrho, f32x4 (&acc)[8]) {
  constexpr int MAXT = (TMASK & 0x80) ? 7 : 5;
  const int r = lane & 15, q = lane >> 4;
  lptr pk = kr + ((r & 1) ? 0 : KC1) + 4092 - 4 * r + 16 * q - 3584 - 64 * rho0;
  lptr pu = ub + ((r & 1) ? UC1 : 0) + 64 * (r >> 1) + 16 * q + 64 * (7 - rho0);
  u32x4 Kf[8], Uf[8];
  lptr pk2 = pk + 8; asm volatile("" : "+v"(pk2));
  auto ldk = [&](int m) { u32x2 lo = *(LAS u32x2*)(pk + 512 * (7 - m)), hi = *(LAS u32x2*)(pk2 + 512 * (7 - m)); return u32x4{lo[0], lo[1], hi[0], hi[1]}; };
#pragma unroll
  for (int j = 0; j <= MAXT; ++j) { Uf[j] = ld16(pu + 512 * j); Kf[MAXT - j] = ldk(MAXT - j); }
#pragma unroll 1
  for (int it = 0; it < nrho; ++it) {
    pk -= 64; pk2 -= 64; pu -= 64;
#pragma unroll
    for (int m = MAXT; m >= 0; --m) {
#pragma unroll
      for (int j = 0; j + m <= MAXT; ++j) {
        if ((TMASK >> (m + j)) & 1) acc[m + j] = MFMA_BF(Uf[j], Kf[m], acc[m + j]);
        if (m == 0) { Uf[j] = ld16(pu + 512 * j); SBAR(); }
      }
      Kf[m] = ldk(m);
      SBAR();
    }
  }
}

DI u64 uni64(u64 v) { return (u64)(unsigned)__builtin_amdgcn_readfirstlane((unsigned)v) | ((u64)(unsigned)__builtin_amdgcn_readfirstlane((unsigned)(v >> 32)) << 32); }
DI int fresh_lane() { int l; asm volatile("v_mbcnt_lo_u32_b32 %0, -1, 0\n\tv_mbcnt_hi_u32_b32 %0, -1, %0" : "=v"(l)); return l; }
#define BAR() do { SBAR(); asm volatile("s_barrier" ::: "memory"); SBAR(); } while (0)
#define BAR_L() do { SBAR(); asm volatile("s_waitcnt lgkmcnt(0)\n\ts_barrier" ::: "memory"); SBAR(); } while (0)
#define LD_SC1(off) __builtin_amdgcn_raw_buffer_load_b128(rs, (off), 0, SC1)
#define ST_SC1(v, off) __builtin_amdgcn_raw_buffer_store_b128((v), rs, (off), 0, SC1)

__global__ __launch_bounds__(512, 2) void fused_kernel(const u16* __restrict__ x, const u16* __restrict__ p_lar, const u16* __restrict__ p_aim,
                                                       const u16* __restrict__ p_cr, const u16* __restrict__ p_ci, const u16* __restrict__ p_ldt,
                                                       const u16* __restrict__ p_dsk, const u16* __restrict__ wg, const u16* __restrict__ wu,
                                                       const u16* __restrict__ wd, u16* __restrict__ out, char* ws) {
  extern __shared__ __attribute__((aligned(16))) char smem[];
  const int tid = __builtin_amdgcn_workitem_id_x(), bid = __builtin_amdgcn_workgroup_id_x();
  lptr lds = (lptr)smem;
  const unsigned lds0 = (unsigned)(uintptr_t)smem;
  const unsigned fb = lds0 + L_FLG;
  const int wid = __builtin_amdgcn_readfirstlane(tid >> 6), lane = tid & 63;
  const int xcd = bid & 7, bi = bid >> 3;
  const int rb = (xcd >> 1) * 4 + (bi >> 3), cb = (xcd & 1) * 8 + (bi & 7);
  const int tg = xcd * 4 + (bi >> 3), nb = bi & 7;
  const int cw = xcd * 32 + bi, odd = cw & 1, c0 = 5 * (cw >> 1) + 2 * odd;
  const int chA = c0 + odd, chC = odd ? c0 : c0 + 2;
  const int rot = (EXP & 2) ? 0 : rb >= 10 ? rb - 10 : rb;
  const __amdgpu_buffer_rsrc_t rs = __builtin_amdgcn_make_buffer_rsrc(ws, 0, (int)WS_END, 0x00020000);
  const int r = lane & 15, q = lane >> 4;

  float pv1[5], pv2[5];
  auto load_params = [&](float (&pv)[5], int ch, int t) {
    const unsigned o = (ch * NM + t * 64 + lane) * 2, oc = ch * 2;
#pragma unroll
    for (int e = 0; e < 5; ++e) pv[e] = 0.f;
    asm volatile("global_load_short_d16_hi %0, %5, %7\n\tglobal_load_short_d16_hi %1, %5, %8\n\tglobal_load_short_d16_hi %2, %5, %9\n\t"
                 "global_load_short_d16_hi %3, %5, %10\n\tglobal_load_short_d16_hi %4, %6, %11"
                 : "+v"(pv[0]), "+v"(pv[1]), "+v"(pv[2]), "+v"(pv[3]), "+v"(pv[4])
                 : "v"(o), "v"(oc), "s"(p_lar), "s"(p_aim), "s"(p_cr), "s"(p_ci), "s"(p_ldt) : "memory");
  };
#define PARAMS_LANDED(pv) do { SBAR(); asm volatile("" : "+v"(pv[0]), "+v"(pv[1]), "+v"(pv[2]), "+v"(pv[3]), "+v"(pv[4]) :: "memory"); } while (0)
  u64 tag = 0;
  const int lw = wid - 4, kw = wid - 6;

  if (wid < 6) {
    const int pc = wid >= 3, pt = wid - 3 * pc;
    load_params(pv1, chA + pc, pt);
    unsigned vx[4], vw[12];
    auto prep = [&]() {
#pragma unroll
    for (int j = 0; j < 4; ++j) { const int row = 8 * j + (lane >> 3); vx[j] = row * 1280 + (((lane & 7) ^ ((row >> 1) & 7)) << 4) + 4096 - 1024 * j; }
#pragma unroll
    for (int j = 0; j < 3; ++j) { const int c = j * 64 + lane, row = (c * 43691) >> 19, pos = c - row * 12;
      const unsigned v = row * 3072 + ((pos ^ (((row >> 3) & 1) << 1)) << 4) + 4096;
#pragma unroll
      for (int g = 0; g < 4; ++g) vw[3 * g + j] = v + g * 16 * 3072 - 1024 * ((3 * g + j) & 3); }
    };
    const char* px = (const char*)x + (size_t)(rb * 128 + lw * 64) * 1280 - 4096;
    const char* pw = (const char*)(lw ? wu : wg) + cb * 192 - 4096;
    auto issue = [&](int s, int sbase) {
      int ks = s + rot; if (ks >= 10) ks -= 10;
      const unsigned base = lds0 + sbase;
      const char* sx = px + ks * 128; const char* sw = pw + (size_t)ks * 64 * 3072;
      glds4(sx, vx[0], vx[1], vx[2], vx[3], base + lw * 8192);
      glds4(sw, vw[0], vw[1], vw[2], vw[3], base + 16384 + lw * 12288);
      glds4(sx + 32 * 1280, vx[0], vx[1], vx[2], vx[3], base + lw * 8192 + 4096);
      glds4(sw, vw[4], vw[5], vw[6], vw[7], base + 16384 + lw * 12288 + 4096);
      glds4(sw, vw[8], vw[9], vw[10], vw[11], base + 16384 + lw * 12288 + 8192);
    };
    if (wid < 4) waitv<0>();
    else { prep(); issue(0, SL0); waitv<20>(); }
    PARAMS_LANDED(pv1);
    STAMP(0);
    const float sc = kpool(lds0 + SA + pc * PCH + pt * 256, pv1[0], pv1[1], pv1[2], pv1[3], pv1[4]);
    if (lane == 0) *(LAS float*)(lds + L_SC + wid * 4) = sc;
    STAMP(1);

    if (wid < 4) {
      if (!(EXP & 4)) __builtin_amdgcn_s_setprio(2);
      const int tw = wid >> 1, fw = wid & 1;
      lptr pB0 = lds + (tw * 64 + r) * 128 + ((q ^ (r >> 1)) << 4);
      lptr pB1 = lds + (tw * 64 + r) * 128 + (((q ^ (r >> 1)) ^ 4) << 4);
      const int sg = (q & 1) * 32;
      lptr pAb = lds + 16384 + (8 * q + (r >> 2)) * 192 + 8 * (lane & 3);
      lptr pA[3] = {pAb + (fw ? 160 - sg : sg), pAb + (fw ? 128 + sg : 32 - sg), pAb + (fw ? 96 - sg : 64 + sg)};
      f32x4 acc[2][3][4];
#pragma unroll
      for (int m = 0; m < 2; ++m)
#pragma unroll
        for (int ii = 0; ii < 3; ++ii)
#pragma unroll
          for (int tb = 0; tb < 4; ++tb) { acc[m][ii][tb] = f32x4{0, 0, 0, 0}; asm volatile("" : "+v"(acc[m][ii][tb])); }
      u32x4 A0[2][3], B0[4], A1[2][3], B1[4];
      auto rd = [&](u32x4 (&A)[2][3], u32x4 (&B)[4], int off, int kk) {
#pragma unroll
        for (int tb = 0; tb < 4; ++tb) B[tb] = ld16((kk ? pB1 : pB0) + off + tb * 2048);
#pragma unroll
        for (int m = 0; m < 2; ++m)
#pragma unroll
          for (int ii = 0; ii < 3; ++ii) A[m][ii] = tr2(pA[ii] + off + m * 12288 + kk * 6144, 768);
      };
      auto mm = [&](u32x4 (&A)[2][3], u32x4 (&B)[4]) {
#pragma unroll
        for (int e = 0; e < 24; ++e) { const int m = e / 12, ii = (e / 4) % 3, tb = e % 4; acc[m][ii][tb] = MFMA_BF(A[m][ii], B[tb], acc[m][ii][tb]); }
      };
      auto sched = [&]() {
#pragma unroll
        for (int k = 0; k < 16; ++k) { SGB(0x8, 1); SGB(0x100, 1); }
        SGB(0x8, 8);
      };
      BAR_L();
      STAMP(2);
      rd(A0, B0, SL0, 0);
      int s0 = SL0, s1 = SL1, s2 = SL2;
#pragma unroll 1
      for (int s = 0; s < 9; ++s) {
        SBAR();
        rd(A1, B1, s0, 1); mm(A0, B0); sched();
        BAR_L();
        rd(A0, B0, s1, 0); mm(A1, B1); sched();
        SBAR();
        const int t = s0; s0 = s1; s1 = s2; s2 = t;
      }
      rd(A1, B1, SL0, 1); mm(A0, B0); sched();
      SBAR();
      flag_add(fb, FL_G1, lane);
      mm(A1, B1);
      SBAR();
      STAMP(3);
      flag_wait(fb, FL_EPI, 1);
      tag = uni64(*(volatile LAS u64*)(lds + L_FLG + FL_TAG * 4));
      STAMP(4);
      {
        float nmb[4], al[4], sd[4];
#pragma unroll
        for (int tb = 0; tb < 4; ++tb) { const f32x2 st = *(LAS f32x2*)(lds + L_STAT + (tw * 64 + tb * 16 + r) * 8); nmb[tb] = -st.x; sd[tb] = st.y; al[tb] = -1.44269504f * __builtin_amdgcn_rcpf(st.y); }
        const unsigned hoff = WS_H + ((rb * 2 + tw) * 16 + cb) * 12288 + lane * 16;
        auto hcalc = [&](const f32x4& g4, const f32x4& u4, const f32x4& cg, const f32x4& cu, int tb) {
          float hv[4];
#pragma unroll
          for (int j = 0; j < 4; ++j) {
            const float g = __builtin_fmaf(nmb[tb], cg[j], g4[j]) * al[tb];
            const float u = __builtin_fmaf(nmb[tb], cu[j], u4[j]);
            const float e = __builtin_amdgcn_exp2f(g);
            hv[j] = u * __builtin_amdgcn_rcpf(__builtin_fmaf(e, sd[tb], sd[tb]));
          }
          return u32x2{pkbf(hv[0], hv[1]), pkbf(hv[2], hv[3])};
        };
        lptr pcs = lds + L_CS + (fw * 48 + 4 * q) * 4;
        const int oa = fw ? 128 : 0, oc = fw ? 0 : 128;
        {
          const f32x4 cga = *(LAS f32x4*)(pcs + oa), cua = *(LAS f32x4*)(pcs + 384 + oa), cgb = *(LAS f32x4*)(pcs + 64), cub = *(LAS f32x4*)(pcs + 384 + 64);
#pragma unroll
          for (int tb = 0; tb < 4; ++tb) {
            const u32x2 ha = hcalc(acc[0][0][tb], acc[1][0][tb], cga, cua, tb), hb = hcalc(acc[0][1][tb], acc[1][1][tb], cgb, cub, tb);
            ST_SC1((u32x4{ha[0], ha[1], hb[0], hb[1]}), hoff + fw * 4096 + tb * 1024);
            SBAR();
          }
        }
        {
          const f32x4 cg = *(LAS f32x4*)(pcs + oc), cu = *(LAS f32x4*)(pcs + 384 + oc);
#pragma unroll
          for (int t2 = 0; t2 < 2; ++t2) {
            const u32x2 ha = hcalc(acc[0][2][2 * t2], acc[1][2][2 * t2], cg, cu, 2 * t2), hb = hcalc(acc[0][2][2 * t2 + 1], acc[1][2][2 * t2 + 1], cg, cu, 2 * t2 + 1);
            ST_SC1((u32x4{ha[0], ha[1], hb[0], hb[1]}), hoff + 8192 + fw * 2048 + t2 * 1024);
            SBAR();
          }
        }
      }
      STAMP(5);
      waitv<2>();
      if (flag_add(fb, FL_H1, lane) == 3u && lane == 0) __hip_atomic_store((GAS u64*)(ws + WS_FH + (rb * 16 + cb) * 8), tag, RLX_AGENT);
      waitv<0>();
      if (flag_add(fb, FL_H2, lane) == 3u && lane == 0) __hip_atomic_store((GAS u64*)(ws + WS_FH + 2048 + (rb * 16 + cb) * 8), tag, RLX_AGENT);
      STAMP(6);
      unsigned bo[4], ao[4];
#pragma unroll
      for (int c = 0; c < 4; ++c) { const int cc = wid * 4 + ((c + tg) & 3); bo[c] = WS_WD + (nb * 48 + cc * 3) * 5120; ao[c] = WS_H + (tg * 16 + cc) * 12288; }
      const unsigned l16 = lane * 16;
#define BFRAG(k, n) __builtin_amdgcn_raw_buffer_load_b128(rs, l16, bo[(k) < 8 ? (k) >> 1 : (k) - 8] + ((k) < 8 ? ((k) & 1) * 5120 : 10240) + (n) * 1024, SC1)
#define AFRAG(k, t) __builtin_amdgcn_raw_buffer_load_b128(rs, l16, ao[(k) < 8 ? (k) >> 1 : (k) - 8] + ((k) < 8 ? ((k) & 1) * 4096 : 8192) + (t) * 1024, SC1)
      lptr park = lds + (wid < 2 ? SL0 + wid * 20480 : SL1 + (wid - 2) * 20480) + lane * 16;
      {
        u32x4 t[20];
#pragma unroll
        for (int j = 0; j < 20; ++j) t[j] = BFRAG(8 + j / 5, j % 5);
        flag_wait(fb, FL_G1, 4);
        flag_wait(fb, FL_KC, 1);
#pragma unroll
        for (int j = 0; j < 20; ++j) st16(park + j * 1024, t[j]);
      }
      u32x4 Bw[4][5];
#pragma unroll
      for (int k = 0; k < 4; ++k)
#pragma unroll
        for (int n = 0; n < 5; ++n) Bw[k][n] = BFRAG(k, n);
      STAMP(7);
      const unsigned fo = WS_FH + ((tg >> 1) * 16 + wid * 4 + (lane & 3)) * 8;
      _Pragma("nounroll") for (int sp = 0; sp < SPIN_GLB; ++sp) { if (wall(gflag_ok(ws, fo, tag))) break; __builtin_amdgcn_s_sleep(2); }
      asm volatile("" ::: "memory");
      STAMP(8);
      f32x4 a2[5][4];
#pragma unroll
      for (int n = 0; n < 5; ++n)
#pragma unroll
        for (int tb = 0; tb < 4; ++tb) a2[n][tb] = f32x4{0, 0, 0, 0};
      u32x4 Ah[4][4];
#pragma unroll
      for (int k = 0; k < 3; ++k)
#pragma unroll
        for (int tb = 0; tb < 4; ++tb) Ah[k][tb] = AFRAG(k, tb);
      u64 f2 = __hip_atomic_load((GAS u64*)(ws + fo + 2048), RLX_AGENT);
      SBAR();
#pragma unroll
      for (int k = 0; k < 12; ++k) {
        if (k == 5) {
          if (!wall(f2 == tag)) { _Pragma("nounroll") for (int sp = 0; sp < SPIN_GLB; ++sp) { if (wall(gflag_ok(ws, fo + 2048, tag))) break; __builtin_amdgcn_s_sleep(2); } }
          asm volatile("" ::: "memory");
        }
        u32x4 hf[4];
        if (k >= 8) {
          const u32x4 X0 = Ah[k & 3][0], X1 = Ah[k & 3][1], Y0 = Ah[k & 3][2], Y1 = Ah[k & 3][3];
          hf[0] = u32x4{X0[0], X0[1], Y0[0], Y0[1]}; hf[1] = u32x4{X0[2], X0[3], Y0[2], Y0[3]};
          hf[2] = u32x4{X1[0], X1[1], Y1[0], Y1[1]}; hf[3] = u32x4{X1[2], X1[3], Y1[2], Y1[3]};
        } else {
#pragma unroll
          for (int tb = 0; tb < 4; ++tb) hf[tb] = Ah[k & 3][tb];
        }
#pragma unroll
        for (int e = 0; e < 20; ++e) {
          const int n = e >> 2, tb = e & 3;
          a2[n][tb] = MFMA_BF(Bw[k & 3][n], hf[tb], a2[n][tb]);
          if (e & 1) {
            const int j = e >> 1;
            if (j < 4) { if (k + 3 < 12) Ah[(k + 3) & 3][j] = AFRAG(k + 3, j); }
            else if (j < 9 && k >= 1 && k + 3 < 12) {
              if (k + 3 < 8) Bw[(k + 3) & 3][j - 4] = BFRAG(k + 3, j - 4);
              else Bw[(k + 3) & 3][j - 4] = ld16(park + ((k - 5) * 5 + j - 4) * 1024);
            }
            SBAR();
          }
        }
#pragma unroll
        for (int n = 0; n < 5; ++n) asm volatile("" : "+v"(a2[n][0]), "+v"(a2[n][1]), "+v"(a2[n][2]), "+v"(a2[n][3]));
      }
      STAMP(9);
      {
        lptr red = lds + (wid < 2 ? SL0 + wid * 20480 : SL1 + (wid - 2) * 20480);
        const int l2 = fresh_lane(), r = l2 & 15, q = l2 >> 4;
#pragma unroll
        for (int n = 0; n < 5; ++n)
#pragma unroll
          for (int tb = 0; tb < 4; ++tb) {
            const int g = n < 4 ? q : 4, m = n < 4 ? n : q;
            *(LAS f32x4*)(red + (4 * g + m) * 1024 + ((tb * 16 + r + 3 * g) & 63) * 16) = a2[n][tb];
          }
      }
      __builtin_amdgcn_s_setprio(0);
    } else {
      waitv<0>();
      BAR_L();
      if (EXP & 16) __builtin_amdgcn_s_setprio(3);
      issue(1, SL1); issue(2, SL2);
      int s0 = SL0, s1 = SL1, s2 = SL2;
#pragma unroll 1
      for (int s = 0; s < 7; ++s) {
        waitv<20>();
        BAR();
        if (!(EXP & 8)) issue(s + 3, s0);
        const int t = s0; s0 = s1; s1 = s2; s2 = t;
      }
      load_params(pv2, chC, lw);
      waitv<25>();
      BAR();
      STAMP(2);
      tag = uni64(*(volatile LAS u64*)(lds + L_FLG + FL_TAG * 4));
      _Pragma("nounroll") for (int sp = 0; sp < SPIN_GLB; ++sp) {
        const u32x4 f0 = LD_SC1(WS_F0 + lane * 16), f1 = LD_SC1(WS_F0 + 1024 + lane * 16);
        const unsigned tl = (unsigned)tag, th = (unsigned)(tag >> 32);
        if (wall(((f0[0] ^ tl) | (f0[1] ^ th) | (f0[2] ^ tl) | (f0[3] ^ th) | (f1[0] ^ tl) | (f1[1] ^ th) | (f1[2] ^ tl) | (f1[3] ^ th)) == 0u)) break;
        __builtin_amdgcn_s_sleep(4);
      }
      asm volatile("" ::: "memory");
      BAR();
      STAMP(3);
      u32x4 uc[8]; unsigned uprev;
      const int n0 = lw * 128 + (lane >> 2) * 8;
      const unsigned uo = WS_XB + n0 * 10240 + (c0 + (lane & 3)) * 16;
      u32x4 sv, cp[10];
      if (lw == 0) {
        sv = LD_SC1(WS_ST + rb * 1024 + lane * 16);
#pragma unroll
        for (int j = 0; j < 10; ++j) cp[j] = LD_SC1(WS_CS + (cb * 10 + j) * 768 + (lane < 48 ? lane : 0) * 16);
      }
#pragma unroll
      for (int j = 0; j < 8; ++j) uc[j] = LD_SC1(uo + j * 10240);
      uprev = __builtin_amdgcn_raw_buffer_load_b32(rs, uo - 10240 + 12, 0, SC1);
      SBAR();
      if (lw == 0) {
        f32x4 cs = {0, 0, 0, 0};
#pragma unroll
        for (int j = 0; j < 10; ++j) cs += __builtin_bit_cast(f32x4, cp[j]);
        st16(lds + L_STAT + lane * 16, sv);
        if (lane < 48) *(LAS f32x4*)(lds + L_CS + lane * 16) = cs;
        flag_set(fb, FL_EPI, 1);
        if (bid == 0 && lane == 0) __hip_atomic_store((GAS u64*)(ws + WS_EPOCH), tag, RLX_AGENT);
      }
      if (n0 == 0) uprev = 0;
      if ((lane & 3) < 3) {
        lptr ud = lds + L_U + (lane & 3) * UCH + 448 + n0 * 16;
#pragma unroll
        for (int j = 0; j < 8; ++j) {
          st16(ud + j * 16, uc[j]);
          const unsigned pw_ = j ? uc[j - 1][3] : uprev;
          st16(ud + UC1 + j * 16, u32x4{__builtin_amdgcn_alignbit(uc[j][0], pw_, 16), __builtin_amdgcn_alignbit(uc[j][1], uc[j][0], 16),
                                        __builtin_amdgcn_alignbit(uc[j][2], uc[j][1], 16), __builtin_amdgcn_alignbit(uc[j][3], uc[j][2], 16)});
        }
      }
      if (lane < 28) {
#pragma unroll
        for (int c = 0; c < 3; ++c) st16(lds + L_U + c * UCH + lw * UC1 + lane * 16, u32x4{0, 0, 0, 0});
      }
      flag_add(fb, FL_U, lane);
    }
  } else {
    const int lnb = (xcd >> 1) * 64 + (xcd & 1) * 32 + bi;
    u32x4 xv[5];
#pragma unroll
    for (int j = 0; j < 5; ++j) xv[j] = *(const u32x4*)(x + (size_t)(lnb * 8 + kw * 4 + q) * DM + (r + 16 * j) * 8);
    const int un = umin(cw * 2 + kw, 383), ukb = un >> 3, unb = un & 7;
    u32x2 wa[8]; u16 wb[8];
    {
      const int c3 = (ukb * 43) >> 7, kt = ukb - 3 * c3;
      const int t0 = kt == 0 ? 0 : kt == 1 ? 80 : 32, t1 = kt == 0 ? 16 : kt == 1 ? 64 : 48;
      const u16* wp = wd + (size_t)(c3 * 96 + 4 * q) * DM + unb * 80;
#pragma unroll
      for (int j = 0; j < 8; ++j) { const u16* rp = wp + ((j < 4 ? t0 : t1) + (j & 3)) * DM; wa[j] = *(const u32x2*)(rp + 4 * r); wb[j] = rp[64 + 4 * (r & 3) + (r >> 2)]; }
    }
    const u64 ep = __hip_atomic_load((GAS u64*)(ws + WS_EPOCH), RLX_AGENT);
    const float dsk = bf1(p_dsk[chA + kw]);
    if (kw == 0 && lane < 16) *(LAS unsigned*)(lds + L_FLG + lane * 4) = 0;
    {
      float v[40]; float s0 = 0.f, s1 = 0.f;
#pragma unroll
      for (int j = 0; j < 20; ++j) { const unsigned w = xv[j >> 2][j & 3]; v[2 * j] = bflo(w); v[2 * j + 1] = bfhi(w); s0 += v[2 * j]; s1 += v[2 * j + 1]; }
      const float mean = row16_sum(s0 + s1) * (1.f / DM);
      float q0 = 0.f, q1 = 0.f;
#pragma unroll
      for (int j = 0; j < 20; ++j) { v[2 * j] -= mean; v[2 * j + 1] -= mean; q0 = __builtin_fmaf(v[2 * j], v[2 * j], q0); q1 = __builtin_fmaf(v[2 * j + 1], v[2 * j + 1], q1); }
      const float var = rbf(row16_sum(q0 + q1) * (1.f / DM));
      const float mb = rbf(mean), sd = rbf(__builtin_amdgcn_sqrtf(rbf(var + 1e-5f)));
      const float rsd = __builtin_amdgcn_rcpf(sd), off = (mean - mb) * rsd;
#pragma unroll
      for (int j = 0; j < 20; ++j) xv[j >> 2][j & 3] = pkbf(__builtin_fmaf(v[2 * j], rsd, off), __builtin_fmaf(v[2 * j + 1], rsd, off));
      if (r == 0) __builtin_amdgcn_raw_buffer_store_b64(u32x2{asu(mb), asu(sd)}, rs, WS_ST + (lnb * 8 + kw * 4 + q) * 8, 0, SC1);
    }
    lptr stg = lds + L_STG + kw * STGW;
    lptr sgw = stg + q * 576 + r * 16, sgr = stg + (r >> 2) * 576 + q * 32 + (lane & 3) * 8;
    const unsigned xo_ = WS_XB + lnb * 10240 + lane * 16 + kw * 8;
    st16(sgw, xv[0]); st16(sgw + 256, xv[1]);
    STAMP(0);
    if (EXP & 32) __builtin_amdgcn_s_setprio(3);
    BAR_L();
    if (rb < 10) {
      const int sg = (q & 1) * 32;
      lptr pa = lds + SL0 + 16384 + kw * 12288 + (8 * q + (r >> 2)) * 192 + 8 * (lane & 3);
      const u32x4 ones = {0x3f803f80u, 0x3f803f80u, 0x3f803f80u, 0x3f803f80u};
      f32x4 cs[6]; u32x4 wf[12];
#pragma unroll
      for (int t = 0; t < 12; ++t) wf[t] = tr2(pa + (t / 6) * 6144 + (t % 6) * 32 + ((t & 1) ? -sg : sg), 768);
      SBAR(); __builtin_amdgcn_s_setprio(3); SBAR();
#pragma unroll
      for (int t = 0; t < 6; ++t) cs[t] = MFMA_BF(ones, wf[t], (f32x4{0, 0, 0, 0}));
#pragma unroll
      for (int t = 0; t < 6; ++t) cs[t] = MFMA_BF(ones, wf[6 + t], cs[t]);
      SBAR(); if (!(EXP & 32)) __builtin_amdgcn_s_setprio(0); SBAR();
      const float c0_ = q == 0 ? cs[0][0] : q == 1 ? cs[1][0] : q == 2 ? cs[2][0] : cs[3][0], c1_ = q == 0 ? cs[4][0] : cs[5][0];
      const unsigned co = WS_CS + (cb * 10 + rb) * 768 + kw * 384 + lane * 4;
      __builtin_amdgcn_raw_buffer_store_b32(asu(c0_), rs, co, 0, SC1);
      if (lane < 32) __builtin_amdgcn_raw_buffer_store_b32(asu(c1_), rs, co + 256, 0, SC1);
    }
#pragma unroll
    for (int j = 0; j < 4; ++j) __builtin_amdgcn_raw_buffer_store_b64(vtr(sgr + j * 128), rs, xo_ + j * 1024, 0, SC1);
    st16(sgw, xv[2]); st16(sgw + 256, xv[3]);
    tag = uni64(ep) + 1;
    *(volatile LAS u64*)(lds + L_FLG + FL_TAG * 4) = tag;
    BAR();
#pragma unroll
    for (int j = 0; j < 4; ++j) __builtin_amdgcn_raw_buffer_store_b64(vtr(sgr + j * 128), rs, xo_ + (4 + j) * 1024, 0, SC1);
    st16(sgw, xv[4]);
    if (cw < 192) {
      const unsigned wo = WS_WD + (unb * 48 + ukb) * 5120 + lane * 16;
#pragma unroll
      for (int f = 0; f < 4; ++f) {
        u32x4 o;
#pragma unroll
        for (int e = 0; e < 4; ++e) o[e] = __builtin_amdgcn_perm(wa[2 * e + 1][f >> 1], wa[2 * e][f >> 1], (f & 1) ? 0x07060302u : 0x05040100u);
        ST_SC1(o, wo + f * 1024);
      }
      ST_SC1((u32x4{wb[0] | ((unsigned)wb[1] << 16), wb[2] | ((unsigned)wb[3] << 16), wb[4] | ((unsigned)wb[5] << 16), wb[6] | ((unsigned)wb[7] << 16)}), wo + 4096);
    }
#pragma unroll
    for (int j = 0; j < 2; ++j) __builtin_amdgcn_raw_buffer_store_b64(vtr(sgr + j * 128), rs, xo_ + (8 + j) * 1024, 0, SC1);
    STAMP(1);
    BAR();
    lptr pool = lds + SA + kw * PCH;
    f32x4 ko[3] = {{0, 0, 0, 0}, {0, 0, 0, 0}, {0, 0, 0, 0}}, ka[3] = {{0, 0, 0, 0}, {0, 0, 0, 0}, {0, 0, 0, 0}};
#pragma unroll 1
    for (int it = 0; it < 6; ++it) {
      if (it == 2 && kw == 0 && lane == 0) __hip_atomic_store((GAS u64*)(ws + WS_F0 + cw * 8), tag, RLX_AGENT);
      if (!(EXP & 1)) koper<2>(pool + it * 128, lane, ka);
      if (it & 1) {
        const float sc = *(LAS float*)(lds + L_SC + (kw * 3 + (it >> 1)) * 4);
#pragma unroll
        for (int i = 0; i < 3; ++i) {
#pragma unroll
          for (int j = 0; j < 4; ++j) ko[i][j] = __builtin_fmaf(ka[i][j], sc, ko[i][j]);
          ka[i] = f32x4{0, 0, 0, 0};
        }
      }
      if (it == 1) waitv<0>();
      BAR();
    }
    STAMP(2);
    load_params(pv2, chC, 2);
    kstore(pool + O_KL, lane, ko, dsk);
    BAR();
    kexpand(pool + O_KREV, pool + O_KL, lane);
    flag_set(fb, FL_KA + kw, 1);
    __builtin_amdgcn_s_setprio(0);
    waitv<0>();
    STAMP(3);
  }

  if (wid >= 4) {
    const int sw = wid - 4;
    const int lane = fresh_lane(), r = lane & 15, q = lane >> 4;
    PARAMS_LANDED(pv2);
    u32x4 xo[3];
#pragma unroll
    for (int j = 0; j < 3; ++j) { const int p = umin(sw + 4 * j, 9); xo[j] = *(const u32x4*)(x + (size_t)(tg * 64 + lane) * DM + nb * 80 + p * 8); }
    const float dskc = bf1(p_dsk[chC]);
    if (sw < 3) {
      const float sc = kpool(lds0 + L_PLC + sw * 256, pv2[0], pv2[1], pv2[2], pv2[3], pv2[4]);
      f32x4 ka[3] = {{0, 0, 0, 0}, {0, 0, 0, 0}, {0, 0, 0, 0}};
#pragma unroll 1
      for (int it = 0; it < 2; ++it) koper<2>(lds + L_PLC + sw * 256 + it * 128, lane, ka);
#pragma unroll
      for (int i = 0; i < 3; ++i) *(LAS f32x4*)(lds + L_KPC + sw * 3072 + i * 1024 + lane * 16) = ka[i] * sc;
      flag_add(fb, FL_KP, lane);
    }
    __builtin_amdgcn_s_setprio(0);
    STAMP(4);
    const int hm = sw >> 1;
    flag_wait(fb, FL_U, 2);
    flag_wait(fb, FL_KA + hm, 1);
    STAMP(5);
    lptr am = lds + SA + hm * PCH;
    lptr pmain = am + O_KL + lane * 16;
    f32x4 ca[8];
#pragma unroll
    for (int t = 0; t < 8; ++t) ca[t] = f32x4{0, 0, 0, 0};
    conv_units<0xff>(am + O_KREV, lds + L_U + (hm + odd) * UCH, lane, (sw & 1) ? 5 : 0, (sw & 1) ? 3 : 5, ca);
    if (sw & 1) {
#pragma unroll
      for (int t = 0; t < 8; ++t) { *(LAS f32x4*)(pmain + t * 1024) = ca[t]; ca[t] = f32x4{0, 0, 0, 0}; }
      flag_set(fb, FL_PA + hm, 1);
      if (sw == 3) {
        flag_wait(fb, FL_KP, 3);
        f32x4 ka[3];
        lptr pp = lds + L_KPC + lane * 16;
#pragma unroll
        for (int i = 0; i < 3; ++i) ka[i] = (*(LAS f32x4*)(pp + i * 1024) + *(LAS f32x4*)(pp + 3072 + i * 1024)) + *(LAS f32x4*)(pp + 6144 + i * 1024);
        kstore(lds + L_KLC, lane, ka, dskc);
        kexpand(lds + L_KC, lds + L_KLC, lane);
        flag_set(fb, FL_KC, 1);
      } else flag_wait(fb, FL_KC, 1);
      lptr uc_ = lds + L_U + (odd ? 0 : 2) * UCH;
      if (odd) conv_units<0x3a>(lds + L_KC, uc_, lane, hm * 4, 4, ca);
      else conv_units<0xc5>(lds + L_KC, uc_, lane, hm * 4, 4, ca);
      f32x4 c4[4];
      c4[0] = odd ? ca[1] : ca[0]; c4[1] = odd ? ca[3] : ca[2]; c4[2] = odd ? ca[4] : ca[6]; c4[3] = odd ? ca[5] : ca[7];
      lptr pc = lds + L_PC;
      if (sw == 3) {
#pragma unroll
        for (int t = 0; t < 4; ++t) *(LAS f32x4*)(pc + t * 1024 + lane * 16) = c4[t];
        flag_set(fb, FL_PC, 1);
      } else {
        flag_wait(fb, FL_PC, 1);
#pragma unroll
        for (int t = 0; t < 4; ++t) c4[t] += *(LAS f32x4*)(pc + t * 1024 + lane * 16);
        waitl();
#pragma unroll
        for (int t = 0; t < 4; ++t) { *(LAS unsigned*)(pc + t * 512 + q * 128 + r * 4) = pkbf(c4[t][1], c4[t][0]); *(LAS unsigned*)(pc + t * 512 + q * 128 + r * 4 + 64) = pkbf(c4[t][3], c4[t][2]); }
#pragma unroll
        for (int i = 0; i < 2; ++i) {
          const int s4 = 2 * i + (lane >> 5);
          const int T = odd ? (s4 == 0 ? 1 : s4 + 2) : (s4 < 2 ? 2 * s4 : s4 + 4);
          ST_SC1(ld16(pc + i * 1024 + lane * 16), WS_Y + ((T * 4 + ((lane >> 3) & 3)) * DM + chC) * 128 + (lane & 7) * 16);
        }
      }
    } else {
      flag_wait(fb, FL_PA + hm, 1);
#pragma unroll
      for (int t = 0; t < 8; ++t) ca[t] += *(LAS f32x4*)(pmain + t * 1024);
      waitl();
      lptr pn = pmain - lane * 16;
#pragma unroll
      for (int t = 0; t < 8; ++t) { *(LAS unsigned*)(pn + t * 512 + q * 128 + r * 4) = pkbf(ca[t][1], ca[t][0]); *(LAS unsigned*)(pn + t * 512 + q * 128 + r * 4 + 64) = pkbf(ca[t][3], ca[t][2]); }
#pragma unroll
      for (int i = 0; i < 4; ++i) ST_SC1(ld16(pmain + i * 1024), WS_Y + ((i * 8 + (lane >> 3)) * DM + chA + hm) * 128 + (lane & 7) * 16);
    }
    STAMP(6);
    waitv<0>();
    if (flag_add(fb, FL_YC, lane) == 3u) {
      if (lane == 0) __hip_atomic_store((GAS u64*)(ws + WS_FY + cw * 8), tag, RLX_AGENT);
      _Pragma("nounroll") for (int sp = 0; sp < SPIN_GLB; ++sp) { if (wall(gflag_ok(ws, WS_FY + (nb * 32 + (lane & 31)) * 8, tag))) break; __builtin_amdgcn_s_sleep(4); }
      flag_set(fb, FL_YOK, 1);
    }
    STAMP(7);
    flag_wait(fb, FL_YOK, 1);
    STAMP(8);
    u32x4 yv[3];
#pragma unroll
    for (int j = 0; j < 3; ++j) { const int p = umin(sw + 4 * j, 9); yv[j] = LD_SC1(WS_Y + (tg * DM + nb * 80 + p * 8) * 128 + lane * 16); }
    lptr yb = lds + L_YB + sw * 3072;
#pragma unroll
    for (int j = 0; j < 3; ++j) st16(yb + j * 1024 + lane * 16, yv[j]);
#pragma unroll
    for (int j = 0; j < 3; ++j) {
      const u32x4 a = tr2(yb + j * 1024 + (r >> 2) * 128 + q * 32 + (lane & 3) * 8, 512);
      u32x4 o;
#pragma unroll
      for (int e = 0; e < 4; ++e) o[e] = pkbf(bflo(xo[j][e]) + bflo(a[e]), bfhi(xo[j][e]) + bfhi(a[e]));
      if (sw + 4 * j < 10) st16(lds + L_BASE + lane * 160 + (sw + 4 * j) * 16, o);
    }
  }
  STAMP(10);
  BAR_L();
  STAMP(11);
  if (wid < 5) {
    const int tid = wid * 64 + fresh_lane();
    const int tok = (tid * 13108) >> 16, g = tid - tok * 5;
    const int o = g * 4096 + ((tok + 3 * g) & 63) * 16;
    f32x4 s[4];
#pragma unroll
    for (int m = 0; m < 4; ++m)
      s[m] = (*(LAS f32x4*)(lds + SL0 + o + m * 1024) + *(LAS f32x4*)(lds + SL0 + 20480 + o + m * 1024)) +
             (*(LAS f32x4*)(lds + SL1 + o + m * 1024) + *(LAS f32x4*)(lds + SL1 + 20480 + o + m * 1024));
    u16* op = out + (size_t)(tg * 64 + tok) * DM + nb * 80 + g * 16;
#pragma unroll
    for (int hh = 0; hh < 2; ++hh) {
      const u32x4 bs = ld16(lds + L_BASE + tid * 32 + hh * 16);
      const int j = 2 * hh;
      *(u32x4*)(op + hh * 8) = u32x4{pkbf(bflo(bs[0]) + s[0][j], bfhi(bs[0]) + s[1][j]), pkbf(bflo(bs[1]) + s[2][j], bfhi(bs[1]) + s[3][j]),
                                     pkbf(bflo(bs[2]) + s[0][j + 1], bfhi(bs[2]) + s[1][j + 1]), pkbf(bflo(bs[3]) + s[2][j + 1], bfhi(bs[3]) + s[3][j + 1])};
    }
  }
}

#if defined(__HIP_DEVICE_COMPILE__)
#pragma clang attribute pop
#endif

extern "C" void kernel_launch(void* const* d_in, const int* in_sizes, int n_in, void* d_out, int out_size, void* d_ws, size_t ws_size, hipStream_t stream) {
  (void)in_sizes; (void)n_in; (void)out_size; (void)ws_size;
  (void)hipFuncSetAttribute((const void*)fused_kernel, hipFuncAttributeMaxDynamicSharedMemorySize, LDS_BYTES);
  fused_kernel<<<256, 512, LDS_BYTES, stream>>>((const u16*)d_in[0], (const u16*)d_in[1], (const u16*)d_in[2], (const u16*)d_in[3], (const u16*)d_in[4],
                                                (const u16*)d_in[5], (const u16*)d_in[6], (const u16*)d_in[8], (const u16*)d_in[9], (const u16*)d_in[10],
                                                (u16*)d_out, (char*)d_ws);
}
```

```cpp
#include <hip/hip_runtime.h>
#include <stdint.h>

#if defined(__HIP_DEVICE_COMPILE__)
#pragma clang attribute push(__attribute__((target("no-packed-fp32-ops"))), apply_to = function)
#endif
typedef unsigned short u16;
typedef unsigned long long u64;
typedef short bf16x8 __attribute__((ext_vector_type(8)));
typedef _Float16 f16x8 __attribute__((ext_vector_type(8)));
typedef short s16x4 __attribute__((ext_vector_type(4)));
typedef float f32x2 __attribute__((ext_vector_type(2)));
typedef float f32x4 __attribute__((ext_vector_type(4)));
typedef unsigned u32x2 __attribute__((ext_vector_type(2)));
typedef unsigned u32x4 __attribute__((ext_vector_type(4)));
typedef _Float16 h16x2 __attribute__((ext_vector_type(2)));
typedef __bf16 b16x2 __attribute__((ext_vector_type(2)));
#define LAS __attribute__((address_space(3)))
#define GAS __attribute__((address_space(1)))
typedef LAS char* lptr;
#define DI __device__ __forceinline__
#define SBAR() __builtin_amdgcn_sched_barrier(0)
#define SGB(m, n) __builtin_amdgcn_sched_group_barrier(m, n, 0)
#define MFMA_BF(a, b, c) __builtin_amdgcn_mfma_f32_16x16x32_bf16(__builtin_bit_cast(bf16x8, a), __builtin_bit_cast(bf16x8, b), c, 0, 0, 0)
#define MFMA_H(a, b, c) __builtin_amdgcn_mfma_f32_16x16x32_f16(__builtin_bit_cast(f16x8, a), __builtin_bit_cast(f16x8, b), c, 0, 0, 0)
#define SC1 16
#define EXP 0

constexpr int DM = 640, NM = 192;
constexpr unsigned WS_EPOCH = 0, WS_F0 = 4096, F0REP = 4352;
constexpr unsigned WS_MB = 262144, MBS = 1280;
constexpr unsigned WS_ST = 65536, WS_CS = 98304, WS_XB = 655360, WS_WD = WS_XB + 2621440 + 4096;
constexpr unsigned WS_H = WS_WD + 1966080, WS_Y = WS_H + 6291456, WS_END = WS_Y + 2621440;
constexpr unsigned WS_STAMP = 16u << 20;
constexpr int STAGE = 40960, SL1 = 0, SA = 40960, SL2 = 81920, SL0 = 122880, POOL_BIAS = SA;
constexpr int PROW = 784, PCH = 23 * PROW;
constexpr int KC1 = 4228, O_KREV = 128, O_KL = 8528;
constexpr int L_FLG = SA + 2 * PCH, L_SC = L_FLG + 128, L_STG = L_FLG + 192, STGW = 2304;
constexpr int L_STAT = L_STG, L_CS = L_STG + 1024;
constexpr int L_RTB = SL2 + 29696;
constexpr int L_LNB = SL2 + 18432, LNR = 1344;
constexpr int UCH = 9232, UC1 = 4640;
constexpr int L_U = SL1 + 128, L_KC = L_U + 3 * UCH + 128, L_PC = L_KC + 8400;
constexpr int L_XT = SL1;
constexpr int L_BASE = SA, L_YB = SA + 10240;
constexpr int LDS_BYTES = 163840;
static_assert(O_KL + 1312 + 8192 <= PCH && L_LNB + 8 * LNR <= L_RTB && L_RTB + 10240 <= SL0 && L_PC + 4096 <= SA && L_STG + 2 * STGW <= SL0 && (L_KC & 15) == 0 && (L_PC & 15) == 0 && (L_STG & 15) == 0, "lds map");
enum { FL_F0 = 0, FL_EPI, FL_U, FL_KA, FL_KB, FL_KC, FL_KP, FL_PA, FL_PB, FL_PC, FL_YC, FL_YOK, FL_H1, FL_H2, FL_G1, FL_S2, FL_TAG = 16 };
constexpr int SPIN_LDS = 1 << 18, SPIN_GLB = 1 << 15;

DI bool wall(bool p) { return __builtin_amdgcn_ballot_w64(!p) == 0; }
DI unsigned umin(unsigned a, unsigned b) { return a < b ? a : b; }
DI unsigned umax(unsigned a, unsigned b) { return a > b ? a : b; }
DI float asf(unsigned v) { return __builtin_bit_cast(float, v); }
DI unsigned asu(float v) { return __builtin_bit_cast(unsigned, v); }
DI float bflo(unsigned v) { return asf(v << 16); }
DI float bfhi(unsigned v) { return asf(v & 0xffff0000u); }
DI float bf1(u16 v) { return asf((unsigned)v << 16); }
DI unsigned pkbf(float lo, float hi) { return __builtin_bit_cast(unsigned, __builtin_convertvector(f32x2{lo, hi}, b16x2)); }
DI unsigned pkh(f32x2 v) { return __builtin_bit_cast(unsigned, __builtin_convertvector(v, h16x2)); }
DI float rbf(float x) { return bflo(pkbf(x, x)); }
template <int CTRL> DI float dppf(float x) { return __builtin_bit_cast(float, __builtin_amdgcn_mov_dpp(__builtin_bit_cast(int, x), CTRL, 0xf, 0xf, true)); }
DI float row16_sum(float v) { v += dppf<0xB1>(v); v += dppf<0x4E>(v); v += dppf<0x124>(v); v += dppf<0x128>(v); return v; }

DI void glds4(const void* sb, unsigned v0, unsigned v1, unsigned v2, unsigned v3, unsigned lds) {
  asm volatile("s_mov_b32 m0, %5\n\ts_nop 0\n\tglobal_load_lds_dwordx4 %0, %4\n\tglobal_load_lds_dwordx4 %1, %4 offset:1024\n\t"
               "global_load_lds_dwordx4 %2, %4 offset:2048\n\tglobal_load_lds_dwordx4 %3, %4 offset:3072"
               :: "v"(v0), "v"(v1), "v"(v2), "v"(v3), "s"(sb), "s"(lds) : "memory");
}
DI void glds1(const void* sb, unsigned v0, unsigned lds) {
  asm volatile("s_mov_b32 m0, %2\n\ts_nop 0\n\tglobal_load_lds_dwordx4 %0, %1" :: "v"(v0), "s"(sb), "s"(lds) : "memory");
}
DI void glds2(const void* sb, unsigned v0, unsigned v1, unsigned lds) {
  asm volatile("s_mov_b32 m0, %3\n\ts_nop 0\n\tglobal_load_lds_dwordx4 %0, %2\n\tglobal_load_lds_dwordx4 %1, %2 offset:1024"
               :: "v"(v0), "v"(v1), "s"(sb), "s"(lds) : "memory");
}
template <int N> DI void waitv() { asm volatile("s_waitcnt vmcnt(%0)" :: "i"(N) : "memory"); }
DI void waitl() { asm volatile("s_waitcnt lgkmcnt(0)" ::: "memory"); }
DI u32x2 vtr(lptr p) { return __builtin_bit_cast(u32x2, __builtin_amdgcn_ds_read_tr16_b64_v4i16((LAS s16x4*)p)); }
DI u32x4 tr2(lptr p, int d) { u32x2 lo = vtr(p), hi = vtr(p + d); return u32x4{lo[0], lo[1], hi[0], hi[1]}; }
DI u32x4 ld16(lptr p) { return *(LAS u32x4*)p; }
DI void st16(lptr p, u32x4 v) { *(LAS u32x4*)p = v; }

DI unsigned flag_ld(unsigned addr) { unsigned r; asm volatile("ds_read_b32 %0, %1\n\ts_waitcnt lgkmcnt(0)" : "=v"(r) : "v"(addr) : "memory"); return __builtin_amdgcn_readfirstlane(r); }
DI void flag_wait(unsigned fb, int idx, unsigned need) { _Pragma("nounroll") for (int sp = 0; sp < SPIN_LDS && flag_ld(fb + idx * 4) < need; ++sp) __builtin_amdgcn_s_sleep(1); }
DI void flag_set(unsigned fb, int idx, unsigned v) {
  asm volatile("s_waitcnt lgkmcnt(0)\n\ts_mov_b64 exec, 1\n\tds_write_b32 %0, %1\n\ts_mov_b64 exec, -1" :: "v"(fb + idx * 4), "v"(v) : "memory"); }
DI unsigned flag_add(unsigned fb, int idx, int lane) {
  unsigned r; (void)lane;
  asm volatile("s_waitcnt lgkmcnt(0)\n\ts_mov_b64 exec, 1\n\tds_add_rtn_u32 %0, %1, %2\n\ts_mov_b64 exec, -1\n\ts_waitcnt lgkmcnt(0)" : "=&v"(r) : "v"(fb + idx * 4), "v"(1u) : "memory");
  return __builtin_amdgcn_readfirstlane(r);
}
#define RLX_AGENT __ATOMIC_RELAXED, __HIP_MEMORY_SCOPE_AGENT
DI bool gflag_ok(const char* ws, unsigned off, u64 tag) { return __hip_atomic_load((GAS u64*)(ws + off), RLX_AGENT) == tag; }

#define STAMP(i) do {} while (0)

DI f32x2 csq(f32x2 x) { const float t = x.x + x.x; return f32x2{__builtin_fmaf(x.x, x.x, -(x.y * x.y)), t * x.y}; }
DI h16x2 toh(f32x2 v) { return __builtin_convertvector(v, h16x2); }
DI h16x2 hmul(h16x2 x, h16x2 y) { return __builtin_elementwise_fma(h16x2{x.y, x.y}, h16x2{-y.y, y.x}, h16x2{x.x, x.x} * y); }
DI f32x2 cexp_p(float a2, float bh, float bl, float p) {
  const float e = __builtin_amdgcn_exp2f(a2 * p);
  const float ph = __builtin_fmaf(bl, p, __builtin_amdgcn_fractf(bh * p));
  return f32x2{e * __builtin_amdgcn_cosf(ph), e * __builtin_amdgcn_sinf(ph)};
}
template <int NS, int ROW0> DI void put4(const unsigned (&tb)[NS], const h16x2 (&a)[NS], const h16x2 (&b)[NS], const h16x2 (&c)[NS], const h16x2 (&d)[NS]) {
#pragma unroll
  for (int e = 0; e < NS; ++e)
    asm volatile("s_mov_b32 m0, %4\n\ts_nop 0\n\tds_write_addtid_b32 %0 offset:%5\n\tds_write_addtid_b32 %1 offset:%5+784\n\t"
                 "ds_write_addtid_b32 %2 offset:%5+1568\n\tds_write_addtid_b32 %3 offset:%5+2352"
                 :: "v"(a[e]), "v"(b[e]), "v"(c[e]), "v"(d[e]), "s"(tb[e]), "i"(ROW0 * PROW + POOL_BIAS) : "memory");
}
#define FORS _Pragma("unroll") for (int e = 0; e < NS; ++e)
template <int NS> DI void kpool(const unsigned (&tb)[NS], const float (&pv)[NS][4], const float (&ldt)[NS], bool join, float (&sc)[NS]) {
  float dt[NS];
  FORS dt[e] = rbf(__builtin_amdgcn_exp2f(ldt[e] * 1.44269504f));
  unsigned eb[NS]; float cs[NS];
  FORS {
    unsigned mi = umax(asu(pv[e][2]) & 0x7fffffffu, asu(pv[e][3]) & 0x7fffffffu);
    mi = umax(mi, asu(dppf<0xB1>(asf(mi)))); mi = umax(mi, asu(dppf<0x4E>(asf(mi)))); mi = umax(mi, asu(dppf<0x124>(asf(mi)))); mi = umax(mi, asu(dppf<0x128>(asf(mi))));
    eb[e] = umax(umax((unsigned)__builtin_amdgcn_readlane(mi, 0), (unsigned)__builtin_amdgcn_readlane(mi, 16)),
                 umax((unsigned)__builtin_amdgcn_readlane(mi, 32), (unsigned)__builtin_amdgcn_readlane(mi, 48))) >> 23;
  }
  if (join) eb[0] = eb[NS - 1] = umax(eb[0], eb[NS - 1]);
  FORS { eb[e] = umin(umax(eb[e], 2u), 252u); cs[e] = asf((254u - eb[e]) << 23); }
  float a2[NS], bh[NS], bl[NS];
  FORS {
    const float ea = rbf(__builtin_amdgcn_exp2f(pv[e][0] * 1.44269504f));
    a2[e] = -(ea * dt[e]) * 1.44269504f;
    const float b = pv[e][1] * dt[e];
    bh[e] = b * 0.15915494f;
    bl[e] = __builtin_fmaf(b, 6.4206382e-9f, __builtin_fmaf(b, 0.15915494f, -bh[e]));
  }
  h16x2 one[NS], w8[NS], w16[NS], w24[NS], w32[NS], w64[NS], w96[NS], w1[NS], w2[NS], w3[NS], w4[NS], C[NS], c4[NS];
  FORS {
    const f32x2 f1 = cexp_p(a2[e], bh[e], bl[e], 1.f), f2 = csq(f1), f4 = csq(f2);
    const f32x2 f8 = csq(f32x2{f4.x, -f4.y}), f16 = csq(f8), f32 = csq(f16), f64 = csq(f32);
    one[e] = h16x2{(_Float16)1.f, (_Float16)0.f};
    w8[e] = toh(f8); w16[e] = toh(f16); w32[e] = toh(f32); w64[e] = toh(f64); w24[e] = hmul(w8[e], w16[e]); w96[e] = hmul(w32[e], w64[e]);
    w1[e] = toh(f1); w2[e] = toh(f2); w4[e] = toh(f4); w3[e] = hmul(w1[e], w2[e]);
    C[e] = toh(f32x2{pv[e][2] * cs[e], pv[e][3] * cs[e]}); c4[e] = hmul(C[e], w4[e]);
  }
  put4<NS, 0>(tb, one, w8, w16, w24);
  put4<NS, 4>(tb, w32, w64, w96, w1);
  put4<NS, 8>(tb, w2, w3, C, c4);
  h16x2 c128[NS], c132[NS], w256[NS], w258[NS], w512[NS], w768[NS], w1024[NS], w1280[NS], w1536[NS], w1792[NS], w1796[NS];
  FORS {
    const f32x2 f128 = cexp_p(a2[e], bh[e], bl[e], 128.f), f256 = csq(f128), f512 = csq(f256);
    const h16x2 w128 = toh(f128);
    w256[e] = toh(f256); w512[e] = toh(f512); c128[e] = hmul(C[e], w128); c132[e] = hmul(c4[e], w128);
    w258[e] = hmul(w256[e], w2[e]); w768[e] = hmul(w512[e], w256[e]);
    w1024[e] = toh(csq(f512)); w1280[e] = hmul(w1024[e], w256[e]); w1536[e] = hmul(w1024[e], w512[e]);
    w1792[e] = hmul(w1024[e], w768[e]); w1796[e] = hmul(w1792[e], w4[e]);
  }
  put4<NS, 12>(tb, c128, c132, w256, w258);
  put4<NS, 16>(tb, w512, w768, w1024, w1280);
  put4<NS, 19>(tb, w1280, w1536, w1792, w1796);
  FORS sc[e] = 2.f * dt[e] * asf(eb[e] << 23);
}
struct KPtr {
  lptr pbx, pby, py, px0, px2;
  DI void init(lptr pool, int lane) {
    const int r = lane & 15, x = r & 3, y = r >> 2;
    lptr p = pool + (lane >> 4) * 16;
    pbx = p + x * PROW; pby = p + (y ? 3 + y : 0) * PROW; py = p + (10 + y) * PROW; px0 = p + (x ? 6 + x : 0) * PROW; px2 = p + (y == 3 ? 22 : 18 + x) * PROW;
  }
  DI void fetch(u32x4 (&v)[6], int o) const {
    v[0] = ld16(pbx + o); v[1] = ld16(pby + o); v[2] = ld16(py + o); v[3] = ld16(px0 + o); v[4] = ld16(pbx + 14 * PROW + o); v[5] = ld16(px2 + o);
  }
  DI void advance() { pbx += 128; pby += 128; py += 128; px0 += 128; px2 += 128; }
};
DI void kprod(const u32x4 (&v)[6], u32x4 (&o)[4]) {
  unsigned t[16];
  if (EXP & 2048) { SBAR(); __builtin_amdgcn_s_setprio(3); SBAR(); }
#pragma unroll
  for (int i = 0; i < 16; ++i) { const unsigned xe = v[(i >> 2) ? 2 + (i >> 2) : 0][i & 3], ye = v[(i >> 2) ? 2 : 1][i & 3];
    asm("v_pk_mul_f16 %0, %1, %2 op_sel_hi:[0,1]" : "=v"(t[i]) : "v"(xe), "v"(ye)); }
  SBAR();
#pragma unroll
  for (int i = 0; i < 16; ++i) { const unsigned xe = v[(i >> 2) ? 2 + (i >> 2) : 0][i & 3], ye = v[(i >> 2) ? 2 : 1][i & 3]; unsigned z;
    asm("v_pk_fma_f16 %0, %1, %2, %3 op_sel:[1,1,0] op_sel_hi:[1,0,1] neg_lo:[0,1,0]" : "=v"(z) : "v"(xe), "v"(ye), "v"(t[i])); o[i >> 2][i & 3] = z; }
  SBAR();
}
template <int H> DI void kprodh(const u32x4 (&v)[6], u32x4 (&o)[4]) {
  unsigned t[8];
#pragma unroll
  for (int i = 8 * H; i < 8 * H + 8; ++i) { const unsigned xe = v[(i >> 2) ? 2 + (i >> 2) : 0][i & 3], ye = v[(i >> 2) ? 2 : 1][i & 3];
    asm("v_pk_mul_f16 %0, %1, %2 op_sel_hi:[0,1]" : "=v"(t[i & 7]) : "v"(xe), "v"(ye)); }
  SBAR();
#pragma unroll
  for (int i = 8 * H; i < 8 * H + 8; ++i) { const unsigned xe = v[(i >> 2) ? 2 + (i >> 2) : 0][i & 3], ye = v[(i >> 2) ? 2 : 1][i & 3]; unsigned z;
    asm("v_pk_fma_f16 %0, %1, %2, %3 op_sel:[1,1,0] op_sel_hi:[1,0,1] neg_lo:[0,1,0]" : "=v"(z) : "v"(xe), "v"(ye), "v"(t[i & 7])); o[i >> 2][i & 3] = z; }
  asm volatile("" : "+v"(o[2 * H]), "+v"(o[2 * H + 1]));
  SBAR();
}
DI void kmma1(const u32x4 (&o0)[4], f32x4 (&acc)[3]) {
  SBAR(); asm volatile("s_setprio 3\n\ts_nop 1"); SBAR();
#pragma unroll
  for (int i = 0; i < 3; ++i) acc[i] = MFMA_H(o0[1 + i], o0[0], acc[i]);
  SBAR(); __builtin_amdgcn_s_setprio(0); SBAR();
}
DI void kscale(f32x4 (&ko)[3], f32x4 (&acc)[3], float sc) {
#pragma unroll
  for (int i = 0; i < 3; ++i) {
#pragma unroll
    for (int j = 0; j < 4; ++j) ko[i][j] = __builtin_fmaf(acc[i][j], sc, ko[i][j]);
    acc[i] = f32x4{0, 0, 0, 0};
  }
}
DI float ubf(const u16* p, int i) { const unsigned w = ((const __attribute__((address_space(4))) unsigned*)(uintptr_t)p)[i >> 1]; return (i & 1) ? bfhi(w) : bflo(w); }
DI void kmma2(const u32x4 (&o0)[4], const u32x4 (&o1)[4], f32x4 (&acc)[3]) {
  SBAR(); asm volatile("s_setprio 3\n\ts_nop 1"); SBAR();
#pragma unroll
  for (int i = 0; i < 3; ++i) acc[i] = MFMA_H(o0[1 + i], o0[0], acc[i]);
#pragma unroll
  for (int i = 0; i < 3; ++i) acc[i] = MFMA_H(o1[1 + i], o1[0], acc[i]);
  SBAR(); __builtin_amdgcn_s_setprio(0); SBAR();
}
DI void kstore(lptr kl, int lane, const f32x4 (&acc)[3], float dsk) {
  const int c = lane & 15, q = lane >> 4;
  LAS u16* t = (LAS u16*)kl;
  const float k0 = acc[0][0] + (lane == 0 ? dsk : 0.f);
  *(LAS u32x2*)(t + 128 * (q >> 1) + 8 * c + 4 * (q & 1)) = u32x2{pkbf(k0, acc[0][1]), pkbf(acc[0][2], acc[0][3])};
  *(LAS unsigned*)(t + 256 + 64 * (q >> 1) + 2 * (q & 1) + 4 * c) = pkbf(acc[1][0], acc[1][1]);
  const unsigned p1 = pkbf(acc[1][2], acc[1][3]);
  const int i1 = 384 + 32 * (q >> 1) + (q & 1) + 2 * c;
  t[i1] = (u16)p1; t[i1 + 64] = (u16)(p1 >> 16);
  const unsigned r0 = pkbf(acc[2][0], acc[2][1]), r1 = pkbf(acc[2][2], acc[2][3]);
  if (!(q & 1)) { const int i2 = 512 + 8 * q + c; t[i2] = (u16)r0; t[i2 + 32] = (u16)(r0 >> 16); t[i2 + 64] = (u16)r1; t[i2 + 96] = (u16)(r1 >> 16); }
  else if (lane == 63) t[640] = (u16)r0;
}
DI unsigned dup16(unsigned a) { return a | (a << 16); }
DI unsigned bsel(unsigned m, unsigned a, unsigned b) { asm("" : "+v"(m)); return (a & m) | (b & ~m); }
DI unsigned rot16(unsigned a) { return __builtin_amdgcn_alignbit(a, a, 16); }
DI void kexpand(lptr kr, lptr kl, int lane) {
  LAS u16* t = (LAS u16*)kl;
  auto put = [&](int ch, unsigned dp, unsigned d0, unsigned d1, unsigned d2, unsigned d3) {
    st16(kr + 16 * ch, u32x4{d0, d1, d2, d3}); st16(kr + KC1 - 4 + 16 * ch, u32x4{dp, d0, d1, d2}); };
  const int n = 63 - lane, n0 = n & 31;
  const unsigned a0 = t[576 + n], b0 = t[577 + n], a1 = t[512 + n], b1 = t[513 + n];
  const unsigned ab = *(LAS unsigned*)(t + 384 + 2 * n), c2 = t[386 + 2 * n];
  const u32x2 v1 = *(LAS u32x2*)(t + 256 + 4 * n0); const unsigned q4 = t[260 + 4 * n0];
  const u32x4 v0 = ld16((lptr)(t + 8 * n0)); const unsigned w = *(LAS unsigned*)(t + 8 * n0 + 8);
  const unsigned mo = (n & 1) ? ~0u : 0u, lo = lane >= 32 ? ~0u : 0u, m31 = n == 31 ? ~0u : 0u;
  put(lane, dup16(b0), dup16(b0), bsel(mo, dup16(b0), b0 | (a0 << 16)), dup16(a0), dup16(a0));
  put(64 + lane, dup16(b1), dup16(b1), bsel(mo, dup16(b1), b1 | (a1 << 16)), dup16(a1), dup16(a1));
  put(128 + lane, dup16(c2), dup16(c2), dup16(ab >> 16), rot16(ab), dup16(ab & 0xffffu));
  put(192 + lane, bsel(lo, bsel(m31, dup16(w & 0xffffu), rot16(w)), dup16(q4)), bsel(lo, rot16(v0[3]), q4 | (v1[1] & 0xffff0000u)),
      bsel(lo, rot16(v0[2]), dup16(v1[1] & 0xffffu)), bsel(lo, rot16(v0[1]), (v1[1] & 0xffffu) | (v1[0] & 0xffff0000u)), bsel(lo, rot16(v0[0]), dup16(v1[0] & 0xffffu)));
  if (lane >= 60) put(319 - lane, lane == 63 ? rot16(v0[0]) : 0u, 0u, 0u, 0u, 0u);
}

struct ConvPlan { int n; int item[36][3]; };
constexpr ConvPlan conv_plan(int tmask, int maxt) {
  ConvPlan p{};
  int fifo[64] = {}, head = 0, tail = 0, carry[16] = {}, nc = 0, per = (tmask == 0xff) ? 1 : 2;
  for (int pass = 0; pass < 2; ++pass) {
    head = tail = 0;
    for (int i = 0; i < nc; ++i) fifo[tail++] = carry[i] + 32;
    int e = 0;
    for (int m = maxt; m >= 0; --m) {
      for (int j = 0; j + m <= maxt; ++j) {
        const bool on = (tmask >> (m + j)) & 1;
        int k = 0;
        if (m == 0) { if (on && j < maxt - 1) p.item[e][k++] = j; else fifo[tail++] = j; }
        if (j + m == maxt) { fifo[tail++] = 8 + 2 * m; fifo[tail++] = 9 + 2 * m; }
        if (on) { while (k < per && head < tail) p.item[e][k++] = fifo[head++]; while (k < 3) p.item[e][k++] = -1; ++e; }
      }
    }
    p.n = e; nc = 0;
    while (head < tail) carry[nc++] = fifo[head++];
  }
  return p;
}
template <int TMASK>
DI void conv_units(lptr kr, lptr ub, int lane, int rho0, int nrho, f32x4 (&acc)[8]) {
  constexpr int MAXT = (TMASK & 0x80) ? 7 : 5;
  constexpr ConvPlan plan = conv_plan(TMASK, MAXT);
  const int r = lane & 15, q = lane >> 4;
  lptr pk = kr + ((r & 1) ? 0 : KC1) + 4092 - 4 * r + 16 * q - 3584 - 64 * rho0 - 64;
  lptr pu = ub + ((r & 1) ? 0 : UC1) + 64 * (r >> 1) + 16 * q + 64 * (7 - rho0) - 64;
  lptr pk2 = pk + 8;
  u32x2 Kl[8], Kh[8]; u32x4 Uf[8];
  asm volatile("" : "+v"(pk), "+v"(pk2), "+v"(pu));
#pragma unroll
  for (int j = 0; j <= MAXT; ++j) { const int m = MAXT - j; Uf[j] = ld16(pu + 64 + 512 * j); Kl[m] = *(LAS u32x2*)(pk + 64 + 512 * (7 - m)); Kh[m] = *(LAS u32x2*)(pk2 + 64 + 512 * (7 - m)); SBAR(); }
#pragma unroll 1
  for (int it = 0; it < nrho; ++it) {
    int e = 0;
#pragma unroll
    for (int m = MAXT; m >= 0; --m) {
#pragma unroll
      for (int j = 0; j + m <= MAXT; ++j) {
        if ((TMASK >> (m + j)) & 1) {
          acc[m + j] = MFMA_BF(Uf[j], (u32x4{Kl[m][0], Kl[m][1], Kh[m][0], Kh[m][1]}), acc[m + j]);
#pragma unroll
          for (int k = 0; k < 3; ++k) {
            const int c = plan.item[e][k];
            if (c >= 0) {
              const int o = (c & 32) ? 64 : 0, cc = c & 31, mm = (cc - 8) >> 1;
              if (cc < 8) Uf[cc] = ld16(pu + o + 512 * cc);
              else if (cc & 1) Kh[mm] = *(LAS u32x2*)(pk2 + o + 512 * (7 - mm));
              else Kl[mm] = *(LAS u32x2*)(pk + o + 512 * (7 - mm));
            }
          }
          SBAR();
          ++e;
        }
      }
    }
    pk -= 64; pk2 -= 64; pu -= 64;
    asm volatile("" : "+v"(pk), "+v"(pk2), "+v"(pu));
  }
}

template <int N> struct IC { static constexpr int value = N; };
DI u64 uni64(u64 v) { return (u64)(unsigned)__builtin_amdgcn_readfirstlane((unsigned)v) | ((u64)(unsigned)__builtin_amdgcn_readfirstlane((unsigned)(v >> 32)) << 32); }
DI int fresh_lane() { int l; asm volatile("v_mbcnt_lo_u32_b32 %0, -1, 0\n\tv_mbcnt_hi_u32_b32 %0, -1, %0" : "=v"(l)); return l; }
#define ARR() do {} while (0)
#define BAR() do { SBAR(); ARR(); asm volatile("s_barrier" ::: "memory"); SBAR(); } while (0)
#define BAR_L() do { SBAR(); asm volatile("s_waitcnt lgkmcnt(0)" ::: "memory"); SBAR(); ARR(); asm volatile("s_barrier" ::: "memory"); SBAR(); } while (0)
#define LD_SC1(off) __builtin_amdgcn_raw_buffer_load_b128(rs, (off), 0, SC1)
#define ST_SC1(v, off) __builtin_amdgcn_raw_buffer_store_b128((v), rs, (off), 0, SC1)

__global__ __launch_bounds__(512, 2) void fused_kernel(const u16* __restrict__ x, const u16* __restrict__ p_lar, const u16* __restrict__ p_aim,
                                                       const u16* __restrict__ p_cr, const u16* __restrict__ p_ci, const u16* __restrict__ p_ldt,
                                                       const u16* __restrict__ p_dsk, const u16* __restrict__ wg, const u16* __restrict__ wu,
                                                       const u16* __restrict__ wd, u16* __restrict__ out, char* ws) {
  extern __shared__ __attribute__((aligned(16))) char smem[];
  const int tid = __builtin_amdgcn_workitem_id_x(), bid = __builtin_amdgcn_workgroup_id_x();
  asm volatile("" :: "s"(x), "s"(p_lar), "s"(p_aim), "s"(p_cr), "s"(p_ci), "s"(p_ldt), "s"(p_dsk), "s"(wg), "s"(wu), "s"(wd), "s"(out), "s"(ws));
  lptr lds = (lptr)smem;
  const unsigned lds0 = (unsigned)(uintptr_t)smem;
  const unsigned fb = lds0 + L_FLG;
  const int wid = __builtin_amdgcn_readfirstlane(tid >> 6), lane = tid & 63;
  const int xcd = bid & 7, bi = bid >> 3;
  const int rb = (xcd >> 1) * 4 + (bi >> 3), cb = (xcd & 1) * 8 + (bi & 7);
  const int tg = (xcd >> 1) * 8 + (bi >> 2), nb = (xcd & 1) * 4 + (bi & 3);
  const int cw = xcd * 32 + bi, odd = cw & 1, c0 = 5 * (cw >> 1) + 2 * odd;
  const int chA = c0 + odd, chC = odd ? c0 : c0 + 2;
  const int rot = (EXP & 2) ? 0 : rb >= 10 ? rb - 10 : rb;
  const __amdgpu_buffer_rsrc_t rs = __builtin_amdgcn_make_buffer_rsrc(ws, 0, (int)WS_END, 0x00020000);
  const int r = lane & 15, q = lane >> 4;

  u64 tag = 0;
  const int lw = wid - 4, kw = wid - 6;
  unsigned vx[4], vw[12];
  auto issue = [&](int s, int sbase, int h, int P0 = 0, int P1 = 5) {
    int ks = s + rot; if (ks >= 10) ks -= 10;
    const unsigned base = lds0 + sbase;
    const char* sx = (const char*)x + (size_t)(rb * 128 + h * 64) * 1280 - 4096 + ks * 128;
    const char* sw = (const char*)(h ? wu : wg) + cb * 192 - 4096 + (size_t)ks * 64 * 3072;
    if (P0 <= 0 && 0 < P1) glds4(sx, vx[0], vx[1], vx[2], vx[3], base + h * 8192);
    if (P0 <= 1 && 1 < P1) glds4(sw, vw[0], vw[1], vw[2], vw[3], base + 16384 + h * 12288);
    if (P0 <= 2 && 2 < P1) glds4(sx + 32 * 1280, vx[0], vx[1], vx[2], vx[3], base + h * 8192 + 4096);
    if (P0 <= 3 && 3 < P1) glds4(sw, vw[4], vw[5], vw[6], vw[7], base + 16384 + h * 12288 + 4096);
    if (P0 <= 4 && 4 < P1) glds4(sw, vw[8], vw[9], vw[10], vw[11], base + 16384 + h * 12288 + 8192);
  };
  auto load_params = [&](unsigned (&pr)[4], int ch, bool tail) {
    const unsigned o = (ch * NM + (tail ? 128 + (lane & ~1) : 2 * lane)) * 2;
    asm volatile("global_load_dword %0, %4, %5\n\tglobal_load_dword %1, %4, %6\n\tglobal_load_dword %2, %4, %7\n\tglobal_load_dword %3, %4, %8"
                 : "=&v"(pr[0]), "=&v"(pr[1]), "=&v"(pr[2]), "=&v"(pr[3]) : "v"(o), "s"(p_lar), "s"(p_aim), "s"(p_cr), "s"(p_ci) : "memory");
  };
  auto load_ldt = [&](int ca, int cb_) { unsigned v; asm volatile("global_load_ushort %0, %1, %2" : "=&v"(v) : "v"((lane < 32 ? ca : cb_) * 2), "s"(p_ldt) : "memory"); return v; };
#define PR_WAIT(p) asm volatile("s_waitcnt vmcnt(0)" : "+v"(p[0]), "+v"(p[1]), "+v"(p[2]), "+v"(p[3]) :: "memory")

  if (wid >= 6) {
    const int lnb = (xcd >> 1) * 64 + (xcd & 1) * 32 + bi;
    u32x4 xv[5];
#pragma unroll
    for (int j = 0; j < 5; ++j) xv[j] = *(const u32x4*)(x + (size_t)(lnb * 8 + kw * 4 + q) * DM + (r + 16 * j) * 8);
    const u64 ep = __hip_atomic_load((GAS u64*)(ws + WS_EPOCH), RLX_AGENT);
    const int un = umin(cw * 2 + kw, 383), ukb = un >> 3, unb = un & 7;
    unsigned rv[5];
    {
      const int c3 = (ukb * 43) >> 7, kt = ukb - 3 * c3;
      const int t0 = kt == 0 ? 0 : kt == 1 ? 80 : 32, t1 = kt == 0 ? 0 : kt == 1 ? 48 : 32;
#pragma unroll
      for (int j = 0; j < 5; ++j) { const int c = j * 64 + lane, row = (c * 6554) >> 16, pos = c - row * 10; rv[j] = (c3 * 96 + row + (row < 16 ? t0 : t1)) * 1280 + unb * 160 + pos * 16; }
    }
    const float dsk = ubf(p_dsk, chA + kw);
    if (kw == 0 && lane < 16) *(LAS unsigned*)(lds + L_FLG + lane * 4) = 0;
    u32x2 stat;
    {
      float v[40]; float s0 = 0.f, s1 = 0.f;
#pragma unroll
      for (int j = 0; j < 20; ++j) { const unsigned w = xv[j >> 2][j & 3]; v[2 * j] = bflo(w); v[2 * j + 1] = bfhi(w); s0 += v[2 * j]; s1 += v[2 * j + 1]; }
      const float mean = row16_sum(s0 + s1) * (1.f / DM);
      tag = uni64(ep) + 1;
      SBAR();
#pragma unroll
      for (int j = 0; j < 5; ++j) glds1(wd, rv[j], lds0 + L_RTB + kw * 5120 + j * 1024);
      SBAR();
      float q0 = 0.f, q1 = 0.f;
#pragma unroll
      for (int j = 0; j < 20; ++j) { v[2 * j] -= mean; v[2 * j + 1] -= mean; q0 = __builtin_fmaf(v[2 * j], v[2 * j], q0); q1 = __builtin_fmaf(v[2 * j + 1], v[2 * j + 1], q1); }
      const float var = rbf(row16_sum(q0 + q1) * (1.f / DM));
      const float mb = rbf(mean), sd = rbf(__builtin_amdgcn_sqrtf(rbf(var + 1e-5f)));
      const float rsd = __builtin_amdgcn_rcpf(sd), off = (mean - mb) * rsd;
#pragma unroll
      for (int j = 0; j < 20; ++j) xv[j >> 2][j & 3] = pkbf(__builtin_fmaf(v[2 * j], rsd, off), __builtin_fmaf(v[2 * j + 1], rsd, off));
      stat = u32x2{asu(mb), asu(sd)};
    }
#pragma unroll
    for (int j = 0; j < 5; ++j) st16(lds + L_LNB + (kw * 4 + q) * LNR + (r + 16 * j) * 16, xv[j]);
    *(volatile LAS u64*)(lds + L_FLG + FL_TAG * 4) = tag;
    waitv<0>();
    STAMP(0);
    BAR_L();
    u32x4 wt[5], wf[12];
#pragma unroll
    for (int j = 0; j < 5; ++j) xv[j] = tr2(lds + L_LNB + (r >> 2) * LNR + (kw * 5 + j) * 128 + q * 32 + (lane & 3) * 8, 4 * LNR);
#pragma unroll
    for (int j = 0; j < 5; ++j) wt[j] = tr2(lds + L_RTB + kw * 5120 + (4 * q + (r >> 2)) * 160 + j * 32 + (lane & 3) * 8, 2560);
    flag_add(fb, FL_S2, lane);
    {
      const int sg = (q & 1) * 32;
      lptr pa = lds + SL0 + 16384 + kw * 12288 + (8 * q + (r >> 2)) * 192 + 8 * (lane & 3);
#pragma unroll
      for (int t = 0; t < 12; ++t) wf[t] = tr2(pa + (t / 6) * 6144 + (t % 6) * 32 + ((t & 1) ? -sg : sg), 768);
    }
    lptr pool = lds + SA + kw * PCH;
    KPtr kp; kp.init(pool, lane);
    u32x4 kv[2][6], kq[4];
    kp.fetch(kv[0], 0); kp.fetch(kv[1], 64);
    BAR_L();
    unsigned pf_sink = 0;
    f32x4 ko[3] = {{0, 0, 0, 0}, {0, 0, 0, 0}, {0, 0, 0, 0}}, ka[3] = {{0, 0, 0, 0}, {0, 0, 0, 0}, {0, 0, 0, 0}};
    u32x4 f0 = {0, 0, 0, 0}, f1 = {0, 0, 0, 0};
#pragma unroll
    for (int sl = 0; sl < 8; ++sl) {
      if (sl == 3 && kw == 0 && lane < 8) __hip_atomic_store((GAS u64*)(ws + WS_F0 + lane * F0REP + cw * 8), tag, RLX_AGENT);
#pragma unroll
      for (int hs = 3 * sl; hs < 3 * sl + 3; ++hs) {
        const int k = hs >> 1;
        if (hs == 16) kscale(ko, ka, *(LAS float*)(lds + L_SC + kw * 4));
        if (!(hs & 1)) kprodh<0>(kv[k & 1], kq);
        else { kprodh<1>(kv[k & 1], kq); if (k + 2 < 12) kp.fetch(kv[k & 1], 64 * (k + 2)); kmma1(kq, ka); }
      }
      if (sl == 0) {
#pragma unroll
        for (int j = 0; j < 5; ++j) ST_SC1(xv[j], WS_XB + lnb * 10240 + (kw * 5 + j) * 1024 + lane * 16);
        if (r == 0) __builtin_amdgcn_raw_buffer_store_b64(stat, rs, WS_ST + (lnb * 8 + kw * 4 + q) * 8, 0, SC1);
      }
      if (sl == 1) {
        if (rb < 10) {
          const u32x4 ones = {0x3f803f80u, 0x3f803f80u, 0x3f803f80u, 0x3f803f80u};
          f32x4 cs[6];
          SBAR(); __builtin_amdgcn_s_setprio(3); SBAR();
#pragma unroll
          for (int t = 0; t < 6; ++t) cs[t] = MFMA_BF(ones, wf[t], (f32x4{0, 0, 0, 0}));
#pragma unroll
          for (int t = 0; t < 6; ++t) cs[t] = MFMA_BF(ones, wf[6 + t], cs[t]);
          SBAR(); __builtin_amdgcn_s_setprio(0); SBAR();
          const float cs0 = q == 0 ? cs[0][0] : q == 1 ? cs[1][0] : q == 2 ? cs[2][0] : cs[3][0], cs1 = q == 0 ? cs[4][0] : cs[5][0];
          const unsigned co = WS_CS + (cb * 10 + rb) * 768 + kw * 384 + lane * 4;
          __builtin_amdgcn_raw_buffer_store_b32(asu(cs0), rs, co, 0, SC1);
          if (lane < 32) __builtin_amdgcn_raw_buffer_store_b32(asu(cs1), rs, co + 256, 0, SC1);
        }
        if (cw < 192) {
#pragma unroll
          for (int f = 0; f < 5; ++f) ST_SC1(wt[f], WS_WD + (unb * 48 + ukb) * 5120 + f * 1024 + lane * 16);
        }
      }
      if (sl == 2) waitv<0>();
      if (sl == 5 && kw == 0) { f0 = LD_SC1(WS_F0 + xcd * F0REP + lane * 16); f1 = LD_SC1(WS_F0 + xcd * F0REP + 1024 + lane * 16); }
      if (sl == 6 && kw == 0) {
        const unsigned tl = (unsigned)tag, th = (unsigned)(tag >> 32);
        _Pragma("nounroll") for (int sp = 0; sp < SPIN_GLB; ++sp) {
          if (wall(((f0[0] ^ tl) | (f0[1] ^ th) | (f0[2] ^ tl) | (f0[3] ^ th) | (f1[0] ^ tl) | (f1[1] ^ th) | (f1[2] ^ tl) | (f1[3] ^ th)) == 0u)) break;
          f0 = LD_SC1(WS_F0 + xcd * F0REP + lane * 16); f1 = LD_SC1(WS_F0 + xcd * F0REP + 1024 + lane * 16);
          __builtin_amdgcn_s_sleep(2);
        }
      }
      if (sl >= 6) {
        const unsigned po = WS_WD + nb * 245760 + (bi >> 2) * 30720 + umin(((sl - 6) * 2 + kw) * 64 + lane, 239u) * 128;
        asm volatile("global_load_dword %0, %1, %2 sc1" : "+v"(pf_sink) : "v"(po), "s"(ws) : "memory");
      }
      BAR();
    }
    STAMP(2);
    kscale(ko, ka, *(LAS float*)(lds + L_SC + 12 + kw * 4));
    kstore(pool + O_KL, lane, ko, dsk);
    kexpand(pool + O_KREV, pool + O_KL, lane);
    flag_set(fb, FL_KA + kw, 1);
    asm volatile("s_waitcnt vmcnt(0)" : "+v"(pf_sink) :: "memory");
    STAMP(3);
  } else {
    if (wid >= 4) {
      {
        if (lw) {
          unsigned pr[4];
          load_params(pr, chC, true);
          unsigned lv = load_ldt(chC, chC);
          PR_WAIT(pr); asm volatile("" : "+v"(lv));
          const unsigned sh = (lane & 1) ? 0u : 16u, tb[1] = {lds0 + SL2 - SA + 512};
          const float pv[1][4] = {{bfhi(pr[0] << sh), bfhi(pr[1] << sh), bfhi(pr[2] << sh), bfhi(pr[3] << sh)}}, ldt[1] = {asf(lv << 16)};
          float sc[1];
          kpool<1>(tb, pv, ldt, false, sc);
          if (lane == 0) *(LAS float*)(lds + L_SC + 20) = sc[0];
        }
#pragma unroll
        for (int j = 0; j < 4; ++j) { const int row = 8 * j + (lane >> 3); vx[j] = row * 1280 + (((lane & 7) ^ ((row >> 1) & 7)) << 4) + 4096 - 1024 * j; }
#pragma unroll
        for (int j = 0; j < 3; ++j) { const int c = j * 64 + lane, row = (c * 43691) >> 19, pos = c - row * 12;
          const unsigned v = row * 3072 + ((pos ^ (((row >> 3) & 1) << 1)) << 4) + 4096;
#pragma unroll
          for (int g = 0; g < 4; ++g) vw[3 * g + j] = v + g * 16 * 3072 - 1024 * ((3 * g + j) & 3); }
        if (!lw) { issue(0, SL0, 0); issue(0, SL0, 1); }
        STAMP(0);
        issue(1, SL1, lw);
        STAMP(1);
      }
      waitv<20>();
      BAR_L();
      u32x4 cv[6][6];
      {
        KPtr kp; kp.init(lds + SL2 + lw * 384, lane);
#pragma unroll
        for (int i = 0; i < 6; ++i) kp.fetch(cv[i], 64 * i);
      }
      const float sca = *(LAS float*)(lds + L_SC + 8), scb = *(LAS float*)(lds + L_SC + (lw ? 20 : 8));
      flag_add(fb, FL_S2, lane);
      flag_wait(fb, FL_S2, 4);
      issue(2, SL2, lw, 0, 2);
      f32x4 ka[3] = {{0, 0, 0, 0}, {0, 0, 0, 0}, {0, 0, 0, 0}};
      f32x4 kc[3] = {{0, 0, 0, 0}, {0, 0, 0, 0}, {0, 0, 0, 0}};
#pragma unroll
      for (int s = 0; s < 7; ++s) {
        if (s == 0) waitv<8>(); else waitv<20>();
        BAR();
        if (s == 0) issue(2, SL2, lw, 2, 5);
        issue(s + 3, s % 3 == 0 ? SL0 : s % 3 == 1 ? SL1 : SL2, lw);
        if (s >= 1 && !(EXP & 4096)) { u32x4 o[4]; kprod(cv[s - 1], o); kmma1(o, ka); }
        if (s == 2) kscale(kc, ka, sca);
      }
      kscale(kc, ka, scb);
      if (lw == 0) {
#pragma unroll
        for (int i = 0; i < 3; ++i) *(LAS f32x4*)(lds + L_STG + i * 1024 + lane * 16) = kc[i];
      }
      waitv<20>();
      BAR_L();
      STAMP(2);
      tag = uni64(*(volatile LAS u64*)(lds + L_FLG + FL_TAG * 4));
      u32x4 uc[8]; unsigned uprev;
      const int n0 = lw * 128 + (lane >> 2) * 8;
      const unsigned uo = WS_XB + n0 * 10240 + (c0 + (lane & 3)) * 16;
      u32x4 sv, cp[10];
      if (lw == 0) {
        sv = LD_SC1(WS_ST + rb * 1024 + lane * 16);
#pragma unroll
        for (int j = 0; j < 10; ++j) cp[j] = LD_SC1(WS_CS + (cb * 10 + j) * 768 + (lane < 48 ? lane : 0) * 16);
      }
#pragma unroll
      for (int j = 0; j < 8; ++j) uc[j] = LD_SC1(uo + j * 10240);
      uprev = __builtin_amdgcn_raw_buffer_load_b32(rs, uo - 10240 + 12, 0, SC1);
      SBAR();
      if (lw) {
#pragma unroll
        for (int i = 0; i < 3; ++i) kc[i] += *(LAS f32x4*)(lds + L_STG + i * 1024 + lane * 16);
        kstore(lds + L_PC, lane, kc, ubf(p_dsk, chC));
        waitv<9>();
      } else waitv<20>();
      BAR();
      STAMP(3);
      if (lw == 0) {
        f32x4 cs = {0, 0, 0, 0};
#pragma unroll
        for (int j = 0; j < 10; ++j) cs += __builtin_bit_cast(f32x4, cp[j]);
        st16(lds + L_STAT + lane * 16, sv);
        if (lane < 48) *(LAS f32x4*)(lds + L_CS + lane * 16) = cs;
        flag_set(fb, FL_EPI, 1);
        if (bid == 0 && lane == 0) __hip_atomic_store((GAS u64*)(ws + WS_EPOCH), tag, RLX_AGENT);
      }
      if (n0 == 0) uprev = 0;
      if ((lane & 3) < 3) {
        lptr ud = lds + L_U + (lane & 3) * UCH + 448 + n0 * 16;
#pragma unroll
        for (int j = 0; j < 8; ++j) {
          st16(ud + j * 16, uc[j]);
          const unsigned pw_ = j ? uc[j - 1][3] : uprev;
          st16(ud + UC1 + j * 16, u32x4{__builtin_amdgcn_alignbit(uc[j][0], pw_, 16), __builtin_amdgcn_alignbit(uc[j][1], uc[j][0], 16),
                                        __builtin_amdgcn_alignbit(uc[j][2], uc[j][1], 16), __builtin_amdgcn_alignbit(uc[j][3], uc[j][2], 16)});
        }
      }
      if (lane < 28) {
#pragma unroll
        for (int c = 0; c < 3; ++c) st16(lds + L_U + c * UCH + lw * UC1 + lane * 16, u32x4{0, 0, 0, 0});
      }
      flag_add(fb, FL_U, lane);
      if (lw) { kexpand(lds + L_KC, lds + L_PC, lane); flag_set(fb, FL_KC, 1); }
    } else {
      unsigned pa[4], pb[4];
      const int ch0 = wid == 2 ? chC : wid == 1 ? chA + 1 : chA, ch1 = wid == 3 ? chA + 1 : ch0;
      load_params(pa, ch0, wid == 3); load_params(pb, ch1, wid == 3);
      unsigned lv = load_ldt(ch0, ch1);
      f32x4 acc[2][3][4];
#pragma unroll
      for (int m = 0; m < 2; ++m)
#pragma unroll
        for (int ii = 0; ii < 3; ++ii)
#pragma unroll
          for (int tb = 0; tb < 4; ++tb) { acc[m][ii][tb] = f32x4{0, 0, 0, 0}; asm volatile("" : "+v"(acc[m][ii][tb])); }
      PR_WAIT(pa); PR_WAIT(pb); asm volatile("" : "+v"(lv));
      STAMP(0);
      {
        const float ldt[2] = {asf((unsigned)__builtin_amdgcn_readlane(lv, 0) << 16), asf((unsigned)__builtin_amdgcn_readlane(lv, 32) << 16)};
        const unsigned shp = (lane & 1) ? 0u : 16u, sh0 = wid == 3 ? shp : 16u, sh1 = wid == 3 ? shp : 0u;
        const float pv[2][4] = {{bfhi(pa[0] << sh0), bfhi(pa[1] << sh0), bfhi(pa[2] << sh0), bfhi(pa[3] << sh0)},
                                {bfhi(pb[0] << sh1), bfhi(pb[1] << sh1), bfhi(pb[2] << sh1), bfhi(pb[3] << sh1)}};
        const unsigned t0 = lds0 + (wid == 2 ? SL2 - SA : wid == 1 ? PCH : wid == 3 ? 512 : 0), tb[2] = {t0, t0 + (wid == 3 ? PCH : 256)};
        float sc[2];
        kpool<2>(tb, pv, ldt, wid != 3, sc);
        if (lane == 0) { *(LAS float*)(lds + L_SC + wid * 4) = sc[0]; if (wid == 3) *(LAS float*)(lds + L_SC + 16) = sc[1]; }
      }
      STAMP(1);
      if (!(EXP & 4)) __builtin_amdgcn_s_setprio(2);
      const int tw = wid >> 1, fw = wid & 1;
      lptr pB0 = lds + (tw * 64 + r) * 128 + ((q ^ (r >> 1)) << 4);
      lptr pB1 = lds + (tw * 64 + r) * 128 + (((q ^ (r >> 1)) ^ 4) << 4);
      const int sg = (q & 1) * 32;
      lptr pAb = lds + 16384 + (8 * q + (r >> 2)) * 192 + 8 * (lane & 3);
      lptr pA[3] = {pAb + (fw ? 160 - sg : sg), pAb + (fw ? 128 + sg : 32 - sg), pAb + (fw ? 96 - sg : 64 + sg)};
      u32x4 A0[2][3], B0[4], A1[2][3], B1[4];
      auto rd = [&](u32x4 (&A)[2][3], u32x4 (&B)[4], int off, int kk) {
#pragma unroll
        for (int tb = 0; tb < 4; ++tb) B[tb] = ld16((kk ? pB1 : pB0) + off + tb * 2048);
#pragma unroll
        for (int m = 0; m < 2; ++m)
#pragma unroll
          for (int ii = 0; ii < 3; ++ii) A[m][ii] = tr2(pA[ii] + off + m * 12288 + kk * 6144, 768);
      };
      auto mm = [&](u32x4 (&A)[2][3], u32x4 (&B)[4]) {
#pragma unroll
        for (int e = 0; e < 24; ++e) { const int m = e / 12, ii = (e / 4) % 3, tb = e % 4; acc[m][ii][tb] = MFMA_BF(A[m][ii], B[tb], acc[m][ii][tb]); }
      };
      auto sched = [&]() {
#pragma unroll
        for (int k = 0; k < 16; ++k) { SGB(0x8, 1); SGB(0x100, 1); }
        SGB(0x8, 8);
      };
      BAR_L();
      STAMP(2);
      rd(A0, B0, SL0, 0);
      int s0 = SL0, s1 = SL1, s2 = SL2;
#pragma unroll 1
      for (int s = 0; s < 9; ++s) {
        SBAR();
        rd(A1, B1, s0, 1); mm(A0, B0); sched();
        BAR_L();
        rd(A0, B0, s1, 0); mm(A1, B1); sched();
        SBAR();
        const int t = s0; s0 = s1; s1 = s2; s2 = t;
      }
      rd(A1, B1, SL0, 1); mm(A0, B0); sched();
      SBAR();
      flag_add(fb, FL_G1, lane);
      mm(A1, B1);
      SBAR();
      if (EXP & 64) __builtin_amdgcn_s_setprio(0);
      STAMP(3);
      unsigned bo[4], ao[4];
#pragma unroll
      for (int c = 0; c < 4; ++c) { const int cc = wid * 4 + ((c + nb) & 3); bo[c] = WS_WD + (nb * 48 + cc * 3) * 5120; ao[c] = WS_H + (tg * 16 + cc) * 12288; }
      const unsigned l16 = lane * 16;
#define BFRAG(k, n) __builtin_amdgcn_raw_buffer_load_b128(rs, l16, bo[(k) < 8 ? (k) >> 1 : (k) - 8] + ((k) < 8 ? ((k) & 1) * 5120 : 10240) + (n) * 1024, SC1)
#define AFRAG(k, t) __builtin_amdgcn_raw_buffer_load_b128(rs, l16, ao[(k) < 8 ? (k) >> 1 : (k) - 8] + ((k) < 8 ? ((k) & 1) * 4096 : 8192) + (t) * 1024, (EXP & 8) ? 0 : SC1)
      u32x4 tp[20];
      flag_wait(fb, FL_EPI, 1);
      tag = uni64(*(volatile LAS u64*)(lds + L_FLG + FL_TAG * 4));
      STAMP(4);
      {
        float nmb[4], al[4], sd[4];
#pragma unroll
        for (int tb = 0; tb < 4; ++tb) { const f32x2 st = *(LAS f32x2*)(lds + L_STAT + (tw * 64 + tb * 16 + r) * 8); nmb[tb] = -st.x; sd[tb] = st.y; al[tb] = -1.44269504f * __builtin_amdgcn_rcpf(st.y); }
        const unsigned hoff = WS_H + ((rb * 2 + tw) * 16 + cb) * 12288 + lane * 16;
        auto hcalc = [&](const f32x4& g4, const f32x4& u4, const f32x4& cg, const f32x4& cu, int tb) {
          float hv[4];
#pragma unroll
          for (int j = 0; j < 4; ++j) {
            const float g = __builtin_fmaf(nmb[tb], cg[j], g4[j]) * al[tb];
            const float u = __builtin_fmaf(nmb[tb], cu[j], u4[j]);
            const float e = __builtin_amdgcn_exp2f(g);
            hv[j] = u * __builtin_amdgcn_rcpf(__builtin_fmaf(e, sd[tb], sd[tb]));
          }
          return u32x2{pkbf(hv[0], hv[1]), pkbf(hv[2], hv[3])};
        };
        lptr pcs = lds + L_CS + (fw * 48 + 4 * q) * 4;
        const int oa = fw ? 128 : 0, oc = fw ? 0 : 128;
        {
          const f32x4 cga = *(LAS f32x4*)(pcs + oa), cua = *(LAS f32x4*)(pcs + 384 + oa), cgb = *(LAS f32x4*)(pcs + 64), cub = *(LAS f32x4*)(pcs + 384 + 64);
#pragma unroll
          for (int tb = 0; tb < 4; ++tb) {
            tp[4 * tb] = BFRAG(8 + (4 * tb) / 5, (4 * tb) % 5); tp[4 * tb + 1] = BFRAG(8 + (4 * tb + 1) / 5, (4 * tb + 1) % 5);
            const u32x2 ha = hcalc(acc[0][0][tb], acc[1][0][tb], cga, cua, tb);
            SGB(0x20, 1); SGB(0x6, 18); SGB(0x20, 1); SBAR();
            tp[4 * tb + 2] = BFRAG(8 + (4 * tb + 2) / 5, (4 * tb + 2) % 5); tp[4 * tb + 3] = BFRAG(8 + (4 * tb + 3) / 5, (4 * tb + 3) % 5);
            const u32x2 hb = hcalc(acc[0][1][tb], acc[1][1][tb], cgb, cub, tb);
            SGB(0x20, 1); SGB(0x6, 18); SGB(0x20, 1); SBAR();
            ST_SC1((u32x4{ha[0], ha[1], hb[0], hb[1]}), hoff + fw * 4096 + tb * 1024);
            SBAR();
          }
        }
        {
          const f32x4 cg = *(LAS f32x4*)(pcs + oc), cu = *(LAS f32x4*)(pcs + 384 + oc);
#pragma unroll
          for (int t2 = 0; t2 < 2; ++t2) {
            tp[16 + 2 * t2] = BFRAG(11, 1 + 2 * t2);
            const u32x2 ha = hcalc(acc[0][2][2 * t2], acc[1][2][2 * t2], cg, cu, 2 * t2);
            SBAR();
            tp[17 + 2 * t2] = BFRAG(11, 2 + 2 * t2);
            const u32x2 hb = hcalc(acc[0][2][2 * t2 + 1], acc[1][2][2 * t2 + 1], cg, cu, 2 * t2 + 1);
            SBAR();
            ST_SC1((u32x4{ha[0], ha[1], hb[0], hb[1]}), hoff + 8192 + fw * 2048 + t2 * 1024);
            SBAR();
          }
        }
      }
      if (EXP & 128) __builtin_amdgcn_s_setprio(0);
      STAMP(5);
      lptr park = lds + SL2 + wid * 20480 + lane * 16;
      waitv<6>();
      if (flag_add(fb, FL_H1, lane) == 3u && lane < 16) __hip_atomic_store((GAS u64*)(ws + WS_MB + (rb * 16 + lane) * MBS + cb * 8), tag, RLX_AGENT);
      flag_wait(fb, FL_G1, 4);
#pragma unroll
      for (int j = 0; j < 16; ++j) st16(park + j * 1024, tp[j]);
      SBAR();
      waitv<0>();
      if (flag_add(fb, FL_H2, lane) == 3u && lane < 16) __hip_atomic_store((GAS u64*)(ws + WS_MB + (rb * 16 + lane) * MBS + 128 + cb * 8), tag, RLX_AGENT);
#pragma unroll
      for (int j = 16; j < 20; ++j) st16(park + j * 1024, tp[j]);
      STAMP(6);
      u32x4 Bw[4][5];
#pragma unroll
      for (int k = 0; k < 4; ++k)
#pragma unroll
        for (int n = 0; n < 5; ++n) Bw[k][n] = BFRAG(k, n);
      STAMP(7);
      const unsigned fo = WS_MB + (tg * 8 + nb) * MBS + (wid * 4 + (lane & 3)) * 8;
      _Pragma("nounroll") for (int sp = 0; sp < SPIN_GLB; ++sp) { if (wall(gflag_ok(ws, fo, tag))) break; __builtin_amdgcn_s_sleep(2); }
      asm volatile("" ::: "memory");
      if (EXP & 1024) __builtin_amdgcn_s_sleep(127);
      STAMP(8);
      f32x4 a2[5][4];
#pragma unroll
      for (int n = 0; n < 5; ++n)
#pragma unroll
        for (int tb = 0; tb < 4; ++tb) a2[n][tb] = f32x4{0, 0, 0, 0};
      u32x4 Ah[4][4];
#pragma unroll
      for (int k = 0; k < 3; ++k)
#pragma unroll
        for (int tb = 0; tb < 4; ++tb) Ah[k][tb] = AFRAG(k, tb);
      u64 f2 = 0;
      SBAR();
#pragma unroll
      for (int k = 0; k < 12; ++k) {
        if (k == 3) { f2 = __hip_atomic_load((GAS u64*)(ws + fo + 128), RLX_AGENT); SBAR(); }
        if (k == 5) {
          if (!wall(f2 == tag)) { _Pragma("nounroll") for (int sp = 0; sp < SPIN_GLB; ++sp) { if (wall(gflag_ok(ws, fo + 128, tag))) break; __builtin_amdgcn_s_sleep(2); } }
          asm volatile("" ::: "memory");
        }
        u32x4 hf[4];
        if (k >= 8) {
          const u32x4 X0 = Ah[k & 3][0], X1 = Ah[k & 3][1], Y0 = Ah[k & 3][2], Y1 = Ah[k & 3][3];
          hf[0] = u32x4{X0[0], X0[1], Y0[0], Y0[1]}; hf[1] = u32x4{X0[2], X0[3], Y0[2], Y0[3]};
          hf[2] = u32x4{X1[0], X1[1], Y1[0], Y1[1]}; hf[3] = u32x4{X1[2], X1[3], Y1[2], Y1[3]};
        } else {
#pragma unroll
          for (int tb = 0; tb < 4; ++tb) hf[tb] = Ah[k & 3][tb];
        }
#pragma unroll
        for (int e = 0; e < 20; ++e) {
          const int n = e >> 2, tb = e & 3;
          a2[n][tb] = MFMA_BF(Bw[k & 3][n], hf[tb], a2[n][tb]);
          if (e & 1) {
            const int j = e >> 1;
            if (j < 4) { if (k + 3 < 12) Ah[(k + 3) & 3][j] = AFRAG(k + 3, j); }
            else if (j < 9 && k >= 1 && k + 3 < 12) {
              if (k + 3 < 8) { if (!(EXP & 16)) Bw[(k + 3) & 3][j - 4] = BFRAG(k + 3, j - 4); }
              else Bw[(k + 3) & 3][j - 4] = ld16(park + ((k - 5) * 5 + j - 4) * 1024);
            }
            SBAR();
          }
        }
#pragma unroll
        for (int n = 0; n < 5; ++n) asm volatile("" : "+v"(a2[n][0]), "+v"(a2[n][1]), "+v"(a2[n][2]), "+v"(a2[n][3]));
        if ((EXP & 512) && k < 11) ARR();
      }
      STAMP(9);
      {
        lptr red = lds + SL2 + wid * 20480;
        const int l2 = fresh_lane(), r = l2 & 15, q = l2 >> 4;
#pragma unroll
        for (int n = 0; n < 5; ++n)
#pragma unroll
          for (int tb = 0; tb < 4; ++tb) {
            *(LAS f32x4*)(red + (4 * n + q) * 1024 + ((tb * 16 + r + 3 * n) & 63) * 16) = a2[n][tb];
          }
      }
      __builtin_amdgcn_s_setprio(0);
    }
  }

  if (wid >= 4 && !(EXP & 1)) {
    const int sw = wid - 4;
    const int lane = fresh_lane(), r = lane & 15, q = lane >> 4;
    if (EXP & 256) __builtin_amdgcn_s_setprio(3);
    const int hm = sw & 1;
    lptr am = lds + SA + hm * PCH;
    f32x4 ca[8];
#pragma unroll
    for (int t = 0; t < 8; ++t) ca[t] = f32x4{0, 0, 0, 0};
    lptr pmain = am + O_KL + 1312 + lane * 16;
    STAMP(4);
    if (sw < 2) flag_wait(fb, FL_KA + hm, 1);
    flag_wait(fb, FL_U, 2);
    STAMP(5);
    conv_units<0xff>(am + O_KREV, lds + L_U + (hm + odd) * UCH, lane, sw < 2 ? 5 : 0, sw < 2 ? 3 : 5, ca);
    if (sw < 2) {
      lptr pc = lds + L_PC;
#pragma unroll
      for (int t = 0; t < 8; ++t) { *(LAS f32x4*)(pmain + t * 1024) = ca[t]; ca[t] = f32x4{0, 0, 0, 0}; }
      flag_set(fb, FL_PA + hm, 1);
      flag_wait(fb, FL_KC, 1);
      lptr uc_ = lds + L_U + (odd ? 0 : 2) * UCH;
      if (odd) conv_units<0x3a>(lds + L_KC, uc_, lane, hm * 4, 4, ca);
      else conv_units<0xc5>(lds + L_KC, uc_, lane, hm * 4, 4, ca);
      __builtin_amdgcn_s_setprio(3);
      STAMP(9);
      f32x4 c4[4];
      c4[0] = odd ? ca[1] : ca[0]; c4[1] = odd ? ca[3] : ca[2]; c4[2] = odd ? ca[4] : ca[6]; c4[3] = odd ? ca[5] : ca[7];
      if (!hm) {
#pragma unroll
        for (int t = 0; t < 4; ++t) *(LAS f32x4*)(pc + t * 1024 + lane * 16) = c4[t];
        flag_set(fb, FL_PC, 1);
      } else {
        flag_wait(fb, FL_PC, 1);
#pragma unroll
        for (int t = 0; t < 4; ++t) c4[t] += *(LAS f32x4*)(pc + t * 1024 + lane * 16);
        waitl();
#pragma unroll
        for (int t = 0; t < 4; ++t) { *(LAS unsigned*)(pc + t * 512 + q * 128 + r * 4) = pkbf(c4[t][0], c4[t][1]); *(LAS unsigned*)(pc + t * 512 + q * 128 + r * 4 + 64) = pkbf(c4[t][2], c4[t][3]); }
#pragma unroll
        for (int i = 0; i < 2; ++i) {
          const int s4 = 2 * i + (lane >> 5);
          const int T = odd ? (s4 == 0 ? 1 : s4 + 2) : (s4 < 2 ? 2 * s4 : s4 + 4);
          ST_SC1(ld16(pc + i * 1024 + lane * 16), WS_Y + ((T * 4 + ((lane >> 3) & 3)) * DM + chC) * 128 + (lane & 7) * 16);
        }
      }
    } else {
      __builtin_amdgcn_s_setprio(3);
      STAMP(9);
      flag_wait(fb, FL_PA + hm, 1);
#pragma unroll
      for (int t = 0; t < 8; ++t) ca[t] += *(LAS f32x4*)(pmain + t * 1024);
#pragma unroll
      for (int t = 0; t < 8; ++t) { *(LAS unsigned*)(am + t * 512 + q * 128 + r * 4) = pkbf(ca[t][0], ca[t][1]); *(LAS unsigned*)(am + t * 512 + q * 128 + r * 4 + 64) = pkbf(ca[t][2], ca[t][3]); }
#pragma unroll
      for (int i = 0; i < 4; ++i) ST_SC1(ld16(am + i * 1024 + lane * 16), WS_Y + ((i * 8 + (lane >> 3)) * DM + chA + hm) * 128 + (lane & 7) * 16);
    }
    STAMP(6);
    waitv<0>();
    if (flag_add(fb, FL_YC, lane) == 3u) {
      if (lane < 32) __hip_atomic_store((GAS u64*)(ws + WS_MB + (lane * 8 + xcd) * MBS + 256 + bi * 8), tag, RLX_AGENT);
      _Pragma("nounroll") for (int sp = 0; sp < SPIN_GLB; ++sp) { if (wall(gflag_ok(ws, WS_MB + (tg * 8 + nb) * MBS + 256 + (lane & 31) * 8, tag))) break; __builtin_amdgcn_s_sleep(4); }
      flag_set(fb, FL_YOK, 1);
    }
    STAMP(7);
    flag_wait(fb, FL_YOK, 1);
    STAMP(8);
    u32x4 yv[3];
#pragma unroll
    for (int j = 0; j < 3; ++j) {
      const int p = umin(sw + 4 * j, 9), c = p * 64 + lane, row = (c * 6554) >> 16;
      glds1((const char*)x + (size_t)tg * 64 * 1280 + nb * 160, row * 1280 + (c - row * 10) * 16, lds0 + L_XT + p * 1024);
    }
#pragma unroll
    for (int j = 0; j < 3; ++j) { const int p = umin(sw + 4 * j, 9); yv[j] = LD_SC1(WS_Y + (tg * DM + nb * 80 + p * 8) * 128 + lane * 16); }
    lptr yb = lds + L_YB + sw * 3072;
#pragma unroll
    for (int j = 0; j < 3; ++j) st16(yb + j * 1024 + lane * 16, yv[j]);
#pragma unroll
    for (int j = 0; j < 3; ++j) {
      const u32x4 a = tr2(yb + j * 1024 + (r >> 2) * 128 + q * 32 + (lane & 3) * 8, 512);
      if (sw + 4 * j < 10) st16(lds + L_BASE + lane * 160 + (sw + 4 * j) * 16, a);
    }
  }
  STAMP(10);
  BAR_L();
  STAMP(11);
  if (wid < 5) {
    const int tid = wid * 64 + fresh_lane();
    const int tok = (tid * 13108) >> 16, g = tid - tok * 5;
    const int o = g * 4096 + ((tok + 3 * g) & 63) * 16;
    f32x4 s[4];
#pragma unroll
    for (int m = 0; m < 4; ++m)
      s[m] = (*(LAS f32x4*)(lds + SL0 + o + m * 1024) + *(LAS f32x4*)(lds + SL0 + 20480 + o + m * 1024)) +
             (*(LAS f32x4*)(lds + SL2 + o + m * 1024) + *(LAS f32x4*)(lds + SL2 + 20480 + o + m * 1024));
    u16* op = out + (size_t)(tg * 64 + tok) * DM + nb * 80 + g * 16;
#pragma unroll
    for (int hh = 0; hh < 2; ++hh) {
      const u32x4 bs = ld16(lds + L_BASE + tid * 32 + hh * 16), xs = ld16(lds + L_XT + tid * 32 + hh * 16);
      const f32x4 sa = s[2 * hh], sb = s[2 * hh + 1];
      *(u32x4*)(op + hh * 8) = u32x4{pkbf((bflo(xs[0]) + bflo(bs[0])) + sa[0], (bfhi(xs[0]) + bfhi(bs[0])) + sa[1]), pkbf((bflo(xs[1]) + bflo(bs[1])) + sa[2], (bfhi(xs[1]) + bfhi(bs[1])) + sa[3]),
                                     pkbf((bflo(xs[2]) + bflo(bs[2])) + sb[0], (bfhi(xs[2]) + bfhi(bs[2])) + sb[1]), pkbf((bflo(xs[3]) + bflo(bs[3])) + sb[2], (bfhi(xs[3]) + bfhi(bs[3])) + sb[3])};
    }
  }
}

#if defined(__HIP_DEVICE_COMPILE__)
#pragma clang attribute pop
#endif

extern "C" void kernel_launch(void* const* d_in, const int* in_sizes, int n_in, void* d_out, int out_size, void* d_ws, size_t ws_size, hipStream_t stream) {
  (void)in_sizes; (void)n_in; (void)out_size; (void)ws_size;
  (void)hipFuncSetAttribute((const void*)fused_kernel, hipFuncAttributeMaxDynamicSharedMemorySize, LDS_BYTES);
  fused_kernel<<<256, 512, LDS_BYTES, stream>>>((const u16*)d_in[0], (const u16*)d_in[1], (const u16*)d_in[2], (const u16*)d_in[3], (const u16*)d_in[4],
                                                (const u16*)d_in[5], (const u16*)d_in[6], (const u16*)d_in[8], (const u16*)d_in[9], (const u16*)d_in[10],
                                                (u16*)d_out, (char*)d_ws);
}
```

```cpp
#include <hip/hip_runtime.h>
#include <stdint.h>

#if defined(__HIP_DEVICE_COMPILE__)
#pragma clang attribute push(__attribute__((target("no-packed-fp32-ops"))), apply_to = function)
#endif
typedef unsigned short u16;
typedef unsigned long long u64;
typedef short bf16x8 __attribute__((ext_vector_type(8)));
typedef _Float16 f16x8 __attribute__((ext_vector_type(8)));
typedef short s16x4 __attribute__((ext_vector_type(4)));
typedef float f32x2 __attribute__((ext_vector_type(2)));
typedef float f32x4 __attribute__((ext_vector_type(4)));
typedef unsigned u32x2 __attribute__((ext_vector_type(2)));
typedef unsigned u32x4 __attribute__((ext_vector_type(4)));
typedef _Float16 h16x2 __attribute__((ext_vector_type(2)));
typedef __bf16 b16x2 __attribute__((ext_vector_type(2)));
#define LAS __attribute__((address_space(3)))
#define GAS __attribute__((address_space(1)))
typedef LAS char* lptr;
#define DI __device__ __forceinline__
#define SBAR() __builtin_amdgcn_sched_barrier(0)
#define SGB(m, n) __builtin_amdgcn_sched_group_barrier(m, n, 0)
#define MFMA_BF(a, b, c) __builtin_amdgcn_mfma_f32_16x16x32_bf16(__builtin_bit_cast(bf16x8, a), __builtin_bit_cast(bf16x8, b), c, 0, 0, 0)
#define MFMA_H(a, b, c) __builtin_amdgcn_mfma_f32_16x16x32_f16(__builtin_bit_cast(f16x8, a), __builtin_bit_cast(f16x8, b), c, 0, 0, 0)
#define SC1 16
#define EXP 0

constexpr int DM = 640, NM = 192;
constexpr unsigned WS_EPOCH = 0, WS_F0 = 4096, F0REP = 4352;
constexpr unsigned WS_MB = 262144, MBS = 1280;
constexpr unsigned WS_ST = 65536, WS_CS = 98304, WS_XB = 655360, WS_WD = WS_XB + 2621440 + 4096;
constexpr unsigned WS_H = WS_WD + 1966080, WS_Y = WS_H + 6291456, WS_END = WS_Y + 2621440;
constexpr unsigned WS_STAMP = 16u << 20;
constexpr int STAGE = 40960, SL1 = 0, SA = 40960, SL2 = 81920, SL0 = 122880, POOL_BIAS = SA;
constexpr int PROW = 784, PCH = 23 * PROW;
constexpr int KC1 = 4228, O_KREV = 128, O_KL = 8528;
constexpr int L_FLG = SA + 2 * PCH, L_SC = L_FLG + 128, L_STG = L_FLG + 192, STGW = 2304;
constexpr int L_STAT = L_STG, L_CS = L_STG + 1024;
constexpr int L_RTB = SL2 + 29696;
constexpr int L_LNB = SL2 + 18432, LNR = 1344;
constexpr int UCH = 9232, UC1 = 4640;
constexpr int L_U = SL1 + 128, L_KC = L_U + 3 * UCH + 128, L_PC = L_KC + 8400;
constexpr int L_XT = SL1;
constexpr int L_BASE = SA, L_YB = SA + 10240;
constexpr int LDS_BYTES = 163840;
static_assert(O_KL + 1312 + 8192 <= PCH && L_LNB + 8 * LNR <= L_RTB && L_RTB + 10240 <= SL0 && L_PC + 4096 <= SA && L_STG + 2 * STGW <= SL0 && (L_KC & 15) == 0 && (L_PC & 15) == 0 && (L_STG & 15) == 0, "lds map");
enum { FL_F0 = 0, FL_EPI, FL_U, FL_KA, FL_KB, FL_KC, FL_KP, FL_PA, FL_PB, FL_PC, FL_YC, FL_YOK, FL_H1, FL_H2, FL_G1, FL_S2, FL_TAG = 16 };
constexpr int SPIN_LDS = 1 << 18, SPIN_GLB = 1 << 15;

DI bool wall(bool p) { return __builtin_amdgcn_ballot_w64(!p) == 0; }
DI unsigned umin(unsigned a, unsigned b) { return a < b ? a : b; }
DI unsigned umax(unsigned a, unsigned b) { return a > b ? a : b; }
DI float asf(unsigned v) { return __builtin_bit_cast(float, v); }
DI unsigned asu(float v) { return __builtin_bit_cast(unsigned, v); }
DI float bflo(unsigned v) { return asf(v << 16); }
DI float bfhi(unsigned v) { return asf(v & 0xffff0000u); }
DI float bf1(u16 v) { return asf((unsigned)v << 16); }
DI unsigned pkbf(float lo, float hi) { return __builtin_bit_cast(unsigned, __builtin_convertvector(f32x2{lo, hi}, b16x2)); }
DI unsigned pkh(f32x2 v) { return __builtin_bit_cast(unsigned, __builtin_convertvector(v, h16x2)); }
DI float rbf(float x) { return bflo(pkbf(x, x)); }
template <int CTRL> DI float dppf(float x) { return __builtin_bit_cast(float, __builtin_amdgcn_mov_dpp(__builtin_bit_cast(int, x), CTRL, 0xf, 0xf, true)); }
DI float row16_sum(float v) { v += dppf<0xB1>(v); v += dppf<0x4E>(v); v += dppf<0x124>(v); v += dppf<0x128>(v); return v; }

DI void glds4(const void* sb, unsigned v0, unsigned v1, unsigned v2, unsigned v3, unsigned lds) {
  asm volatile("s_mov_b32 m0, %5\n\ts_nop 0\n\tglobal_load_lds_dwordx4 %0, %4\n\tglobal_load_lds_dwordx4 %1, %4 offset:1024\n\t"
               "global_load_lds_dwordx4 %2, %4 offset:2048\n\tglobal_load_lds_dwordx4 %3, %4 offset:3072"
               :: "v"(v0), "v"(v1), "v"(v2), "v"(v3), "s"(sb), "s"(lds) : "memory");
}
DI void glds1(const void* sb, unsigned v0, unsigned lds) {
  asm volatile("s_mov_b32 m0, %2\n\ts_nop 0\n\tglobal_load_lds_dwordx4 %0, %1" :: "v"(v0), "s"(sb), "s"(lds) : "memory");
}
DI void glds2(const void* sb, unsigned v0, unsigned v1, unsigned lds) {
  asm volatile("s_mov_b32 m0, %3\n\ts_nop 0\n\tglobal_load_lds_dwordx4 %0, %2\n\tglobal_load_lds_dwordx4 %1, %2 offset:1024"
               :: "v"(v0), "v"(v1), "s"(sb), "s"(lds) : "memory");
}
template <int N> DI void waitv() { asm volatile("s_waitcnt vmcnt(%0)" :: "i"(N) : "memory"); }
DI void waitl() { asm volatile("s_waitcnt lgkmcnt(0)" ::: "memory"); }
DI u32x2 vtr(lptr p) { return __builtin_bit_cast(u32x2, __builtin_amdgcn_ds_read_tr16_b64_v4i16((LAS s16x4*)p)); }
DI u32x4 tr2(lptr p, int d) { u32x2 lo = vtr(p), hi = vtr(p + d); return u32x4{lo[0], lo[1], hi[0], hi[1]}; }
DI u32x4 ld16(lptr p) { return *(LAS u32x4*)p; }
DI void st16(lptr p, u32x4 v) { *(LAS u32x4*)p = v; }

DI unsigned flag_ld(unsigned addr) { unsigned r; asm volatile("ds_read_b32 %0, %1\n\ts_waitcnt lgkmcnt(0)" : "=v"(r) : "v"(addr) : "memory"); return __builtin_amdgcn_readfirstlane(r); }
DI void flag_wait(unsigned fb, int idx, unsigned need) { _Pragma("nounroll") for (int sp = 0; sp < SPIN_LDS && flag_ld(fb + idx * 4) < need; ++sp) __builtin_amdgcn_s_sleep(1); }
DI void flag_set(unsigned fb, int idx, unsigned v) {
  asm volatile("s_waitcnt lgkmcnt(0)\n\ts_mov_b64 exec, 1\n\tds_write_b32 %0, %1\n\ts_mov_b64 exec, -1" :: "v"(fb + idx * 4), "v"(v) : "memory"); }
DI unsigned flag_add(unsigned fb, int idx, int lane) {
  unsigned r; (void)lane;
  asm volatile("s_waitcnt lgkmcnt(0)\n\ts_mov_b64 exec, 1\n\tds_add_rtn_u32 %0, %1, %2\n\ts_mov_b64 exec, -1\n\ts_waitcnt lgkmcnt(0)" : "=&v"(r) : "v"(fb + idx * 4), "v"(1u) : "memory");
  return __builtin_amdgcn_readfirstlane(r);
}
#define RLX_AGENT __ATOMIC_RELAXED, __HIP_MEMORY_SCOPE_AGENT
DI bool gflag_ok(const char* ws, unsigned off, u64 tag) { return __hip_atomic_load((GAS u64*)(ws + off), RLX_AGENT) == tag; }

#define STAMP(i) do {} while (0)

DI f32x2 csq(f32x2 x) { const float t = x.x + x.x; return f32x2{__builtin_fmaf(x.x, x.x, -(x.y * x.y)), t * x.y}; }
DI h16x2 toh(f32x2 v) { return __builtin_convertvector(v, h16x2); }
DI h16x2 hmul(h16x2 x, h16x2 y) { return __builtin_elementwise_fma(h16x2{x.y, x.y}, h16x2{-y.y, y.x}, h16x2{x.x, x.x} * y); }
DI f32x2 cexp_p(float a2, float bh, float bl, float p) {
  const float e = __builtin_amdgcn_exp2f(a2 * p);
  const float ph = __builtin_fmaf(bl, p, __builtin_amdgcn_fractf(bh * p));
  return f32x2{e * __builtin_amdgcn_cosf(ph), e * __builtin_amdgcn_sinf(ph)};
}
template <int NS, int ROW0> DI void put4(const unsigned (&tb)[NS], const h16x2 (&a)[NS], const h16x2 (&b)[NS], const h16x2 (&c)[NS], const h16x2 (&d)[NS]) {
#pragma unroll
  for (int e = 0; e < NS; ++e)
    asm volatile("s_mov_b32 m0, %4\n\ts_nop 0\n\tds_write_addtid_b32 %0 offset:%5\n\tds_write_addtid_b32 %1 offset:%5+784\n\t"
                 "ds_write_addtid_b32 %2 offset:%5+1568\n\tds_write_addtid_b32 %3 offset:%5+2352"
                 :: "v"(a[e]), "v"(b[e]), "v"(c[e]), "v"(d[e]), "s"(tb[e]), "i"(ROW0 * PROW + POOL_BIAS) : "memory");
}
#define FORS _Pragma("unroll") for (int e = 0; e < NS; ++e)
template <int NS> DI void kpool(const unsigned (&tb)[NS], const float (&pv)[NS][4], const float (&ldt)[NS], bool join, float (&sc)[NS]) {
  float dt[NS];
  FORS dt[e] = rbf(__builtin_amdgcn_exp2f(ldt[e] * 1.44269504f));
  unsigned eb[NS]; float cs[NS];
  FORS {
    unsigned mi = umax(asu(pv[e][2]) & 0x7fffffffu, asu(pv[e][3]) & 0x7fffffffu);
    mi = umax(mi, asu(dppf<0xB1>(asf(mi)))); mi = umax(mi, asu(dppf<0x4E>(asf(mi)))); mi = umax(mi, asu(dppf<0x124>(asf(mi)))); mi = umax(mi, asu(dppf<0x128>(asf(mi))));
    eb[e] = umax(umax((unsigned)__builtin_amdgcn_readlane(mi, 0), (unsigned)__builtin_amdgcn_readlane(mi, 16)),
                 umax((unsigned)__builtin_amdgcn_readlane(mi, 32), (unsigned)__builtin_amdgcn_readlane(mi, 48))) >> 23;
  }
  if (join) eb[0] = eb[NS - 1] = umax(eb[0], eb[NS - 1]);
  FORS { eb[e] = umin(umax(eb[e], 2u), 252u); cs[e] = asf((254u - eb[e]) << 23); }
  float a2[NS], bh[NS], bl[NS];
  FORS {
    const float ea = rbf(__builtin_amdgcn_exp2f(pv[e][0] * 1.44269504f));
    a2[e] = -(ea * dt[e]) * 1.44269504f;
    const float b = pv[e][1] * dt[e];
    bh[e] = b * 0.15915494f;
    bl[e] = __builtin_fmaf(b, 6.4206382e-9f, __builtin_fmaf(b, 0.15915494f, -bh[e]));
  }
  h16x2 one[NS], w8[NS], w16[NS], w24[NS], w32[NS], w64[NS], w96[NS], w1[NS], w2[NS], w3[NS], w4[NS], C[NS], c4[NS];
  FORS {
    const f32x2 f1 = cexp_p(a2[e], bh[e], bl[e], 1.f), f2 = csq(f1), f4 = csq(f2);
    const f32x2 f8 = csq(f32x2{f4.x, -f4.y}), f16 = csq(f8), f32 = csq(f16), f64 = csq(f32);
    one[e] = h16x2{(_Float16)1.f, (_Float16)0.f};
    w8[e] = toh(f8); w16[e] = toh(f16); w32[e] = toh(f32); w64[e] = toh(f64); w24[e] = hmul(w8[e], w16[e]); w96[e] = hmul(w32[e], w64[e]);
    w1[e] = toh(f1); w2[e] = toh(f2); w4[e] = toh(f4); w3[e] = hmul(w1[e], w2[e]);
    C[e] = toh(f32x2{pv[e][2] * cs[e], pv[e][3] * cs[e]}); c4[e] = hmul(C[e], w4[e]);
  }
  put4<NS, 0>(tb, one, w8, w16, w24);
  put4<NS, 4>(tb, w32, w64, w96, w1);
  put4<NS, 8>(tb, w2, w3, C, c4);
  h16x2 c128[NS], c132[NS], w256[NS], w258[NS], w512[NS], w768[NS], w1024[NS], w1280[NS], w1536[NS], w1792[NS], w1796[NS];
  FORS {
    const f32x2 f128 = cexp_p(a2[e], bh[e], bl[e], 128.f), f256 = csq(f128), f512 = csq(f256);
    const h16x2 w128 = toh(f128);
    w256[e] = toh(f256); w512[e] = toh(f512); c128[e] = hmul(C[e], w128); c132[e] = hmul(c4[e], w128);
    w258[e] = hmul(w256[e], w2[e]); w768[e] = hmul(w512[e], w256[e]);
    w1024[e] = toh(csq(f512)); w1280[e] = hmul(w1024[e], w256[e]); w1536[e] = hmul(w1024[e], w512[e]);
    w1792[e] = hmul(w1024[e], w768[e]); w1796[e] = hmul(w1792[e], w4[e]);
  }
  put4<NS, 12>(tb, c128, c132, w256, w258);
  put4<NS, 16>(tb, w512, w768, w1024, w1280);
  put4<NS, 19>(tb, w1280, w1536, w1792, w1796);
  FORS sc[e] = 2.f * dt[e] * asf(eb[e] << 23);
}
struct KPtr {
  lptr pbx, pby, py, px0, px2;
  DI void init(lptr pool, int lane) {
    const int r = lane & 15, x = r & 3, y = r >> 2;
    lptr p = pool + (lane >> 4) * 16;
    pbx = p + x * PROW; pby = p + (y ? 3 + y : 0) * PROW; py = p + (10 + y) * PROW; px0 = p + (x ? 6 + x : 0) * PROW; px2 = p + (y == 3 ? 22 : 18 + x) * PROW;
  }
  DI void fetch(u32x4 (&v)[6], int o) const {
    v[0] = ld16(pbx + o); v[1] = ld16(pby + o); v[2] = ld16(py + o); v[3] = ld16(px0 + o); v[4] = ld16(pbx + 14 * PROW + o); v[5] = ld16(px2 + o);
  }
  DI void advance() { pbx += 128; pby += 128; py += 128; px0 += 128; px2 += 128; }
};
DI void kprod(const u32x4 (&v)[6], u32x4 (&o)[4]) {
  unsigned t[16];
  if (EXP & 2048) { SBAR(); __builtin_amdgcn_s_setprio(3); SBAR(); }
#pragma unroll
  for (int i = 0; i < 16; ++i) { const unsigned xe = v[(i >> 2) ? 2 + (i >> 2) : 0][i & 3], ye = v[(i >> 2) ? 2 : 1][i & 3];
    asm("v_pk_mul_f16 %0, %1, %2 op_sel_hi:[0,1]" : "=v"(t[i]) : "v"(xe), "v"(ye)); }
  SBAR();
#pragma unroll
  for (int i = 0; i < 16; ++i) { const unsigned xe = v[(i >> 2) ? 2 + (i >> 2) : 0][i & 3], ye = v[(i >> 2) ? 2 : 1][i & 3]; unsigned z;
    asm("v_pk_fma_f16 %0, %1, %2, %3 op_sel:[1,1,0] op_sel_hi:[1,0,1] neg_lo:[0,1,0]" : "=v"(z) : "v"(xe), "v"(ye), "v"(t[i])); o[i >> 2][i & 3] = z; }
  SBAR();
}
template <int H> DI void kprodh(const u32x4 (&v)[6], u32x4 (&o)[4]) {
  unsigned t[8];
#pragma unroll
  for (int i = 8 * H; i < 8 * H + 8; ++i) { const unsigned xe = v[(i >> 2) ? 2 + (i >> 2) : 0][i & 3], ye = v[(i >> 2) ? 2 : 1][i & 3];
    asm("v_pk_mul_f16 %0, %1, %2 op_sel_hi:[0,1]" : "=v"(t[i & 7]) : "v"(xe), "v"(ye)); }
  SBAR();
#pragma unroll
  for (int i = 8 * H; i < 8 * H + 8; ++i) { const unsigned xe = v[(i >> 2) ? 2 + (i >> 2) : 0][i & 3], ye = v[(i >> 2) ? 2 : 1][i & 3]; unsigned z;
    asm("v_pk_fma_f16 %0, %1, %2, %3 op_sel:[1,1,0] op_sel_hi:[1,0,1] neg_lo:[0,1,0]" : "=v"(z) : "v"(xe), "v"(ye), "v"(t[i & 7])); o[i >> 2][i & 3] = z; }
  asm volatile("" : "+v"(o[2 * H]), "+v"(o[2 * H + 1]));
  SBAR();
}
DI void kmma1(const u32x4 (&o0)[4], f32x4 (&acc)[3]) {
  SBAR(); asm volatile("s_setprio 3\n\ts_nop 1"); SBAR();
#pragma unroll
  for (int i = 0; i < 3; ++i) acc[i] = MFMA_H(o0[1 + i], o0[0], acc[i]);
  SBAR(); __builtin_amdgcn_s_setprio(0); SBAR();
}
DI void kscale(f32x4 (&ko)[3], f32x4 (&acc)[3], float sc) {
#pragma unroll
  for (int i = 0; i < 3; ++i) {
#pragma unroll
    for (int j = 0; j < 4; ++j) ko[i][j] = __builtin_fmaf(acc[i][j], sc, ko[i][j]);
    acc[i] = f32x4{0, 0, 0, 0};
  }
}
DI float ubf(const u16* p, int i) { const unsigned w = ((const __attribute__((address_space(4))) unsigned*)(uintptr_t)p)[i >> 1]; return (i & 1) ? bfhi(w) : bflo(w); }
DI void kmma2(const u32x4 (&o0)[4], const u32x4 (&o1)[4], f32x4 (&acc)[3]) {
  SBAR(); asm volatile("s_setprio 3\n\ts_nop 1"); SBAR();
#pragma unroll
  for (int i = 0; i < 3; ++i) acc[i] = MFMA_H(o0[1 + i], o0[0], acc[i]);
#pragma unroll
  for (int i = 0; i < 3; ++i) acc[i] = MFMA_H(o1[1 + i], o1[0], acc[i]);
  SBAR(); __builtin_amdgcn_s_setprio(0); SBAR();
}
DI void kstore(lptr kl, int lane, const f32x4 (&acc)[3], float dsk) {
  const int c = lane & 15, q = lane >> 4;
  LAS u16* t = (LAS u16*)kl;
  const float k0 = acc[0][0] + (lane == 0 ? dsk : 0.f);
  *(LAS u32x2*)(t + 128 * (q >> 1) + 8 * c + 4 * (q & 1)) = u32x2{pkbf(k0, acc[0][1]), pkbf(acc[0][2], acc[0][3])};
  *(LAS unsigned*)(t + 256 + 64 * (q >> 1) + 2 * (q & 1) + 4 * c) = pkbf(acc[1][0], acc[1][1]);
  const unsigned p1 = pkbf(acc[1][2], acc[1][3]);
  const int i1 = 384 + 32 * (q >> 1) + (q & 1) + 2 * c;
  t[i1] = (u16)p1; t[i1 + 64] = (u16)(p1 >> 16);
  const unsigned r0 = pkbf(acc[2][0], acc[2][1]), r1 = pkbf(acc[2][2], acc[2][3]);
  if (!(q & 1)) { const int i2 = 512 + 8 * q + c; t[i2] = (u16)r0; t[i2 + 32] = (u16)(r0 >> 16); t[i2 + 64] = (u16)r1; t[i2 + 96] = (u16)(r1 >> 16); }
  else if (lane == 63) t[640] = (u16)r0;
}
DI unsigned dup16(unsigned a) { return a | (a << 16); }
DI unsigned bsel(unsigned m, unsigned a, unsigned b) { asm("" : "+v"(m)); return (a & m) | (b & ~m); }
DI unsigned rot16(unsigned a) { return __builtin_amdgcn_alignbit(a, a, 16); }
DI void kexpand(lptr kr, lptr kl, int lane) {
  LAS u16* t = (LAS u16*)kl;
  auto put = [&](int ch, unsigned dp, unsigned d0, unsigned d1, unsigned d2, unsigned d3) {
    st16(kr + 16 * ch, u32x4{d0, d1, d2, d3}); st16(kr + KC1 - 4 + 16 * ch, u32x4{dp, d0, d1, d2}); };
  const int n = 63 - lane, n0 = n & 31;
  const unsigned a0 = t[576 + n], b0 = t[577 + n], a1 = t[512 + n], b1 = t[513 + n];
  const unsigned ab = *(LAS unsigned*)(t + 384 + 2 * n), c2 = t[386 + 2 * n];
  const u32x2 v1 = *(LAS u32x2*)(t + 256 + 4 * n0); const unsigned q4 = t[260 + 4 * n0];
  const u32x4 v0 = ld16((lptr)(t + 8 * n0)); const unsigned w = *(LAS unsigned*)(t + 8 * n0 + 8);
  const unsigned mo = (n & 1) ? ~0u : 0u, lo = lane >= 32 ? ~0u : 0u, m31 = n == 31 ? ~0u : 0u;
  put(lane, dup16(b0), dup16(b0), bsel(mo, dup16(b0), b0 | (a0 << 16)), dup16(a0), dup16(a0));
  put(64 + lane, dup16(b1), dup16(b1), bsel(mo, dup16(b1), b1 | (a1 << 16)), dup16(a1), dup16(a1));
  put(128 + lane, dup16(c2), dup16(c2), dup16(ab >> 16), rot16(ab), dup16(ab & 0xffffu));
  put(192 + lane, bsel(lo, bsel(m31, dup16(w & 0xffffu), rot16(w)), dup16(q4)), bsel(lo, rot16(v0[3]), q4 | (v1[1] & 0xffff0000u)),
      bsel(lo, rot16(v0[2]), dup16(v1[1] & 0xffffu)), bsel(lo, rot16(v0[1]), (v1[1] & 0xffffu) | (v1[0] & 0xffff0000u)), bsel(lo, rot16(v0[0]), dup16(v1[0] & 0xffffu)));
  if (lane >= 60) put(319 - lane, lane == 63 ? rot16(v0[0]) : 0u, 0u, 0u, 0u, 0u);
}

struct ConvPlan { int n; int item[36][3]; };
constexpr ConvPlan conv_plan(int tmask, int maxt) {
  ConvPlan p{};
  int fifo[64] = {}, head = 0, tail = 0, carry[16] = {}, nc = 0, per = (tmask == 0xff) ? 1 : 2;
  for (int pass = 0; pass < 2; ++pass) {
    head = tail = 0;
    for (int i = 0; i < nc; ++i) fifo[tail++] = carry[i] + 32;
    int e = 0;
    for (int m = maxt; m >= 0; --m) {
      for (int j = 0; j + m <= maxt; ++j) {
        const bool on = (tmask >> (m + j)) & 1;
        int k = 0;
        if (m == 0) { if (on && j < maxt - 1) p.item[e][k++] = j; else fifo[tail++] = j; }
        if (j + m == maxt) { fifo[tail++] = 8 + 2 * m; fifo[tail++] = 9 + 2 * m; }
        if (on) { while (k < per && head < tail) p.item[e][k++] = fifo[head++]; while (k < 3) p.item[e][k++] = -1; ++e; }
      }
    }
    p.n = e; nc = 0;
    while (head < tail) carry[nc++] = fifo[head++];
  }
  return p;
}
template <int TMASK>
DI void conv_units(lptr kr, lptr ub, int lane, int rho0, int nrho, f32x4 (&acc)[8]) {
  constexpr int MAXT = (TMASK & 0x80) ? 7 : 5;
  constexpr ConvPlan plan = conv_plan(TMASK, MAXT);
  const int r = lane & 15, q = lane >> 4;
  lptr pk = kr + ((r & 1) ? 0 : KC1) + 4092 - 4 * r + 16 * q - 3584 - 64 * rho0 - 64;
  lptr pu = ub + ((r & 1) ? 0 : UC1) + 64 * (r >> 1) + 16 * q + 64 * (7 - rho0) - 64;
  lptr pk2 = pk + 8;
  u32x2 Kl[8], Kh[8]; u32x4 Uf[8];
  asm volatile("" : "+v"(pk), "+v"(pk2), "+v"(pu));
#pragma unroll
  for (int j = 0; j <= MAXT; ++j) { const int m = MAXT - j; Uf[j] = ld16(pu + 64 + 512 * j); Kl[m] = *(LAS u32x2*)(pk + 64 + 512 * (7 - m)); Kh[m] = *(LAS u32x2*)(pk2 + 64 + 512 * (7 - m)); SBAR(); }
#pragma unroll 1
  for (int it = 0; it < nrho; ++it) {
    int e = 0;
#pragma unroll
    for (int m = MAXT; m >= 0; --m) {
#pragma unroll
      for (int j = 0; j + m <= MAXT; ++j) {
        if ((TMASK >> (m + j)) & 1) {
          acc[m + j] = MFMA_BF(Uf[j], (u32x4{Kl[m][0], Kl[m][1], Kh[m][0], Kh[m][1]}), acc[m + j]);
#pragma unroll
          for (int k = 0; k < 3; ++k) {
            const int c = plan.item[e][k];
            if (c >= 0) {
              const int o = (c & 32) ? 64 : 0, cc = c & 31, mm = (cc - 8) >> 1;
              if (cc < 8) Uf[cc] = ld16(pu + o + 512 * cc);
              else if (cc & 1) Kh[mm] = *(LAS u32x2*)(pk2 + o + 512 * (7 - mm));
              else Kl[mm] = *(LAS u32x2*)(pk + o + 512 * (7 - mm));
            }
          }
          SBAR();
          ++e;
        }
      }
    }
    pk -= 64; pk2 -= 64; pu -= 64;
    asm volatile("" : "+v"(pk), "+v"(pk2), "+v"(pu));
  }
}

template <int N> struct IC { static constexpr int value = N; };
DI u64 uni64(u64 v) { return (u64)(unsigned)__builtin_amdgcn_readfirstlane((unsigned)v) | ((u64)(unsigned)__builtin_amdgcn_readfirstlane((unsigned)(v >> 32)) << 32); }
DI int fresh_lane() { int l; asm volatile("v_mbcnt_lo_u32_b32 %0, -1, 0\n\tv_mbcnt_hi_u32_b32 %0, -1, %0" : "=v"(l)); return l; }
#define ARR() do {} while (0)
#define BAR() do { SBAR(); ARR(); asm volatile("s_barrier" ::: "memory"); SBAR(); } while (0)
#define BAR_L() do { SBAR(); asm volatile("s_waitcnt lgkmcnt(0)" ::: "memory"); SBAR(); ARR(); asm volatile("s_barrier" ::: "memory"); SBAR(); } while (0)
#define LD_SC1(off) __builtin_amdgcn_raw_buffer_load_b128(rs, (off), 0, SC1)
#define ST_SC1(v, off) __builtin_amdgcn_raw_buffer_store_b128((v), rs, (off), 0, SC1)

__global__ __launch_bounds__(512, 2) void fused_kernel(const u16* __restrict__ x, const u16* __restrict__ p_lar, const u16* __restrict__ p_aim,
                                                       const u16* __restrict__ p_cr, const u16* __restrict__ p_ci, const u16* __restrict__ p_ldt,
                                                       const u16* __restrict__ p_dsk, const u16* __restrict__ wg, const u16* __restrict__ wu,
                                                       const u16* __restrict__ wd, u16* __restrict__ out, char* ws) {
  extern __shared__ __attribute__((aligned(16))) char smem[];
  const int tid = __builtin_amdgcn_workitem_id_x(), bid = __builtin_amdgcn_workgroup_id_x();
  unsigned cp0_, cp1_;
  { u64 pc_; asm volatile("s_getpc_b64 %0" : "=s"(pc_));
    asm volatile("global_load_dword %0, %2, %3\n\tglobal_load_dword %1, %2, %3 offset:64" : "=&v"(cp0_), "=&v"(cp1_) : "v"((unsigned)(tid < 320 ? tid * 128 : 0)), "s"(pc_ & ~127ull) : "memory"); }
  asm volatile("" :: "s"(x), "s"(p_lar), "s"(p_aim), "s"(p_cr), "s"(p_ci), "s"(p_ldt), "s"(p_dsk), "s"(wg), "s"(wu), "s"(wd), "s"(out), "s"(ws));
  lptr lds = (lptr)smem;
  const unsigned lds0 = (unsigned)(uintptr_t)smem;
  const unsigned fb = lds0 + L_FLG;
  const int wid = __builtin_amdgcn_readfirstlane(tid >> 6), lane = tid & 63;
  const int xcd = bid & 7, bi = bid >> 3;
  const int rb = (xcd >> 1) * 4 + (bi >> 3), cb = (xcd & 1) * 8 + (bi & 7);
  const int tg = (xcd >> 1) * 8 + (bi >> 2), nb = (xcd & 1) * 4 + (bi & 3);
  const int cw = xcd * 32 + bi, odd = cw & 1, c0 = 5 * (cw >> 1) + 2 * odd;
  const int chA = c0 + odd, chC = odd ? c0 : c0 + 2;
  const int rot = (EXP & 2) ? 0 : rb >= 10 ? rb - 10 : rb;
  const __amdgpu_buffer_rsrc_t rs = __builtin_amdgcn_make_buffer_rsrc(ws, 0, (int)WS_END, 0x00020000);
  const int r = lane & 15, q = lane >> 4;

  u64 tag = 0;
  const int lw = wid - 4, kw = wid - 6;
  unsigned vx[4], vw[12];
  auto issue = [&](int s, int sbase, int h, int P0 = 0, int P1 = 5) {
    int ks = s + rot; if (ks >= 10) ks -= 10;
    const unsigned base = lds0 + sbase;
    const char* sx = (const char*)x + (size_t)(rb * 128 + h * 64) * 1280 - 4096 + ks * 128;
    const char* sw = (const char*)(h ? wu : wg) + cb * 192 - 4096 + (size_t)ks * 64 * 3072;
    if (P0 <= 0 && 0 < P1) glds4(sx, vx[0], vx[1], vx[2], vx[3], base + h * 8192);
    if (P0 <= 1 && 1 < P1) glds4(sw, vw[0], vw[1], vw[2], vw[3], base + 16384 + h * 12288);
    if (P0 <= 2 && 2 < P1) glds4(sx + 32 * 1280, vx[0], vx[1], vx[2], vx[3], base + h * 8192 + 4096);
    if (P0 <= 3 && 3 < P1) glds4(sw, vw[4], vw[5], vw[6], vw[7], base + 16384 + h * 12288 + 4096);
    if (P0 <= 4 && 4 < P1) glds4(sw, vw[8], vw[9], vw[10], vw[11], base + 16384 + h * 12288 + 8192);
  };
  auto load_params = [&](unsigned (&pr)[4], int ch, bool tail) {
    const unsigned o = (ch * NM + (tail ? 128 + (lane & ~1) : 2 * lane)) * 2;
    asm volatile("global_load_dword %0, %4, %5\n\tglobal_load_dword %1, %4, %6\n\tglobal_load_dword %2, %4, %7\n\tglobal_load_dword %3, %4, %8"
                 : "=&v"(pr[0]), "=&v"(pr[1]), "=&v"(pr[2]), "=&v"(pr[3]) : "v"(o), "s"(p_lar), "s"(p_aim), "s"(p_cr), "s"(p_ci) : "memory");
  };
  auto load_ldt = [&](int ca, int cb_) { unsigned v; asm volatile("global_load_ushort %0, %1, %2" : "=&v"(v) : "v"((lane < 32 ? ca : cb_) * 2), "s"(p_ldt) : "memory"); return v; };
#define PR_WAIT(p) asm volatile("s_waitcnt vmcnt(0)" : "+v"(p[0]), "+v"(p[1]), "+v"(p[2]), "+v"(p[3]) :: "memory")

  if (wid >= 6) {
    const int lnb = (xcd >> 1) * 64 + (xcd & 1) * 32 + bi;
    u32x4 xv[5];
#pragma unroll
    for (int j = 0; j < 5; ++j) xv[j] = *(const u32x4*)(x + (size_t)(lnb * 8 + kw * 4 + q) * DM + (r + 16 * j) * 8);
    const u64 ep = __hip_atomic_load((GAS u64*)(ws + WS_EPOCH), RLX_AGENT);
    const int un = umin(cw * 2 + kw, 383), ukb = un >> 3, unb = un & 7;
    unsigned rv[5];
    {
      const int c3 = (ukb * 43) >> 7, kt = ukb - 3 * c3;
      const int t0 = kt == 0 ? 0 : kt == 1 ? 80 : 32, t1 = kt == 0 ? 0 : kt == 1 ? 48 : 32;
#pragma unroll
      for (int j = 0; j < 5; ++j) { const int c = j * 64 + lane, row = (c * 6554) >> 16, pos = c - row * 10; rv[j] = (c3 * 96 + row + (row < 16 ? t0 : t1)) * 1280 + unb * 160 + pos * 16; }
    }
    const float dsk = ubf(p_dsk, chA + kw);
    if (kw == 0 && lane < 16) *(LAS unsigned*)(lds + L_FLG + lane * 4) = 0;
    u32x2 stat;
    {
      float v[40]; float s0 = 0.f, s1 = 0.f;
#pragma unroll
      for (int j = 0; j < 20; ++j) { const unsigned w = xv[j >> 2][j & 3]; v[2 * j] = bflo(w); v[2 * j + 1] = bfhi(w); s0 += v[2 * j]; s1 += v[2 * j + 1]; }
      const float mean = row16_sum(s0 + s1) * (1.f / DM);
      tag = uni64(ep) + 1;
      SBAR();
#pragma unroll
      for (int j = 0; j < 5; ++j) glds1(wd, rv[j], lds0 + L_RTB + kw * 5120 + j * 1024);
      SBAR();
      float q0 = 0.f, q1 = 0.f;
#pragma unroll
      for (int j = 0; j < 20; ++j) { v[2 * j] -= mean; v[2 * j + 1] -= mean; q0 = __builtin_fmaf(v[2 * j], v[2 * j], q0); q1 = __builtin_fmaf(v[2 * j + 1], v[2 * j + 1], q1); }
      const float var = rbf(row16_sum(q0 + q1) * (1.f / DM));
      const float mb = rbf(mean), sd = rbf(__builtin_amdgcn_sqrtf(rbf(var + 1e-5f)));
      const float rsd = __builtin_amdgcn_rcpf(sd), off = (mean - mb) * rsd;
#pragma unroll
      for (int j = 0; j < 20; ++j) xv[j >> 2][j & 3] = pkbf(__builtin_fmaf(v[2 * j], rsd, off), __builtin_fmaf(v[2 * j + 1], rsd, off));
      stat = u32x2{asu(mb), asu(sd)};
    }
#pragma unroll
    for (int j = 0; j < 5; ++j) st16(lds + L_LNB + (kw * 4 + q) * LNR + (r + 16 * j) * 16, xv[j]);
    *(volatile LAS u64*)(lds + L_FLG + FL_TAG * 4) = tag;
    waitv<0>();
    STAMP(0);
    BAR_L();
    u32x4 wt[5], wf[12];
#pragma unroll
    for (int j = 0; j < 5; ++j) xv[j] = tr2(lds + L_LNB + (r >> 2) * LNR + (kw * 5 + j) * 128 + q * 32 + (lane & 3) * 8, 4 * LNR);
#pragma unroll
    for (int j = 0; j < 5; ++j) wt[j] = tr2(lds + L_RTB + kw * 5120 + (4 * q + (r >> 2)) * 160 + j * 32 + (lane & 3) * 8, 2560);
    flag_add(fb, FL_S2, lane);
    {
      const int sg = (q & 1) * 32;
      lptr pa = lds + SL0 + 16384 + kw * 12288 + (8 * q + (r >> 2)) * 192 + 8 * (lane & 3);
#pragma unroll
      for (int t = 0; t < 12; ++t) wf[t] = tr2(pa + (t / 6) * 6144 + (t % 6) * 32 + ((t & 1) ? -sg : sg), 768);
    }
    lptr pool = lds + SA + kw * PCH;
    KPtr kp; kp.init(pool, lane);
    u32x4 kv[2][6], kq[4];
    kp.fetch(kv[0], 0); kp.fetch(kv[1], 64);
    BAR_L();
    unsigned pf_sink = 0;
    f32x4 ko[3] = {{0, 0, 0, 0}, {0, 0, 0, 0}, {0, 0, 0, 0}}, ka[3] = {{0, 0, 0, 0}, {0, 0, 0, 0}, {0, 0, 0, 0}};
    u32x4 f0 = {0, 0, 0, 0}, f1 = {0, 0, 0, 0};
#pragma unroll
    for (int sl = 0; sl < 8; ++sl) {
      if (sl == 3 && kw == 0 && lane < 8) __hip_atomic_store((GAS u64*)(ws + WS_F0 + lane * F0REP + cw * 8), tag, RLX_AGENT);
#pragma unroll
      for (int hs = 3 * sl; hs < 3 * sl + 3; ++hs) {
        const int k = hs >> 1;
        if (hs == 16) kscale(ko, ka, *(LAS float*)(lds + L_SC + kw * 4));
        if (!(hs & 1)) kprodh<0>(kv[k & 1], kq);
        else { kprodh<1>(kv[k & 1], kq); if (k + 2 < 12) kp.fetch(kv[k & 1], 64 * (k + 2)); kmma1(kq, ka); }
      }
      if (sl == 0) {
#pragma unroll
        for (int j = 0; j < 5; ++j) ST_SC1(xv[j], WS_XB + lnb * 10240 + (kw * 5 + j) * 1024 + lane * 16);
        if (r == 0) __builtin_amdgcn_raw_buffer_store_b64(stat, rs, WS_ST + (lnb * 8 + kw * 4 + q) * 8, 0, SC1);
      }
      if (sl == 1) {
        if (rb < 10) {
          const u32x4 ones = {0x3f803f80u, 0x3f803f80u, 0x3f803f80u, 0x3f803f80u};
          f32x4 cs[6];
          SBAR(); __builtin_amdgcn_s_setprio(3); SBAR();
#pragma unroll
          for (int t = 0; t < 6; ++t) cs[t] = MFMA_BF(ones, wf[t], (f32x4{0, 0, 0, 0}));
#pragma unroll
          for (int t = 0; t < 6; ++t) cs[t] = MFMA_BF(ones, wf[6 + t], cs[t]);
          SBAR(); __builtin_amdgcn_s_setprio(0); SBAR();
          const float cs0 = q == 0 ? cs[0][0] : q == 1 ? cs[1][0] : q == 2 ? cs[2][0] : cs[3][0], cs1 = q == 0 ? cs[4][0] : cs[5][0];
          const unsigned co = WS_CS + (cb * 10 + rb) * 768 + kw * 384 + lane * 4;
          __builtin_amdgcn_raw_buffer_store_b32(asu(cs0), rs, co, 0, SC1);
          if (lane < 32) __builtin_amdgcn_raw_buffer_store_b32(asu(cs1), rs, co + 256, 0, SC1);
        }
        if (cw < 192) {
#pragma unroll
          for (int f = 0; f < 5; ++f) ST_SC1(wt[f], WS_WD + (unb * 48 + ukb) * 5120 + f * 1024 + lane * 16);
        }
      }
      if (sl == 2) waitv<0>();
      if (sl == 5 && kw == 0) { f0 = LD_SC1(WS_F0 + xcd * F0REP + lane * 16); f1 = LD_SC1(WS_F0 + xcd * F0REP + 1024 + lane * 16); }
      if (sl == 6 && kw == 0) {
        const unsigned tl = (unsigned)tag, th = (unsigned)(tag >> 32);
        _Pragma("nounroll") for (int sp = 0; sp < SPIN_GLB; ++sp) {
          if (wall(((f0[0] ^ tl) | (f0[1] ^ th) | (f0[2] ^ tl) | (f0[3] ^ th) | (f1[0] ^ tl) | (f1[1] ^ th) | (f1[2] ^ tl) | (f1[3] ^ th)) == 0u)) break;
          f0 = LD_SC1(WS_F0 + xcd * F0REP + lane * 16); f1 = LD_SC1(WS_F0 + xcd * F0REP + 1024 + lane * 16);
          __builtin_amdgcn_s_sleep(2);
        }
      }
      if (sl >= 6) {
        const unsigned po = WS_WD + nb * 245760 + (bi >> 2) * 30720 + umin(((sl - 6) * 2 + kw) * 64 + lane, 239u) * 128;
        asm volatile("global_load_dword %0, %1, %2 sc1" : "+v"(pf_sink) : "v"(po), "s"(ws) : "memory");
      }
      BAR();
    }
    STAMP(2);
    kscale(ko, ka, *(LAS float*)(lds + L_SC + 12 + kw * 4));
    kstore(pool + O_KL, lane, ko, dsk);
    kexpand(pool + O_KREV, pool + O_KL, lane);
    flag_set(fb, FL_KA + kw, 1);
    asm volatile("s_waitcnt vmcnt(0)" : "+v"(pf_sink) :: "memory");
    STAMP(3);
  } else {
    if (wid >= 4) {
      {
        if (lw) {
          unsigned pr[4];
          load_params(pr, chC, true);
          unsigned lv = load_ldt(chC, chC);
          PR_WAIT(pr); asm volatile("" : "+v"(lv));
          const unsigned sh = (lane & 1) ? 0u : 16u, tb[1] = {lds0 + SL2 - SA + 512};
          const float pv[1][4] = {{bfhi(pr[0] << sh), bfhi(pr[1] << sh), bfhi(pr[2] << sh), bfhi(pr[3] << sh)}}, ldt[1] = {asf(lv << 16)};
          float sc[1];
          kpool<1>(tb, pv, ldt, false, sc);
          if (lane == 0) *(LAS float*)(lds + L_SC + 20) = sc[0];
        }
#pragma unroll
        for (int j = 0; j < 4; ++j) { const int row = 8 * j + (lane >> 3); vx[j] = row * 1280 + (((lane & 7) ^ ((row >> 1) & 7)) << 4) + 4096 - 1024 * j; }
#pragma unroll
        for (int j = 0; j < 3; ++j) { const int c = j * 64 + lane, row = (c * 43691) >> 19, pos = c - row * 12;
          const unsigned v = row * 3072 + ((pos ^ (((row >> 3) & 1) << 1)) << 4) + 4096;
#pragma unroll
          for (int g = 0; g < 4; ++g) vw[3 * g + j] = v + g * 16 * 3072 - 1024 * ((3 * g + j) & 3); }
        if (!lw) { issue(0, SL0, 0); issue(0, SL0, 1); }
        STAMP(0);
        issue(1, SL1, lw);
        STAMP(1);
      }
      waitv<20>();
      BAR_L();
      u32x4 cv[6][6];
      {
        KPtr kp; kp.init(lds + SL2 + lw * 384, lane);
#pragma unroll
        for (int i = 0; i < 6; ++i) kp.fetch(cv[i], 64 * i);
      }
      const float sca = *(LAS float*)(lds + L_SC + 8), scb = *(LAS float*)(lds + L_SC + (lw ? 20 : 8));
      flag_add(fb, FL_S2, lane);
      flag_wait(fb, FL_S2, 4);
      issue(2, SL2, lw, 0, 2);
      f32x4 ka[3] = {{0, 0, 0, 0}, {0, 0, 0, 0}, {0, 0, 0, 0}};
      f32x4 kc[3] = {{0, 0, 0, 0}, {0, 0, 0, 0}, {0, 0, 0, 0}};
#pragma unroll
      for (int s = 0; s < 7; ++s) {
        if (s == 0) waitv<8>(); else waitv<20>();
        BAR();
        if (s == 0) issue(2, SL2, lw, 2, 5);
        issue(s + 3, s % 3 == 0 ? SL0 : s % 3 == 1 ? SL1 : SL2, lw);
        if (s >= 1 && !(EXP & 4096)) { u32x4 o[4]; kprod(cv[s - 1], o); kmma1(o, ka); }
        if (s == 2) kscale(kc, ka, sca);
      }
      kscale(kc, ka, scb);
      if (lw == 0) {
#pragma unroll
        for (int i = 0; i < 3; ++i) *(LAS f32x4*)(lds + L_STG + i * 1024 + lane * 16) = kc[i];
      }
      waitv<20>();
      BAR_L();
      STAMP(2);
      tag = uni64(*(volatile LAS u64*)(lds + L_FLG + FL_TAG * 4));
      u32x4 uc[8]; unsigned uprev;
      const int n0 = lw * 128 + (lane >> 2) * 8;
      const unsigned uo = WS_XB + n0 * 10240 + (c0 + (lane & 3)) * 16;
      u32x4 sv, cp[10];
      if (lw == 0) {
        sv = LD_SC1(WS_ST + rb * 1024 + lane * 16);
#pragma unroll
        for (int j = 0; j < 10; ++j) cp[j] = LD_SC1(WS_CS + (cb * 10 + j) * 768 + (lane < 48 ? lane : 0) * 16);
      }
#pragma unroll
      for (int j = 0; j < 8; ++j) uc[j] = LD_SC1(uo + j * 10240);
      uprev = __builtin_amdgcn_raw_buffer_load_b32(rs, uo - 10240 + 12, 0, SC1);
      SBAR();
      if (lw) {
#pragma unroll
        for (int i = 0; i < 3; ++i) kc[i] += *(LAS f32x4*)(lds + L_STG + i * 1024 + lane * 16);
        kstore(lds + L_PC, lane, kc, ubf(p_dsk, chC));
        waitv<9>();
      } else waitv<20>();
      BAR();
      STAMP(3);
      if (lw == 0) {
        f32x4 cs = {0, 0, 0, 0};
#pragma unroll
        for (int j = 0; j < 10; ++j) cs += __builtin_bit_cast(f32x4, cp[j]);
        st16(lds + L_STAT + lane * 16, sv);
        if (lane < 48) *(LAS f32x4*)(lds + L_CS + lane * 16) = cs;
        flag_set(fb, FL_EPI, 1);
        if (bid == 0 && lane == 0) __hip_atomic_store((GAS u64*)(ws + WS_EPOCH), tag, RLX_AGENT);
      }
      if (n0 == 0) uprev = 0;
      if ((lane & 3) < 3) {
        lptr ud = lds + L_U + (lane & 3) * UCH + 448 + n0 * 16;
#pragma unroll
        for (int j = 0; j < 8; ++j) {
          st16(ud + j * 16, uc[j]);
          const unsigned pw_ = j ? uc[j - 1][3] : uprev;
          st16(ud + UC1 + j * 16, u32x4{__builtin_amdgcn_alignbit(uc[j][0], pw_, 16), __builtin_amdgcn_alignbit(uc[j][1], uc[j][0], 16),
                                        __builtin_amdgcn_alignbit(uc[j][2], uc[j][1], 16), __builtin_amdgcn_alignbit(uc[j][3], uc[j][2], 16)});
        }
      }
      if (lane < 28) {
#pragma unroll
        for (int c = 0; c < 3; ++c) st16(lds + L_U + c * UCH + lw * UC1 + lane * 16, u32x4{0, 0, 0, 0});
      }
      flag_add(fb, FL_U, lane);
      if (lw) { kexpand(lds + L_KC, lds + L_PC, lane); flag_set(fb, FL_KC, 1); }
    } else {
      unsigned pa[4], pb[4];
      const int ch0 = wid == 2 ? chC : wid == 1 ? chA + 1 : chA, ch1 = wid == 3 ? chA + 1 : ch0;
      load_params(pa, ch0, wid == 3); load_params(pb, ch1, wid == 3);
      unsigned lv = load_ldt(ch0, ch1);
      f32x4 acc[2][3][4];
#pragma unroll
      for (int m = 0; m < 2; ++m)
#pragma unroll
        for (int ii = 0; ii < 3; ++ii)
#pragma unroll
          for (int tb = 0; tb < 4; ++tb) { acc[m][ii][tb] = f32x4{0, 0, 0, 0}; asm volatile("" : "+v"(acc[m][ii][tb])); }
      PR_WAIT(pa); PR_WAIT(pb); asm volatile("" : "+v"(lv));
      STAMP(0);
      {
        const float ldt[2] = {asf((unsigned)__builtin_amdgcn_readlane(lv, 0) << 16), asf((unsigned)__builtin_amdgcn_readlane(lv, 32) << 16)};
        const unsigned shp = (lane & 1) ? 0u : 16u, sh0 = wid == 3 ? shp : 16u, sh1 = wid == 3 ? shp : 0u;
        const float pv[2][4] = {{bfhi(pa[0] << sh0), bfhi(pa[1] << sh0), bfhi(pa[2] << sh0), bfhi(pa[3] << sh0)},
                                {bfhi(pb[0] << sh1), bfhi(pb[1] << sh1), bfhi(pb[2] << sh1), bfhi(pb[3] << sh1)}};
        const unsigned t0 = lds0 + (wid == 2 ? SL2 - SA : wid == 1 ? PCH : wid == 3 ? 512 : 0), tb[2] = {t0, t0 + (wid == 3 ? PCH : 256)};
        float sc[2];
        kpool<2>(tb, pv, ldt, wid != 3, sc);
        if (lane == 0) { *(LAS float*)(lds + L_SC + wid * 4) = sc[0]; if (wid == 3) *(LAS float*)(lds + L_SC + 16) = sc[1]; }
      }
      STAMP(1);
      if (!(EXP & 4)) __builtin_amdgcn_s_setprio(2);
      const int tw = wid >> 1, fw = wid & 1;
      lptr pB0 = lds + (tw * 64 + r) * 128 + ((q ^ (r >> 1)) << 4);
      lptr pB1 = lds + (tw * 64 + r) * 128 + (((q ^ (r >> 1)) ^ 4) << 4);
      const int sg = (q & 1) * 32;
      lptr pAb = lds + 16384 + (8 * q + (r >> 2)) * 192 + 8 * (lane & 3);
      lptr pA[3] = {pAb + (fw ? 160 - sg : sg), pAb + (fw ? 128 + sg : 32 - sg), pAb + (fw ? 96 - sg : 64 + sg)};
      u32x4 A0[2][3], B0[4], A1[2][3], B1[4];
      auto rd = [&](u32x4 (&A)[2][3], u32x4 (&B)[4], int off, int kk) {
#pragma unroll
        for (int tb = 0; tb < 4; ++tb) B[tb] = ld16((kk ? pB1 : pB0) + off + tb * 2048);
#pragma unroll
        for (int m = 0; m < 2; ++m)
#pragma unroll
          for (int ii = 0; ii < 3; ++ii) A[m][ii] = tr2(pA[ii] + off + m * 12288 + kk * 6144, 768);
      };
      auto mm = [&](u32x4 (&A)[2][3], u32x4 (&B)[4]) {
#pragma unroll
        for (int e = 0; e < 24; ++e) { const int m = e / 12, ii = (e / 4) % 3, tb = e % 4; acc[m][ii][tb] = MFMA_BF(A[m][ii], B[tb], acc[m][ii][tb]); }
      };
      auto sched = [&]() {
#pragma unroll
        for (int k = 0; k < 16; ++k) { SGB(0x8, 1); SGB(0x100, 1); }
        SGB(0x8, 8);
      };
      BAR_L();
      STAMP(2);
      rd(A0, B0, SL0, 0);
      int s0 = SL0, s1 = SL1, s2 = SL2;
#pragma unroll 1
      for (int s = 0; s < 9; ++s) {
        SBAR();
        rd(A1, B1, s0, 1); mm(A0, B0); sched();
        BAR_L();
        rd(A0, B0, s1, 0); mm(A1, B1); sched();
        SBAR();
        const int t = s0; s0 = s1; s1 = s2; s2 = t;
      }
      rd(A1, B1, SL0, 1); mm(A0, B0); sched();
      SBAR();
      flag_add(fb, FL_G1, lane);
      mm(A1, B1);
      SBAR();
      if (EXP & 64) __builtin_amdgcn_s_setprio(0);
      STAMP(3);
      unsigned bo[4], ao[4];
#pragma unroll
      for (int c = 0; c < 4; ++c) { const int cc = wid * 4 + ((c + nb) & 3); bo[c] = WS_WD + (nb * 48 + cc * 3) * 5120; ao[c] = WS_H + (tg * 16 + cc) * 12288; }
      const unsigned l16 = lane * 16;
#define BFRAG(k, n) __builtin_amdgcn_raw_buffer_load_b128(rs, l16, bo[(k) < 8 ? (k) >> 1 : (k) - 8] + ((k) < 8 ? ((k) & 1) * 5120 : 10240) + (n) * 1024, SC1)
#define AFRAG(k, t) __builtin_amdgcn_raw_buffer_load_b128(rs, l16, ao[(k) < 8 ? (k) >> 1 : (k) - 8] + ((k) < 8 ? ((k) & 1) * 4096 : 8192) + (t) * 1024, (EXP & 8) ? 0 : SC1)
      u32x4 tp[20];
      flag_wait(fb, FL_EPI, 1);
      tag = uni64(*(volatile LAS u64*)(lds + L_FLG + FL_TAG * 4));
      STAMP(4);
      {
        float nmb[4], al[4], sd[4];
#pragma unroll
        for (int tb = 0; tb < 4; ++tb) { const f32x2 st = *(LAS f32x2*)(lds + L_STAT + (tw * 64 + tb * 16 + r) * 8); nmb[tb] = -st.x; sd[tb] = st.y; al[tb] = -1.44269504f * __builtin_amdgcn_rcpf(st.y); }
        const unsigned hoff = WS_H + ((rb * 2 + tw) * 16 + cb) * 12288 + lane * 16;
        auto hcalc = [&](const f32x4& g4, const f32x4& u4, const f32x4& cg, const f32x4& cu, int tb) {
          float hv[4];
#pragma unroll
          for (int j = 0; j < 4; ++j) {
            const float g = __builtin_fmaf(nmb[tb], cg[j], g4[j]) * al[tb];
            const float u = __builtin_fmaf(nmb[tb], cu[j], u4[j]);
            const float e = __builtin_amdgcn_exp2f(g);
            hv[j] = u * __builtin_amdgcn_rcpf(__builtin_fmaf(e, sd[tb], sd[tb]));
          }
          return u32x2{pkbf(hv[0], hv[1]), pkbf(hv[2], hv[3])};
        };
        lptr pcs = lds + L_CS + (fw * 48 + 4 * q) * 4;
        const int oa = fw ? 128 : 0, oc = fw ? 0 : 128;
        {
          const f32x4 cga = *(LAS f32x4*)(pcs + oa), cua = *(LAS f32x4*)(pcs + 384 + oa), cgb = *(LAS f32x4*)(pcs + 64), cub = *(LAS f32x4*)(pcs + 384 + 64);
#pragma unroll
          for (int tb = 0; tb < 4; ++tb) {
            tp[4 * tb] = BFRAG(8 + (4 * tb) / 5, (4 * tb) % 5); tp[4 * tb + 1] = BFRAG(8 + (4 * tb + 1) / 5, (4 * tb + 1) % 5);
            const u32x2 ha = hcalc(acc[0][0][tb], acc[1][0][tb], cga, cua, tb);
            SGB(0x20, 1); SGB(0x6, 18); SGB(0x20, 1); SBAR();
            tp[4 * tb + 2] = BFRAG(8 + (4 * tb + 2) / 5, (4 * tb + 2) % 5); tp[4 * tb + 3] = BFRAG(8 + (4 * tb + 3) / 5, (4 * tb + 3) % 5);
            const u32x2 hb = hcalc(acc[0][1][tb], acc[1][1][tb], cgb, cub, tb);
            SGB(0x20, 1); SGB(0x6, 18); SGB(0x20, 1); SBAR();
            ST_SC1((u32x4{ha[0], ha[1], hb[0], hb[1]}), hoff + fw * 4096 + tb * 1024);
            SBAR();
          }
        }
        {
          const f32x4 cg = *(LAS f32x4*)(pcs + oc), cu = *(LAS f32x4*)(pcs + 384 + oc);
#pragma unroll
          for (int t2 = 0; t2 < 2; ++t2) {
            tp[16 + 2 * t2] = BFRAG(11, 1 + 2 * t2);
            const u32x2 ha = hcalc(acc[0][2][2 * t2], acc[1][2][2 * t2], cg, cu, 2 * t2);
            SBAR();
            tp[17 + 2 * t2] = BFRAG(11, 2 + 2 * t2);
            const u32x2 hb = hcalc(acc[0][2][2 * t2 + 1], acc[1][2][2 * t2 + 1], cg, cu, 2 * t2 + 1);
            SBAR();
            ST_SC1((u32x4{ha[0], ha[1], hb[0], hb[1]}), hoff + 8192 + fw * 2048 + t2 * 1024);
            SBAR();
          }
        }
      }
      if (EXP & 128) __builtin_amdgcn_s_setprio(0);
      STAMP(5);
      lptr park = lds + SL2 + wid * 20480 + lane * 16;
      waitv<6>();
      if (flag_add(fb, FL_H1, lane) == 3u && lane < 16) __hip_atomic_store((GAS u64*)(ws + WS_MB + (rb * 16 + lane) * MBS + cb * 8), tag, RLX_AGENT);
      flag_wait(fb, FL_G1, 4);
#pragma unroll
      for (int j = 0; j < 16; ++j) st16(park + j * 1024, tp[j]);
      SBAR();
      waitv<0>();
      if (flag_add(fb, FL_H2, lane) == 3u && lane < 16) __hip_atomic_store((GAS u64*)(ws + WS_MB + (rb * 16 + lane) * MBS + 128 + cb * 8), tag, RLX_AGENT);
#pragma unroll
      for (int j = 16; j < 20; ++j) st16(park + j * 1024, tp[j]);
      STAMP(6);
      u32x4 Bw[4][5];
#pragma unroll
      for (int k = 0; k < 4; ++k)
#pragma unroll
        for (int n = 0; n < 5; ++n) Bw[k][n] = BFRAG(k, n);
      STAMP(7);
      const unsigned fo = WS_MB + (tg * 8 + nb) * MBS + (wid * 4 + (lane & 3)) * 8;
      _Pragma("nounroll") for (int sp = 0; sp < SPIN_GLB; ++sp) { if (wall(gflag_ok(ws, fo, tag))) break; __builtin_amdgcn_s_sleep(2); }
      asm volatile("" ::: "memory");
      if (EXP & 1024) __builtin_amdgcn_s_sleep(127);
      STAMP(8);
      f32x4 a2[5][4];
#pragma unroll
      for (int n = 0; n < 5; ++n)
#pragma unroll
        for (int tb = 0; tb < 4; ++tb) a2[n][tb] = f32x4{0, 0, 0, 0};
      u32x4 Ah[4][4];
#pragma unroll
      for (int k = 0; k < 3; ++k)
#pragma unroll
        for (int tb = 0; tb < 4; ++tb) Ah[k][tb] = AFRAG(k, tb);
      u64 f2 = 0;
      SBAR();
#pragma unroll
      for (int k = 0; k < 12; ++k) {
        if (k == 3) { f2 = __hip_atomic_load((GAS u64*)(ws + fo + 128), RLX_AGENT); SBAR(); }
        if (k == 5) {
          if (!wall(f2 == tag)) { _Pragma("nounroll") for (int sp = 0; sp < SPIN_GLB; ++sp) { if (wall(gflag_ok(ws, fo + 128, tag))) break; __builtin_amdgcn_s_sleep(2); } }
          asm volatile("" ::: "memory");
        }
        u32x4 hf[4];
        if (k >= 8) {
          const u32x4 X0 = Ah[k & 3][0], X1 = Ah[k & 3][1], Y0 = Ah[k & 3][2], Y1 = Ah[k & 3][3];
          hf[0] = u32x4{X0[0], X0[1], Y0[0], Y0[1]}; hf[1] = u32x4{X0[2], X0[3], Y0[2], Y0[3]};
          hf[2] = u32x4{X1[0], X1[1], Y1[0], Y1[1]}; hf[3] = u32x4{X1[2], X1[3], Y1[2], Y1[3]};
        } else {
#pragma unroll
          for (int tb = 0; tb < 4; ++tb) hf[tb] = Ah[k & 3][tb];
        }
#pragma unroll
        for (int e = 0; e < 20; ++e) {
          const int n = e >> 2, tb = e & 3;
          a2[n][tb] = MFMA_BF(Bw[k & 3][n], hf[tb], a2[n][tb]);
          if (e & 1) {
            const int j = e >> 1;
            if (j < 4) { if (k + 3 < 12) Ah[(k + 3) & 3][j] = AFRAG(k + 3, j); }
            else if (j < 9 && k >= 1 && k + 3 < 12) {
              if (k + 3 < 8) { if (!(EXP & 16)) Bw[(k + 3) & 3][j - 4] = BFRAG(k + 3, j - 4); }
              else Bw[(k + 3) & 3][j - 4] = ld16(park + ((k - 5) * 5 + j - 4) * 1024);
            }
            SBAR();
          }
        }
#pragma unroll
        for (int n = 0; n < 5; ++n) asm volatile("" : "+v"(a2[n][0]), "+v"(a2[n][1]), "+v"(a2[n][2]), "+v"(a2[n][3]));
        if ((EXP & 512) && k < 11) ARR();
      }
      STAMP(9);
      {
        lptr red = lds + SL2 + wid * 20480;
        const int l2 = fresh_lane(), r = l2 & 15, q = l2 >> 4;
#pragma unroll
        for (int n = 0; n < 5; ++n)
#pragma unroll
          for (int tb = 0; tb < 4; ++tb) {
            *(LAS f32x4*)(red + (4 * n + q) * 1024 + ((tb * 16 + r + 3 * n) & 63) * 16) = a2[n][tb];
          }
      }
      __builtin_amdgcn_s_setprio(0);
    }
  }

  if (wid >= 4 && !(EXP & 1)) {
    const int sw = wid - 4;
    const int lane = fresh_lane(), r = lane & 15, q = lane >> 4;
    if (EXP & 256) __builtin_amdgcn_s_setprio(3);
    const int hm = sw & 1;
    lptr am = lds + SA + hm * PCH;
    f32x4 ca[8];
#pragma unroll
    for (int t = 0; t < 8; ++t) ca[t] = f32x4{0, 0, 0, 0};
    lptr pmain = am + O_KL + 1312 + lane * 16;
    STAMP(4);
    if (sw < 2) flag_wait(fb, FL_KA + hm, 1);
    flag_wait(fb, FL_U, 2);
    STAMP(5);
    conv_units<0xff>(am + O_KREV, lds + L_U + (hm + odd) * UCH, lane, sw < 2 ? 5 : 0, sw < 2 ? 3 : 5, ca);
    if (sw < 2) {
      lptr pc = lds + L_PC;
#pragma unroll
      for (int t = 0; t < 8; ++t) { *(LAS f32x4*)(pmain + t * 1024) = ca[t]; ca[t] = f32x4{0, 0, 0, 0}; }
      flag_set(fb, FL_PA + hm, 1);
      flag_wait(fb, FL_KC, 1);
      lptr uc_ = lds + L_U + (odd ? 0 : 2) * UCH;
      if (odd) conv_units<0x3a>(lds + L_KC, uc_, lane, hm * 4, 4, ca);
      else conv_units<0xc5>(lds + L_KC, uc_, lane, hm * 4, 4, ca);
      __builtin_amdgcn_s_setprio(3);
      STAMP(9);
      f32x4 c4[4];
      c4[0] = odd ? ca[1] : ca[0]; c4[1] = odd ? ca[3] : ca[2]; c4[2] = odd ? ca[4] : ca[6]; c4[3] = odd ? ca[5] : ca[7];
      if (!hm) {
#pragma unroll
        for (int t = 0; t < 4; ++t) *(LAS f32x4*)(pc + t * 1024 + lane * 16) = c4[t];
        flag_set(fb, FL_PC, 1);
      } else {
        flag_wait(fb, FL_PC, 1);
#pragma unroll
        for (int t = 0; t < 4; ++t) c4[t] += *(LAS f32x4*)(pc + t * 1024 + lane * 16);
        waitl();
#pragma unroll
        for (int t = 0; t < 4; ++t) { *(LAS unsigned*)(pc + t * 512 + q * 128 + r * 4) = pkbf(c4[t][0], c4[t][1]); *(LAS unsigned*)(pc + t * 512 + q * 128 + r * 4 + 64) = pkbf(c4[t][2], c4[t][3]); }
#pragma unroll
        for (int i = 0; i < 2; ++i) {
          const int s4 = 2 * i + (lane >> 5);
          const int T = odd ? (s4 == 0 ? 1 : s4 + 2) : (s4 < 2 ? 2 * s4 : s4 + 4);
          ST_SC1(ld16(pc + i * 1024 + lane * 16), WS_Y + ((T * 4 + ((lane >> 3) & 3)) * DM + chC) * 128 + (lane & 7) * 16);
        }
      }
    } else {
      __builtin_amdgcn_s_setprio(3);
      STAMP(9);
      flag_wait(fb, FL_PA + hm, 1);
#pragma unroll
      for (int t = 0; t < 8; ++t) ca[t] += *(LAS f32x4*)(pmain + t * 1024);
#pragma unroll
      for (int t = 0; t < 8; ++t) { *(LAS unsigned*)(am + t * 512 + q * 128 + r * 4) = pkbf(ca[t][0], ca[t][1]); *(LAS unsigned*)(am + t * 512 + q * 128 + r * 4 + 64) = pkbf(ca[t][2], ca[t][3]); }
#pragma unroll
      for (int i = 0; i < 4; ++i) ST_SC1(ld16(am + i * 1024 + lane * 16), WS_Y + ((i * 8 + (lane >> 3)) * DM + chA + hm) * 128 + (lane & 7) * 16);
    }
    STAMP(6);
    waitv<0>();
    if (flag_add(fb, FL_YC, lane) == 3u) {
      if (lane < 32) __hip_atomic_store((GAS u64*)(ws + WS_MB + (lane * 8 + xcd) * MBS + 256 + bi * 8), tag, RLX_AGENT);
      _Pragma("nounroll") for (int sp = 0; sp < SPIN_GLB; ++sp) { if (wall(gflag_ok(ws, WS_MB + (tg * 8 + nb) * MBS + 256 + (lane & 31) * 8, tag))) break; __builtin_amdgcn_s_sleep(4); }
      flag_set(fb, FL_YOK, 1);
    }
    STAMP(7);
    flag_wait(fb, FL_YOK, 1);
    STAMP(8);
    u32x4 yv[3];
#pragma unroll
    for (int j = 0; j < 3; ++j) {
      const int p = umin(sw + 4 * j, 9), c = p * 64 + lane, row = (c * 6554) >> 16;
      glds1((const char*)x + (size_t)tg * 64 * 1280 + nb * 160, row * 1280 + (c - row * 10) * 16, lds0 + L_XT + p * 1024);
    }
#pragma unroll
    for (int j = 0; j < 3; ++j) { const int p = umin(sw + 4 * j, 9); yv[j] = LD_SC1(WS_Y + (tg * DM + nb * 80 + p * 8) * 128 + lane * 16); }
    lptr yb = lds + L_YB + sw * 3072;
#pragma unroll
    for (int j = 0; j < 3; ++j) st16(yb + j * 1024 + lane * 16, yv[j]);
#pragma unroll
    for (int j = 0; j < 3; ++j) {
      const u32x4 a = tr2(yb + j * 1024 + (r >> 2) * 128 + q * 32 + (lane & 3) * 8, 512);
      if (sw + 4 * j < 10) st16(lds + L_BASE + lane * 160 + (sw + 4 * j) * 16, a);
    }
  }
  asm volatile("" :: "v"(cp0_), "v"(cp1_));
  STAMP(10);
  BAR_L();
  STAMP(11);
  if (wid < 5) {
    const int tid = wid * 64 + fresh_lane();
    const int tok = (tid * 13108) >> 16, g = tid - tok * 5;
    const int o = g * 4096 + ((tok + 3 * g) & 63) * 16;
    f32x4 s[4];
#pragma unroll
    for (int m = 0; m < 4; ++m)
      s[m] = (*(LAS f32x4*)(lds + SL0 + o + m * 1024) + *(LAS f32x4*)(lds + SL0 + 20480 + o + m * 1024)) +
             (*(LAS f32x4*)(lds + SL2 + o + m * 1024) + *(LAS f32x4*)(lds + SL2 + 20480 + o + m * 1024));
    u16* op = out + (size_t)(tg * 64 + tok) * DM + nb * 80 + g * 16;
#pragma unroll
    for (int hh = 0; hh < 2; ++hh) {
      const u32x4 bs = ld16(lds + L_BASE + tid * 32 + hh * 16), xs = ld16(lds + L_XT + tid * 32 + hh * 16);
      const f32x4 sa = s[2 * hh], sb = s[2 * hh + 1];
      *(u32x4*)(op + hh * 8) = u32x4{pkbf((bflo(xs[0]) + bflo(bs[0])) + sa[0], (bfhi(xs[0]) + bfhi(bs[0])) + sa[1]), pkbf((bflo(xs[1]) + bflo(bs[1])) + sa[2], (bfhi(xs[1]) + bfhi(bs[1])) + sa[3]),
                                     pkbf((bflo(xs[2]) + bflo(bs[2])) + sb[0], (bfhi(xs[2]) + bfhi(bs[2])) + sb[1]), pkbf((bflo(xs[3]) + bflo(bs[3])) + sb[2], (bfhi(xs[3]) + bfhi(bs[3])) + sb[3])};
    }
  }
}

#if defined(__HIP_DEVICE_COMPILE__)
#pragma clang attribute pop
#endif

extern "C" void kernel_launch(void* const* d_in, const int* in_sizes, int n_in, void* d_out, int out_size, void* d_ws, size_t ws_size, hipStream_t stream) {
  (void)in_sizes; (void)n_in; (void)out_size; (void)ws_size;
  (void)hipFuncSetAttribute((const void*)fused_kernel, hipFuncAttributeMaxDynamicSharedMemorySize, LDS_BYTES);
  fused_kernel<<<256, 512, LDS_BYTES, stream>>>((const u16*)d_in[0], (const u16*)d_in[1], (const u16*)d_in[2], (const u16*)d_in[3], (const u16*)d_in[4],
                                                (const u16*)d_in[5], (const u16*)d_in[6], (const u16*)d_in[8], (const u16*)d_in[9], (const u16*)d_in[10],
                                                (u16*)d_out, (char*)d_ws);
}
```

```cpp
#include <hip/hip_runtime.h>
#include <stdint.h>

#if defined(__HIP_DEVICE_COMPILE__)
#pragma clang attribute push(__attribute__((target("no-packed-fp32-ops"))), apply_to = function)
#endif
typedef unsigned short u16;
typedef unsigned long long u64;
typedef short bf16x8 __attribute__((ext_vector_type(8)));
typedef _Float16 f16x8 __attribute__((ext_vector_type(8)));
typedef short s16x4 __attribute__((ext_vector_type(4)));
typedef float f32x2 __attribute__((ext_vector_type(2)));
typedef float f32x4 __attribute__((ext_vector_type(4)));
typedef unsigned u32x2 __attribute__((ext_vector_type(2)));
typedef unsigned u32x4 __attribute__((ext_vector_type(4)));
typedef _Float16 h16x2 __attribute__((ext_vector_type(2)));
typedef __bf16 b16x2 __attribute__((ext_vector_type(2)));
#define LAS __attribute__((address_space(3)))
#define GAS __attribute__((address_space(1)))
typedef LAS char* lptr;
#define DI __device__ __forceinline__
#define SBAR() __builtin_amdgcn_sched_barrier(0)
#define SGB(m, n) __builtin_amdgcn_sched_group_barrier(m, n, 0)
#define MFMA_BF(a, b, c) __builtin_amdgcn_mfma_f32_16x16x32_bf16(__builtin_bit_cast(bf16x8, a), __builtin_bit_cast(bf16x8, b), c, 0, 0, 0)
#define MFMA_H(a, b, c) __builtin_amdgcn_mfma_f32_16x16x32_f16(__builtin_bit_cast(f16x8, a), __builtin_bit_cast(f16x8, b), c, 0, 0, 0)
#define SC1 16
#define EXP 0

constexpr int DM = 640, NM = 192;
constexpr unsigned WS_EPOCH = 0, WS_F0 = 4096, F0REP = 4352;
constexpr unsigned WS_MB = 262144, MBS = 1280;
constexpr unsigned WS_ST = 65536, WS_CS = 98304, WS_XB = 655360, WS_WD = WS_XB + 2621440 + 4096;
constexpr unsigned WS_H = WS_WD + 1966080, WS_Y = WS_H + 6291456, WS_END = WS_Y + 2621440;
constexpr unsigned WS_STAMP = 16u << 20;
constexpr int STAGE = 40960, SL1 = 0, SA = 40960, SL2 = 81920, SL0 = 122880, POOL_BIAS = SA;
constexpr int PROW = 784, PCH = 23 * PROW;
constexpr int KC1 = 4228, O_KREV = 128, O_KL = 8528;
constexpr int L_FLG = SA + 2 * PCH, L_SC = L_FLG + 128, L_STG = L_FLG + 192, STGW = 2304;
constexpr int L_STAT = L_STG, L_CS = L_STG + 1024;
constexpr int L_RTB = SL2 + 29696;
constexpr int L_LNT = L_STG;
constexpr int L_LNB = SL2 + 18432, LNR = 1344;
constexpr int UCH = 9232, UC1 = 4640;
constexpr int L_U = SL1 + 128, L_KC = L_U + 3 * UCH + 128, L_PC = L_KC + 8400;
constexpr int L_XT = SL1;
constexpr int L_BASE = SA, L_YB = SA + 10240;
constexpr int LDS_BYTES = 163840;
static_assert(O_KL + 1312 + 8192 <= PCH && L_LNB + 8 * LNR <= L_RTB && L_RTB + 10240 + 128 <= SL0 && L_PC + 4096 <= SA && L_STG + 2 * STGW <= SL0 && (L_KC & 15) == 0 && (L_PC & 15) == 0 && (L_STG & 15) == 0, "lds map");
enum { FL_F0 = 0, FL_EPI, FL_U, FL_KA, FL_KB, FL_KC, FL_KP, FL_PA, FL_PB, FL_PC, FL_YC, FL_YOK, FL_H1, FL_H2, FL_G1, FL_S2, FL_TAG = 16 };
constexpr int SPIN_LDS = 1 << 18, SPIN_GLB = 1 << 15;

DI bool wall(bool p) { return __builtin_amdgcn_ballot_w64(!p) == 0; }
DI unsigned umin(unsigned a, unsigned b) { return a < b ? a : b; }
DI unsigned umax(unsigned a, unsigned b) { return a > b ? a : b; }
DI float asf(unsigned v) { return __builtin_bit_cast(float, v); }
DI unsigned asu(float v) { return __builtin_bit_cast(unsigned, v); }
DI float bflo(unsigned v) { return asf(v << 16); }
DI float bfhi(unsigned v) { return asf(v & 0xffff0000u); }
DI float bf1(u16 v) { return asf((unsigned)v << 16); }
DI unsigned pkbf(float lo, float hi) { return __builtin_bit_cast(unsigned, __builtin_convertvector(f32x2{lo, hi}, b16x2)); }
DI unsigned pkh(f32x2 v) { return __builtin_bit_cast(unsigned, __builtin_convertvector(v, h16x2)); }
DI float rbf(float x) { return bflo(pkbf(x, x)); }
template <int CTRL> DI float dppf(float x) { return __builtin_bit_cast(float, __builtin_amdgcn_mov_dpp(__builtin_bit_cast(int, x), CTRL, 0xf, 0xf, true)); }
DI float row16_sum(float v) { v += dppf<0xB1>(v); v += dppf<0x4E>(v); v += dppf<0x124>(v); v += dppf<0x128>(v); return v; }

DI void glds4(const void* sb, unsigned v0, unsigned v1, unsigned v2, unsigned v3, unsigned lds) {
  asm volatile("s_mov_b32 m0, %5\n\ts_nop 0\n\tglobal_load_lds_dwordx4 %0, %4\n\tglobal_load_lds_dwordx4 %1, %4 offset:1024\n\t"
               "global_load_lds_dwordx4 %2, %4 offset:2048\n\tglobal_load_lds_dwordx4 %3, %4 offset:3072"
               :: "v"(v0), "v"(v1), "v"(v2), "v"(v3), "s"(sb), "s"(lds) : "memory");
}
DI void glds1(const void* sb, unsigned v0, unsigned lds) {
  asm volatile("s_mov_b32 m0, %2\n\ts_nop 0\n\tglobal_load_lds_dwordx4 %0, %1" :: "v"(v0), "s"(sb), "s"(lds) : "memory");
}
DI void glds2(const void* sb, unsigned v0, unsigned v1, unsigned lds) {
  asm volatile("s_mov_b32 m0, %3\n\ts_nop 0\n\tglobal_load_lds_dwordx4 %0, %2\n\tglobal_load_lds_dwordx4 %1, %2 offset:1024"
               :: "v"(v0), "v"(v1), "s"(sb), "s"(lds) : "memory");
}
template <int N> DI void waitv() { asm volatile("s_waitcnt vmcnt(%0)" :: "i"(N) : "memory"); }
DI void waitl() { asm volatile("s_waitcnt lgkmcnt(0)" ::: "memory"); }
DI u32x2 vtr(lptr p) { return __builtin_bit_cast(u32x2, __builtin_amdgcn_ds_read_tr16_b64_v4i16((LAS s16x4*)p)); }
DI u32x4 tr2(lptr p, int d) { u32x2 lo = vtr(p), hi = vtr(p + d); return u32x4{lo[0], lo[1], hi[0], hi[1]}; }
DI u32x4 ld16(lptr p) { return *(LAS u32x4*)p; }
DI void st16(lptr p, u32x4 v) { *(LAS u32x4*)p = v; }

DI unsigned flag_ld(unsigned addr) { unsigned r; asm volatile("ds_read_b32 %0, %1\n\ts_waitcnt lgkmcnt(0)" : "=v"(r) : "v"(addr) : "memory"); return __builtin_amdgcn_readfirstlane(r); }
DI void flag_wait(unsigned fb, int idx, unsigned need) { _Pragma("nounroll") for (int sp = 0; sp < SPIN_LDS && flag_ld(fb + idx * 4) < need; ++sp) __builtin_amdgcn_s_sleep(1); }
DI void flag_set(unsigned fb, int idx, unsigned v) {
  asm volatile("s_waitcnt lgkmcnt(0)\n\ts_mov_b64 exec, 1\n\tds_write_b32 %0, %1\n\ts_mov_b64 exec, -1" :: "v"(fb + idx * 4), "v"(v) : "memory"); }
DI unsigned flag_add(unsigned fb, int idx, int lane) {
  unsigned r; (void)lane;
  asm volatile("s_waitcnt lgkmcnt(0)\n\ts_mov_b64 exec, 1\n\tds_add_rtn_u32 %0, %1, %2\n\ts_mov_b64 exec, -1\n\ts_waitcnt lgkmcnt(0)" : "=&v"(r) : "v"(fb + idx * 4), "v"(1u) : "memory");
  return __builtin_amdgcn_readfirstlane(r);
}
#define RLX_AGENT __ATOMIC_RELAXED, __HIP_MEMORY_SCOPE_AGENT
DI bool gflag_ok(const char* ws, unsigned off, u64 tag) { return __hip_atomic_load((GAS u64*)(ws + off), RLX_AGENT) == tag; }

#define STAMP(i) do {} while (0)

DI f32x2 csq(f32x2 x) { const float t = x.x + x.x; return f32x2{__builtin_fmaf(x.x, x.x, -(x.y * x.y)), t * x.y}; }
DI h16x2 toh(f32x2 v) { return __builtin_convertvector(v, h16x2); }
DI h16x2 hmul(h16x2 x, h16x2 y) { return __builtin_elementwise_fma(h16x2{x.y, x.y}, h16x2{-y.y, y.x}, h16x2{x.x, x.x} * y); }
DI f32x2 cexp_p(float a2, float bh, float bl, float p) {
  const float e = __builtin_amdgcn_exp2f(a2 * p);
  const float ph = __builtin_fmaf(bl, p, __builtin_amdgcn_fractf(bh * p));
  return f32x2{e * __builtin_amdgcn_cosf(ph), e * __builtin_amdgcn_sinf(ph)};
}
template <int NS, int ROW0> DI void put4(const unsigned (&tb)[NS], const h16x2 (&a)[NS], const h16x2 (&b)[NS], const h16x2 (&c)[NS], const h16x2 (&d)[NS]) {
#pragma unroll
  for (int e = 0; e < NS; ++e)
    asm volatile("s_mov_b32 m0, %4\n\ts_nop 0\n\tds_write_addtid_b32 %0 offset:%5\n\tds_write_addtid_b32 %1 offset:%5+784\n\t"
                 "ds_write_addtid_b32 %2 offset:%5+1568\n\tds_write_addtid_b32 %3 offset:%5+2352"
                 :: "v"(a[e]), "v"(b[e]), "v"(c[e]), "v"(d[e]), "s"(tb[e]), "i"(ROW0 * PROW + POOL_BIAS) : "memory");
}
#define FORS _Pragma("unroll") for (int e = 0; e < NS; ++e)
template <int NS> DI void kpool(const unsigned (&tb)[NS], const float (&pv)[NS][4], const float (&ldt)[NS], bool join, float (&sc)[NS]) {
  float dt[NS];
  FORS dt[e] = rbf(__builtin_amdgcn_exp2f(ldt[e] * 1.44269504f));
  unsigned eb[NS]; float cs[NS];
  FORS {
    unsigned mi = umax(asu(pv[e][2]) & 0x7fffffffu, asu(pv[e][3]) & 0x7fffffffu);
    mi = umax(mi, asu(dppf<0xB1>(asf(mi)))); mi = umax(mi, asu(dppf<0x4E>(asf(mi)))); mi = umax(mi, asu(dppf<0x124>(asf(mi)))); mi = umax(mi, asu(dppf<0x128>(asf(mi))));
    eb[e] = umax(umax((unsigned)__builtin_amdgcn_readlane(mi, 0), (unsigned)__builtin_amdgcn_readlane(mi, 16)),
                 umax((unsigned)__builtin_amdgcn_readlane(mi, 32), (unsigned)__builtin_amdgcn_readlane(mi, 48))) >> 23;
  }
  if (join) eb[0] = eb[NS - 1] = umax(eb[0], eb[NS - 1]);
  FORS { eb[e] = umin(umax(eb[e], 2u), 252u); cs[e] = asf((254u - eb[e]) << 23); }
  float a2[NS], bh[NS], bl[NS];
  FORS {
    const float ea = rbf(__builtin_amdgcn_exp2f(pv[e][0] * 1.44269504f));
    a2[e] = -(ea * dt[e]) * 1.44269504f;
    const float b = pv[e][1] * dt[e];
    bh[e] = b * 0.15915494f;
    bl[e] = __builtin_fmaf(b, 6.4206382e-9f, __builtin_fmaf(b, 0.15915494f, -bh[e]));
  }
  h16x2 one[NS], w8[NS], w16[NS], w24[NS], w32[NS], w64[NS], w96[NS], w1[NS], w2[NS], w3[NS], w4[NS], C[NS], c4[NS];
  FORS {
    const f32x2 f1 = cexp_p(a2[e], bh[e], bl[e], 1.f), f2 = csq(f1), f4 = csq(f2);
    const f32x2 f8 = csq(f32x2{f4.x, -f4.y}), f16 = csq(f8), f32 = csq(f16), f64 = csq(f32);
    one[e] = h16x2{(_Float16)1.f, (_Float16)0.f};
    w8[e] = toh(f8); w16[e] = toh(f16); w32[e] = toh(f32); w64[e] = toh(f64); w24[e] = hmul(w8[e], w16[e]); w96[e] = hmul(w32[e], w64[e]);
    w1[e] = toh(f1); w2[e] = toh(f2); w4[e] = toh(f4); w3[e] = hmul(w1[e], w2[e]);
    C[e] = toh(f32x2{pv[e][2] * cs[e], pv[e][3] * cs[e]}); c4[e] = hmul(C[e], w4[e]);
  }
  put4<NS, 0>(tb, one, w8, w16, w24);
  put4<NS, 4>(tb, w32, w64, w96, w1);
  put4<NS, 8>(tb, w2, w3, C, c4);
  h16x2 c128[NS], c132[NS], w256[NS], w258[NS], w512[NS], w768[NS], w1024[NS], w1280[NS], w1536[NS], w1792[NS], w1796[NS];
  FORS {
    const f32x2 f128 = cexp_p(a2[e], bh[e], bl[e], 128.f), f256 = csq(f128), f512 = csq(f256);
    const h16x2 w128 = toh(f128);
    w256[e] = toh(f256); w512[e] = toh(f512); c128[e] = hmul(C[e], w128); c132[e] = hmul(c4[e], w128);
    w258[e] = hmul(w256[e], w2[e]); w768[e] = hmul(w512[e], w256[e]);
    w1024[e] = toh(csq(f512)); w1280[e] = hmul(w1024[e], w256[e]); w1536[e] = hmul(w1024[e], w512[e]);
    w1792[e] = hmul(w1024[e], w768[e]); w1796[e] = hmul(w1792[e], w4[e]);
  }
  put4<NS, 12>(tb, c128, c132, w256, w258);
  put4<NS, 16>(tb, w512, w768, w1024, w1280);
  put4<NS, 19>(tb, w1280, w1536, w1792, w1796);
  FORS sc[e] = 2.f * dt[e] * asf(eb[e] << 23);
}
struct KPtr {
  lptr pbx, pby, py, px0, px2;
  DI void init(lptr pool, int lane) {
    const int r = lane & 15, x = r & 3, y = r >> 2;
    lptr p = pool + (lane >> 4) * 16;
    pbx = p + x * PROW; pby = p + (y ? 3 + y : 0) * PROW; py = p + (10 + y) * PROW; px0 = p + (x ? 6 + x : 0) * PROW; px2 = p + (y == 3 ? 22 : 18 + x) * PROW;
  }
  DI void fetch(u32x4 (&v)[6], int o) const {
    v[0] = ld16(pbx + o); v[1] = ld16(pby + o); v[2] = ld16(py + o); v[3] = ld16(px0 + o); v[4] = ld16(pbx + 14 * PROW + o); v[5] = ld16(px2 + o);
  }
  DI void advance() { pbx += 128; pby += 128; py += 128; px0 += 128; px2 += 128; }
};
DI void kprod(const u32x4 (&v)[6], u32x4 (&o)[4]) {
  unsigned t[16];
  if (EXP & 2048) { SBAR(); __builtin_amdgcn_s_setprio(3); SBAR(); }
#pragma unroll
  for (int i = 0; i < 16; ++i) { const unsigned xe = v[(i >> 2) ? 2 + (i >> 2) : 0][i & 3], ye = v[(i >> 2) ? 2 : 1][i & 3];
    asm("v_pk_mul_f16 %0, %1, %2 op_sel_hi:[0,1]" : "=v"(t[i]) : "v"(xe), "v"(ye)); }
  SBAR();
#pragma unroll
  for (int i = 0; i < 16; ++i) { const unsigned xe = v[(i >> 2) ? 2 + (i >> 2) : 0][i & 3], ye = v[(i >> 2) ? 2 : 1][i & 3]; unsigned z;
    asm("v_pk_fma_f16 %0, %1, %2, %3 op_sel:[1,1,0] op_sel_hi:[1,0,1] neg_lo:[0,1,0]" : "=v"(z) : "v"(xe), "v"(ye), "v"(t[i])); o[i >> 2][i & 3] = z; }
  SBAR();
}
template <int H> DI void kprodh(const u32x4 (&v)[6], u32x4 (&o)[4]) {
  unsigned t[8];
#pragma unroll
  for (int i = 8 * H; i < 8 * H + 8; ++i) { const unsigned xe = v[(i >> 2) ? 2 + (i >> 2) : 0][i & 3], ye = v[(i >> 2) ? 2 : 1][i & 3];
    asm("v_pk_mul_f16 %0, %1, %2 op_sel_hi:[0,1]" : "=v"(t[i & 7]) : "v"(xe), "v"(ye)); }
  SBAR();
#pragma unroll
  for (int i = 8 * H; i < 8 * H + 8; ++i) { const unsigned xe = v[(i >> 2) ? 2 + (i >> 2) : 0][i & 3], ye = v[(i >> 2) ? 2 : 1][i & 3]; unsigned z;
    asm("v_pk_fma_f16 %0, %1, %2, %3 op_sel:[1,1,0] op_sel_hi:[1,0,1] neg_lo:[0,1,0]" : "=v"(z) : "v"(xe), "v"(ye), "v"(t[i & 7])); o[i >> 2][i & 3] = z; }
  asm volatile("" : "+v"(o[2 * H]), "+v"(o[2 * H + 1]));
  SBAR();
}
DI void kmma1(const u32x4 (&o0)[4], f32x4 (&acc)[3]) {
  SBAR(); asm volatile("s_setprio 3\n\ts_nop 1"); SBAR();
#pragma unroll
  for (int i = 0; i < 3; ++i) acc[i] = MFMA_H(o0[1 + i], o0[0], acc[i]);
  SBAR(); __builtin_amdgcn_s_setprio(0); SBAR();
}
DI void kscale(f32x4 (&ko)[3], f32x4 (&acc)[3], float sc) {
#pragma unroll
  for (int i = 0; i < 3; ++i) {
#pragma unroll
    for (int j = 0; j < 4; ++j) ko[i][j] = __builtin_fmaf(acc[i][j], sc, ko[i][j]);
    acc[i] = f32x4{0, 0, 0, 0};
  }
}
DI float ubf(const u16* p, int i) { const unsigned w = ((const __attribute__((address_space(4))) unsigned*)(uintptr_t)p)[i >> 1]; return (i & 1) ? bfhi(w) : bflo(w); }
DI void kmma2(const u32x4 (&o0)[4], const u32x4 (&o1)[4], f32x4 (&acc)[3]) {
  SBAR(); asm volatile("s_setprio 3\n\ts_nop 1"); SBAR();
#pragma unroll
  for (int i = 0; i < 3; ++i) acc[i] = MFMA_H(o0[1 + i], o0[0], acc[i]);
#pragma unroll
  for (int i = 0; i < 3; ++i) acc[i] = MFMA_H(o1[1 + i], o1[0], acc[i]);
  SBAR(); __builtin_amdgcn_s_setprio(0); SBAR();
}
DI void kstore(lptr kl, int lane, const f32x4 (&acc)[3], float dsk) {
  const int c = lane & 15, q = lane >> 4;
  LAS u16* t = (LAS u16*)kl;
  const float k0 = acc[0][0] + (lane == 0 ? dsk : 0.f);
  *(LAS u32x2*)(t + 128 * (q >> 1) + 8 * c + 4 * (q & 1)) = u32x2{pkbf(k0, acc[0][1]), pkbf(acc[0][2], acc[0][3])};
  *(LAS unsigned*)(t + 256 + 64 * (q >> 1) + 2 * (q & 1) + 4 * c) = pkbf(acc[1][0], acc[1][1]);
  const unsigned p1 = pkbf(acc[1][2], acc[1][3]);
  const int i1 = 384 + 32 * (q >> 1) + (q & 1) + 2 * c;
  t[i1] = (u16)p1; t[i1 + 64] = (u16)(p1 >> 16);
  const unsigned r0 = pkbf(acc[2][0], acc[2][1]), r1 = pkbf(acc[2][2], acc[2][3]);
  if (!(q & 1)) { const int i2 = 512 + 8 * q + c; t[i2] = (u16)r0; t[i2 + 32] = (u16)(r0 >> 16); t[i2 + 64] = (u16)r1; t[i2 + 96] = (u16)(r1 >> 16); }
  else if (lane == 63) t[640] = (u16)r0;
}
DI unsigned dup16(unsigned a) { return a | (a << 16); }
DI unsigned bsel(unsigned m, unsigned a, unsigned b) { asm("" : "+v"(m)); return (a & m) | (b & ~m); }
DI unsigned rot16(unsigned a) { return __builtin_amdgcn_alignbit(a, a, 16); }
DI void kexpand(lptr kr, lptr kl, int lane) {
  LAS u16* t = (LAS u16*)kl;
  auto put = [&](int ch, unsigned dp, unsigned d0, unsigned d1, unsigned d2, unsigned d3) {
    st16(kr + 16 * ch, u32x4{d0, d1, d2, d3}); st16(kr + KC1 - 4 + 16 * ch, u32x4{dp, d0, d1, d2}); };
  const int n = 63 - lane, n0 = n & 31;
  const unsigned a0 = t[576 + n], b0 = t[577 + n], a1 = t[512 + n], b1 = t[513 + n];
  const unsigned ab = *(LAS unsigned*)(t + 384 + 2 * n), c2 = t[386 + 2 * n];
  const u32x2 v1 = *(LAS u32x2*)(t + 256 + 4 * n0); const unsigned q4 = t[260 + 4 * n0];
  const u32x4 v0 = ld16((lptr)(t + 8 * n0)); const unsigned w = *(LAS unsigned*)(t + 8 * n0 + 8);
  const unsigned mo = (n & 1) ? ~0u : 0u, lo = lane >= 32 ? ~0u : 0u, m31 = n == 31 ? ~0u : 0u;
  put(lane, dup16(b0), dup16(b0), bsel(mo, dup16(b0), b0 | (a0 << 16)), dup16(a0), dup16(a0));
  put(64 + lane, dup16(b1), dup16(b1), bsel(mo, dup16(b1), b1 | (a1 << 16)), dup16(a1), dup16(a1));
  put(128 + lane, dup16(c2), dup16(c2), dup16(ab >> 16), rot16(ab), dup16(ab & 0xffffu));
  put(192 + lane, bsel(lo, bsel(m31, dup16(w & 0xffffu), rot16(w)), dup16(q4)), bsel(lo, rot16(v0[3]), q4 | (v1[1] & 0xffff0000u)),
      bsel(lo, rot16(v0[2]), dup16(v1[1] & 0xffffu)), bsel(lo, rot16(v0[1]), (v1[1] & 0xffffu) | (v1[0] & 0xffff0000u)), bsel(lo, rot16(v0[0]), dup16(v1[0] & 0xffffu)));
  if (lane >= 60) put(319 - lane, lane == 63 ? rot16(v0[0]) : 0u, 0u, 0u, 0u, 0u);
}

struct ConvPlan { int n; int item[36][3]; };
constexpr ConvPlan conv_plan(int tmask, int maxt) {
  ConvPlan p{};
  int fifo[64] = {}, head = 0, tail = 0, carry[16] = {}, nc = 0, per = (tmask == 0xff) ? 1 : 2;
  for (int pass = 0; pass < 2; ++pass) {
    head = tail = 0;
    for (int i = 0; i < nc; ++i) fifo[tail++] = carry[i] + 32;
    int e = 0;
    for (int m = maxt; m >= 0; --m) {
      for (int j = 0; j + m <= maxt; ++j) {
        const bool on = (tmask >> (m + j)) & 1;
        int k = 0;
        if (m == 0) { if (on && j < maxt - 1) p.item[e][k++] = j; else fifo[tail++] = j; }
        if (j + m == maxt) { fifo[tail++] = 8 + 2 * m; fifo[tail++] = 9 + 2 * m; }
        if (on) { while (k < per && head < tail) p.item[e][k++] = fifo[head++]; while (k < 3) p.item[e][k++] = -1; ++e; }
      }
    }
    p.n = e; nc = 0;
    while (head < tail) carry[nc++] = fifo[head++];
  }
  return p;
}
template <int TMASK>
DI void conv_units(lptr kr, lptr ub, int lane, int rho0, int nrho, f32x4 (&acc)[8]) {
  constexpr int MAXT = (TMASK & 0x80) ? 7 : 5;
  constexpr ConvPlan plan = conv_plan(TMASK, MAXT);
  const int r = lane & 15, q = lane >> 4;
  lptr pk = kr + ((r & 1) ? 0 : KC1) + 4092 - 4 * r + 16 * q - 3584 - 64 * rho0 - 64;
  lptr pu = ub + ((r & 1) ? 0 : UC1) + 64 * (r >> 1) + 16 * q + 64 * (7 - rho0) - 64;
  lptr pk2 = pk + 8;
  u32x2 Kl[8], Kh[8]; u32x4 Uf[8];
  asm volatile("" : "+v"(pk), "+v"(pk2), "+v"(pu));
#pragma unroll
  for (int j = 0; j <= MAXT; ++j) { const int m = MAXT - j; Uf[j] = ld16(pu + 64 + 512 * j); Kl[m] = *(LAS u32x2*)(pk + 64 + 512 * (7 - m)); Kh[m] = *(LAS u32x2*)(pk2 + 64 + 512 * (7 - m)); SBAR(); }
#pragma unroll 1
  for (int it = 0; it < nrho; ++it) {
    int e = 0;
#pragma unroll
    for (int m = MAXT; m >= 0; --m) {
#pragma unroll
      for (int j = 0; j + m <= MAXT; ++j) {
        if ((TMASK >> (m + j)) & 1) {
          acc[m + j] = MFMA_BF(Uf[j], (u32x4{Kl[m][0], Kl[m][1], Kh[m][0], Kh[m][1]}), acc[m + j]);
#pragma unroll
          for (int k = 0; k < 3; ++k) {
            const int c = plan.item[e][k];
            if (c >= 0) {
              const int o = (c & 32) ? 64 : 0, cc = c & 31, mm = (cc - 8) >> 1;
              if (cc < 8) Uf[cc] = ld16(pu + o + 512 * cc);
              else if (cc & 1) Kh[mm] = *(LAS u32x2*)(pk2 + o + 512 * (7 - mm));
              else Kl[mm] = *(LAS u32x2*)(pk + o + 512 * (7 - mm));
            }
          }
          SBAR();
          ++e;
        }
      }
    }
    pk -= 64; pk2 -= 64; pu -= 64;
    asm volatile("" : "+v"(pk), "+v"(pk2), "+v"(pu));
  }
}

template <int N> struct IC { static constexpr int value = N; };
DI u64 uni64(u64 v) { return (u64)(unsigned)__builtin_amdgcn_readfirstlane((unsigned)v) | ((u64)(unsigned)__builtin_amdgcn_readfirstlane((unsigned)(v >> 32)) << 32); }
DI int fresh_lane() { int l; asm volatile("v_mbcnt_lo_u32_b32 %0, -1, 0\n\tv_mbcnt_hi_u32_b32 %0, -1, %0" : "=v"(l)); return l; }
#define ARR() do {} while (0)
#define BAR() do { SBAR(); ARR(); asm volatile("s_barrier" ::: "memory"); SBAR(); } while (0)
#define BAR_L() do { SBAR(); asm volatile("s_waitcnt lgkmcnt(0)" ::: "memory"); SBAR(); ARR(); asm volatile("s_barrier" ::: "memory"); SBAR(); } while (0)
#define LD_SC1(off) __builtin_amdgcn_raw_buffer_load_b128(rs, (off), 0, SC1)
#define ST_SC1(v, off) __builtin_amdgcn_raw_buffer_store_b128((v), rs, (off), 0, SC1)

__global__ __launch_bounds__(512, 2) void fused_kernel(const u16* __restrict__ x, const u16* __restrict__ p_lar, const u16* __restrict__ p_aim,
                                                       const u16* __restrict__ p_cr, const u16* __restrict__ p_ci, const u16* __restrict__ p_ldt,
                                                       const u16* __restrict__ p_dsk, const u16* __restrict__ wg, const u16* __restrict__ wu,
                                                       const u16* __restrict__ wd, u16* __restrict__ out, char* ws) {
  extern __shared__ __attribute__((aligned(16))) char smem[];
  const int tid = __builtin_amdgcn_workitem_id_x(), bid = __builtin_amdgcn_workgroup_id_x();
  u64 pc_; asm volatile("s_getpc_b64 %0" : "=s"(pc_));
  lptr lds = (lptr)smem;
  const unsigned lds0 = (unsigned)(uintptr_t)smem;
  const unsigned fb = lds0 + L_FLG;
  const int wraw = __builtin_amdgcn_readfirstlane(tid >> 6), lane = tid & 63;
  const int wid = wraw ^ ((wraw >> 2) << 1);
  const int xcd = bid & 7, bi = bid >> 3;
  const int rb = (xcd >> 1) * 4 + (bi >> 3), cb = (xcd & 1) * 8 + (bi & 7);
  const int tg = (xcd >> 1) * 8 + (bi >> 2), nb = (xcd & 1) * 4 + (bi & 3);
  const int cw = xcd * 32 + bi, odd = cw & 1, c0 = 5 * (cw >> 1) + 2 * odd;
  const int chA = c0 + odd, chC = odd ? c0 : c0 + 2;
  const int rot = (EXP & 2) ? 0 : rb >= 10 ? rb - 10 : rb;
  const __amdgpu_buffer_rsrc_t rs = __builtin_amdgcn_make_buffer_rsrc(ws, 0, (int)WS_END, 0x00020000);
  const int r = lane & 15, q = lane >> 4;

  u64 tag = 0;
  const int lw = wid - 4, kw = wid - 6;
  unsigned vx[4], vw[12];
  auto issue = [&](int s, int sbase, int h, int P0 = 0, int P1 = 5) {
    int ks = s + rot; if (ks >= 10) ks -= 10;
    const unsigned base = lds0 + sbase;
    const char* sx = (const char*)x + (size_t)(rb * 128 + h * 64) * 1280 - 4096 + ks * 128;
    const char* sw = (const char*)(h ? wu : wg) + cb * 192 - 4096 + (size_t)ks * 64 * 3072;
    if (P0 <= 0 && 0 < P1) glds4(sx, vx[0], vx[1], vx[2], vx[3], base + h * 8192);
    if (P0 <= 1 && 1 < P1) glds4(sw, vw[0], vw[1], vw[2], vw[3], base + 16384 + h * 12288);
    if (P0 <= 2 && 2 < P1) glds4(sx + 32 * 1280, vx[0], vx[1], vx[2], vx[3], base + h * 8192 + 4096);
    if (P0 <= 3 && 3 < P1) glds4(sw, vw[4], vw[5], vw[6], vw[7], base + 16384 + h * 12288 + 4096);
    if (P0 <= 4 && 4 < P1) glds4(sw, vw[8], vw[9], vw[10], vw[11], base + 16384 + h * 12288 + 8192);
  };
  auto prep_dma = [&](int ln) {
#pragma unroll
    for (int j = 0; j < 4; ++j) { const int row = 8 * j + (ln >> 3); vx[j] = row * 1280 + (((ln & 7) ^ ((row >> 1) & 7)) << 4) + 4096 - 1024 * j; }
#pragma unroll
    for (int j = 0; j < 3; ++j) { const int c = j * 64 + ln, row = (c * 43691) >> 19, pos = c - row * 12;
      const unsigned v = row * 3072 + ((pos ^ (((row >> 3) & 1) << 1)) << 4) + 4096;
#pragma unroll
      for (int g = 0; g < 4; ++g) vw[3 * g + j] = v + g * 16 * 3072 - 1024 * ((3 * g + j) & 3); }
  };
  const int lnb = (xcd >> 1) * 64 + (xcd & 1) * 32 + bi;
  const int pidx = (0x200210 >> (4 * wid)) & 3, ptl = (0x28 >> wid) & 1, p3 = (0x08 >> wid) & 1;
  const int ch0 = c0 + ((0x0210210 >> (4 * (pidx + odd))) & 3), ch1 = ch0 + p3;
  const int pt0 = pidx * PCH + (pidx >> 1) * (SL2 - SA - 2 * PCH) + ptl * 512;
  unsigned o = ch0 * 384 + ptl * 256 + (lane & 31) * 4 + (lane >> 5) * (128 - ptl * 128 + p3 * 384);
  unsigned xo0 = lane * 16, xo1 = q * 1280 + r * 16 + 1024;
  asm volatile("" : "+v"(o), "+v"(xo0), "+v"(xo1));
  asm volatile("" :: "s"(x), "s"(p_lar), "s"(p_aim), "s"(p_cr), "s"(p_ci), "s"(p_ldt), "s"(p_dsk), "s"(wg), "s"(wu), "s"(wd), "s"(out), "s"(ws));
  if ((0x2f >> wid) & 1) {
    asm volatile("s_mov_b32 m0, %5\n\ts_nop 0\n\tglobal_load_lds_dword %0, %1\n\tglobal_load_lds_dword %0, %2 offset:784\n\t"
                 "global_load_lds_dword %0, %3 offset:1568\n\tglobal_load_lds_dword %0, %4 offset:2352"
                 :: "v"(o), "s"(p_lar), "s"((const char*)p_aim - 784), "s"((const char*)p_cr - 1568), "s"((const char*)p_ci - 2352), "s"(lds0 + SA + pt0) : "memory");
  }
  if (wid >= 6) {
    const char* xr = (const char*)x + (size_t)(lnb * 8 + kw * 4) * 1280;
#pragma unroll
    for (int j = 0; j < 4; ++j) glds1(xr + j * 1280, xo0, lds0 + L_LNB + (kw * 4 + j) * LNR);
    glds1(xr, xo1, lds0 + L_LNT + kw * 1024);
  } else if (wid >= 4) { if (lane < 16) *(LAS unsigned*)(lds + L_FLG + lane * 4) = 0; prep_dma(lane); }
  SBAR(); asm volatile("s_waitcnt lgkmcnt(0)\n\ts_barrier" ::: "memory"); SBAR();
  const float ldt0 = ubf(p_ldt, ch0), ldt1 = ubf(p_ldt, ch1);
  if (wid == 4 || wid == 5) {
    asm volatile("s_mov_b32 m0, %2\n\ts_mov_b64 exec, 0x3ff\n\tglobal_load_lds_dword %0, %1\n\ts_mov_b64 exec, -1"
                 :: "v"(((bi & 15) * 20 + lw * 10 + lane) * 128), "s"(pc_ & ~127ull), "s"(lds0 + L_RTB + 10240 + lw * 64) : "memory");
    issue(0, SL0, lw);
  }
  auto lds_params = [&](float (&pv)[4], int m) {
#pragma unroll
    for (int e = 0; e < 4; ++e) pv[e] = bf1(*(LAS u16*)(lds + SA + pt0 + e * PROW + m * 2));
  };
  if (wid >= 6) {
    waitv<0>();
    STAMP(0);
    lptr xp = lds + L_LNB + (kw * 4 + q) * LNR + r * 16;
    u32x4 xv[5];
#pragma unroll
    for (int j = 0; j < 4; ++j) xv[j] = ld16(xp + 256 * j);
    xv[4] = ld16(lds + L_LNT + kw * 1024 + lane * 16);
    float v[40]; float s0 = 0.f, s1 = 0.f;
#pragma unroll
    for (int j = 0; j < 20; ++j) { const unsigned w = xv[j >> 2][j & 3]; v[2 * j] = bflo(w); v[2 * j + 1] = bfhi(w); s0 += v[2 * j]; s1 += v[2 * j + 1]; }
    const float mean = row16_sum(s0 + s1) * (1.f / DM);
    float q0 = 0.f, q1 = 0.f;
#pragma unroll
    for (int j = 0; j < 20; ++j) { v[2 * j] -= mean; v[2 * j + 1] -= mean; q0 = __builtin_fmaf(v[2 * j], v[2 * j], q0); q1 = __builtin_fmaf(v[2 * j + 1], v[2 * j + 1], q1); }
    const float var = rbf(row16_sum(q0 + q1) * (1.f / DM));
    const float mb = rbf(mean), sd = rbf(__builtin_amdgcn_sqrtf(rbf(var + 1e-5f)));
    const float rsd = __builtin_amdgcn_rcpf(sd), off = (mean - mb) * rsd;
#pragma unroll
    for (int j = 0; j < 20; ++j) xv[j >> 2][j & 3] = pkbf(__builtin_fmaf(v[2 * j], rsd, off), __builtin_fmaf(v[2 * j + 1], rsd, off));
#pragma unroll
    for (int j = 0; j < 5; ++j) st16(xp + 256 * j, xv[j]);
    SBAR();
    {
      const int un = umin(cw * 2 + kw, 383), ukb = un >> 3, unb = un & 7;
      const int c3 = (ukb * 43) >> 7, kt = ukb - 3 * c3;
      const int t0 = kt == 0 ? 0 : kt == 1 ? 80 : 32, t1 = kt == 0 ? 0 : kt == 1 ? 48 : 32;
#pragma unroll
      for (int j = 0; j < 5; ++j) { const int c = j * 64 + lane, row = (c * 6554) >> 16, pos = c - row * 10;
        glds1(wd, (c3 * 96 + row + (row < 16 ? t0 : t1)) * 1280 + unb * 160 + pos * 16, lds0 + L_RTB + kw * 5120 + j * 1024); }
    }
    SBAR();
    if (r == 0) __builtin_amdgcn_raw_buffer_store_b64(u32x2{asu(mb), asu(sd)}, rs, WS_ST + (lnb * 8 + kw * 4 + q) * 8, 0, SC1);
  } else if (wid == 4) {
    prep_dma(fresh_lane());
    STAMP(0);
    issue(1, SL1, 0); issue(1, SL1, 1);
    waitv<40>();
  } else if (wid == 5) {
    waitv<21>();
    STAMP(0);
    const unsigned tb[1] = {lds0 + pt0};
    const float ldt[1] = {ldt0};
    float pv[1][4], sc[1];
    lds_params(pv[0], lane);
    kpool<1>(tb, pv, ldt, false, sc);
    if (lane == 0) *(LAS float*)(lds + L_SC + 20) = sc[0];
    flag_add(fb, FL_KP, lane);
    waitv<0>();
  } else {
    waitv<0>();
    STAMP(0);
    const unsigned tb[2] = {lds0 + pt0, lds0 + pt0 + 256 + p3 * (PCH - 256)};
    const float ldt[2] = {ldt0, ldt1};
    float pv[2][4], sc[2];
    lds_params(pv[0], lane); lds_params(pv[1], 64 + lane);
    kpool<2>(tb, pv, ldt, wid != 3, sc);
    if (lane == 0) { *(LAS float*)(lds + L_SC + wid * 4) = sc[0]; if (wid == 3) *(LAS float*)(lds + L_SC + 16) = sc[1]; }
    if (wid == 2) flag_add(fb, FL_KP, lane);
  }
  STAMP(1);

  if (wid >= 6) {
    const int un = umin(cw * 2 + kw, 383), ukb = un >> 3, unb = un & 7;
    const float dsk = ubf(p_dsk, chA + kw);
    u32x4 xv[5];
    u64 ep = __hip_atomic_load((GAS u64*)(ws + WS_EPOCH), RLX_AGENT);
    BAR_L();
    u32x4 wt[5], wf[12];
#pragma unroll
    for (int j = 0; j < 5; ++j) xv[j] = tr2(lds + L_LNB + (r >> 2) * LNR + (kw * 5 + j) * 128 + q * 32 + (lane & 3) * 8, 4 * LNR);
    {
      const int sg = (q & 1) * 32;
      lptr pa = lds + SL0 + 16384 + kw * 12288 + (8 * q + (r >> 2)) * 192 + 8 * (lane & 3);
#pragma unroll
      for (int t = 0; t < 12; ++t) wf[t] = tr2(pa + (t / 6) * 6144 + (t % 6) * 32 + ((t & 1) ? -sg : sg), 768);
    }
    lptr pool = lds + SA + kw * PCH;
    KPtr kp; kp.init(pool, lane);
    u32x4 kv[2][6], kq[4];
    kp.fetch(kv[0], 0); kp.fetch(kv[1], 64);
    SBAR();
    asm volatile("s_waitcnt vmcnt(0)" : "+v"(ep) :: "memory");
#pragma unroll
    for (int j = 0; j < 5; ++j) wt[j] = tr2(lds + L_RTB + kw * 5120 + (4 * q + (r >> 2)) * 160 + j * 32 + (lane & 3) * 8, 2560);
    tag = uni64(ep) + 1;
    BAR_L();
    *(volatile LAS u64*)(lds + L_FLG + FL_TAG * 4) = tag;
    unsigned pf_sink = 0;
    f32x4 ko[3] = {{0, 0, 0, 0}, {0, 0, 0, 0}, {0, 0, 0, 0}}, ka[3] = {{0, 0, 0, 0}, {0, 0, 0, 0}, {0, 0, 0, 0}};
    u32x4 f0 = {0, 0, 0, 0}, f1 = {0, 0, 0, 0};
#pragma unroll
    for (int sl = 0; sl < 8; ++sl) {
      if (sl == 3 && kw == 0 && lane < 8) __hip_atomic_store((GAS u64*)(ws + WS_F0 + lane * F0REP + cw * 8), tag, RLX_AGENT);
#pragma unroll
      for (int hs = 3 * sl; hs < 3 * sl + 3; ++hs) {
        const int k = hs >> 1;
        if (hs == 16) kscale(ko, ka, *(LAS float*)(lds + L_SC + kw * 4));
        if (!(hs & 1)) kprodh<0>(kv[k & 1], kq);
        else { kprodh<1>(kv[k & 1], kq); if (k + 2 < 12) kp.fetch(kv[k & 1], 64 * (k + 2)); kmma1(kq, ka); }
      }
      if (sl == 0) {
#pragma unroll
        for (int j = 0; j < 5; ++j) ST_SC1(xv[j], WS_XB + lnb * 10240 + (kw * 5 + j) * 1024 + lane * 16);
      }
      if (sl == 1) {
        if (rb < 10) {
          const u32x4 ones = {0x3f803f80u, 0x3f803f80u, 0x3f803f80u, 0x3f803f80u};
          f32x4 cs[6];
          SBAR(); __builtin_amdgcn_s_setprio(3); SBAR();
#pragma unroll
          for (int t = 0; t < 6; ++t) cs[t] = MFMA_BF(ones, wf[t], (f32x4{0, 0, 0, 0}));
#pragma unroll
          for (int t = 0; t < 6; ++t) cs[t] = MFMA_BF(ones, wf[6 + t], cs[t]);
          SBAR(); __builtin_amdgcn_s_setprio(0); SBAR();
          const float cs0 = q == 0 ? cs[0][0] : q == 1 ? cs[1][0] : q == 2 ? cs[2][0] : cs[3][0], cs1 = q == 0 ? cs[4][0] : cs[5][0];
          const unsigned co = WS_CS + (cb * 10 + rb) * 768 + kw * 384 + lane * 4;
          __builtin_amdgcn_raw_buffer_store_b32(asu(cs0), rs, co, 0, SC1);
          if (lane < 32) __builtin_amdgcn_raw_buffer_store_b32(asu(cs1), rs, co + 256, 0, SC1);
        }
        if (cw < 192) {
#pragma unroll
          for (int f = 0; f < 5; ++f) ST_SC1(wt[f], WS_WD + (unb * 48 + ukb) * 5120 + f * 1024 + lane * 16);
        }
      }
      if (sl == 2) waitv<0>();
      if (sl == 5 && kw == 0) { f0 = LD_SC1(WS_F0 + xcd * F0REP + lane * 16); f1 = LD_SC1(WS_F0 + xcd * F0REP + 1024 + lane * 16); }
      if (sl == 6 && kw == 0) {
        const unsigned tl = (unsigned)tag, th = (unsigned)(tag >> 32);
        _Pragma("nounroll") for (int sp = 0; sp < SPIN_GLB; ++sp) {
          if (wall(((f0[0] ^ tl) | (f0[1] ^ th) | (f0[2] ^ tl) | (f0[3] ^ th) | (f1[0] ^ tl) | (f1[1] ^ th) | (f1[2] ^ tl) | (f1[3] ^ th)) == 0u)) break;
          f0 = LD_SC1(WS_F0 + xcd * F0REP + lane * 16); f1 = LD_SC1(WS_F0 + xcd * F0REP + 1024 + lane * 16);
          __builtin_amdgcn_s_sleep(2);
        }
      }
      if (sl >= 6) {
        const unsigned po = WS_WD + nb * 245760 + (bi >> 2) * 30720 + umin(((sl - 6) * 2 + kw) * 64 + lane, 239u) * 128;
        asm volatile("global_load_dword %0, %1, %2 sc1" : "+v"(pf_sink) : "v"(po), "s"(ws) : "memory");
      }
      BAR();
    }
    STAMP(2);
    kscale(ko, ka, *(LAS float*)(lds + L_SC + 12 + kw * 4));
    kstore(pool + O_KL, lane, ko, dsk);
    kexpand(pool + O_KREV, pool + O_KL, lane);
    flag_set(fb, FL_KA + kw, 1);
    asm volatile("s_waitcnt vmcnt(0)" : "+v"(pf_sink) :: "memory");
    STAMP(3);
  } else {
    if (wid >= 4) {
      u32x4 cv[6][6];
      {
        KPtr kp; kp.init(lds + SL2 + lw * 384, lane);
        flag_wait(fb, FL_KP, 2);
#pragma unroll
        for (int i = 0; i < 6; ++i) kp.fetch(cv[i], 64 * i);
      }
      const float sca = *(LAS float*)(lds + L_SC + 8), scb = *(LAS float*)(lds + L_SC + (lw ? 20 : 8));
      SBAR();
      prep_dma(fresh_lane());
      BAR_L();
      issue(2, SL2, lw, 0, 1); issue(2, SL2, lw, 2, 3);
      f32x4 ka[3] = {{0, 0, 0, 0}, {0, 0, 0, 0}, {0, 0, 0, 0}};
      f32x4 kc[3] = {{0, 0, 0, 0}, {0, 0, 0, 0}, {0, 0, 0, 0}};
#pragma unroll
      for (int s = 0; s < 7; ++s) {
        if (s == 0) waitv<8>(); else waitv<20>();
        BAR();
        if (s == 0) { issue(2, SL2, lw, 1, 2); issue(2, SL2, lw, 3, 5); }
        issue(s + 3, s % 3 == 0 ? SL0 : s % 3 == 1 ? SL1 : SL2, lw);
        if (s >= 1 && !(EXP & 4096)) { u32x4 o[4]; kprod(cv[s - 1], o); kmma1(o, ka); }
        if (s == 2) kscale(kc, ka, sca);
      }
      kscale(kc, ka, scb);
      if (lw == 0) {
#pragma unroll
        for (int i = 0; i < 3; ++i) *(LAS f32x4*)(lds + L_STG + i * 1024 + lane * 16) = kc[i];
      }
      waitv<20>();
      BAR_L();
      STAMP(2);
      tag = uni64(*(volatile LAS u64*)(lds + L_FLG + FL_TAG * 4));
      u32x4 uc[8]; unsigned uprev;
      const int n0 = lw * 128 + (lane >> 2) * 8;
      const unsigned uo = WS_XB + n0 * 10240 + (c0 + (lane & 3)) * 16;
      u32x4 sv, cp[10];
      if (lw == 0) {
        sv = LD_SC1(WS_ST + rb * 1024 + lane * 16);
#pragma unroll
        for (int j = 0; j < 10; ++j) cp[j] = LD_SC1(WS_CS + (cb * 10 + j) * 768 + (lane < 48 ? lane : 0) * 16);
      }
#pragma unroll
      for (int j = 0; j < 8; ++j) uc[j] = LD_SC1(uo + j * 10240);
      uprev = __builtin_amdgcn_raw_buffer_load_b32(rs, uo - 10240 + 12, 0, SC1);
      SBAR();
      if (lw) {
#pragma unroll
        for (int i = 0; i < 3; ++i) kc[i] += *(LAS f32x4*)(lds + L_STG + i * 1024 + lane * 16);
        kstore(lds + L_PC, lane, kc, ubf(p_dsk, chC));
        waitv<9>();
      } else waitv<20>();
      BAR();
      STAMP(3);
      if (lw == 0) {
        f32x4 cs = {0, 0, 0, 0};
#pragma unroll
        for (int j = 0; j < 10; ++j) cs += __builtin_bit_cast(f32x4, cp[j]);
        st16(lds + L_STAT + lane * 16, sv);
        if (lane < 48) *(LAS f32x4*)(lds + L_CS + lane * 16) = cs;
        flag_set(fb, FL_EPI, 1);
        if (bid == 0 && lane == 0) __hip_atomic_store((GAS u64*)(ws + WS_EPOCH), tag, RLX_AGENT);
      }
      if (n0 == 0) uprev = 0;
      if ((lane & 3) < 3) {
        lptr ud = lds + L_U + (lane & 3) * UCH + 448 + n0 * 16;
#pragma unroll
        for (int j = 0; j < 8; ++j) {
          st16(ud + j * 16, uc[j]);
          const unsigned pw_ = j ? uc[j - 1][3] : uprev;
          st16(ud + UC1 + j * 16, u32x4{__builtin_amdgcn_alignbit(uc[j][0], pw_, 16), __builtin_amdgcn_alignbit(uc[j][1], uc[j][0], 16),
                                        __builtin_amdgcn_alignbit(uc[j][2], uc[j][1], 16), __builtin_amdgcn_alignbit(uc[j][3], uc[j][2], 16)});
        }
      }
      if (lane < 28) {
#pragma unroll
        for (int c = 0; c < 3; ++c) st16(lds + L_U + c * UCH + lw * UC1 + lane * 16, u32x4{0, 0, 0, 0});
      }
      flag_add(fb, FL_U, lane);
      if (lw) { kexpand(lds + L_KC, lds + L_PC, lane); flag_set(fb, FL_KC, 1); }
    } else {
      f32x4 acc[2][3][4];
#pragma unroll
      for (int m = 0; m < 2; ++m)
#pragma unroll
        for (int ii = 0; ii < 3; ++ii)
#pragma unroll
          for (int tb = 0; tb < 4; ++tb) { acc[m][ii][tb] = f32x4{0, 0, 0, 0}; asm volatile("" : "+v"(acc[m][ii][tb])); }
      if (!(EXP & 4)) __builtin_amdgcn_s_setprio(2);
      const int tw = wid >> 1, fw = wid & 1;
      lptr pB0 = lds + (tw * 64 + r) * 128 + ((q ^ (r >> 1)) << 4);
      lptr pB1 = lds + (tw * 64 + r) * 128 + (((q ^ (r >> 1)) ^ 4) << 4);
      const int sg = (q & 1) * 32;
      lptr pAb = lds + 16384 + (8 * q + (r >> 2)) * 192 + 8 * (lane & 3);
      lptr pA[3] = {pAb + (fw ? 160 - sg : sg), pAb + (fw ? 128 + sg : 32 - sg), pAb + (fw ? 96 - sg : 64 + sg)};
      u32x4 A0[2][3], B0[4], A1[2][3], B1[4];
      auto rd = [&](u32x4 (&A)[2][3], u32x4 (&B)[4], int off, int kk) {
#pragma unroll
        for (int tb = 0; tb < 4; ++tb) B[tb] = ld16((kk ? pB1 : pB0) + off + tb * 2048);
#pragma unroll
        for (int m = 0; m < 2; ++m)
#pragma unroll
          for (int ii = 0; ii < 3; ++ii) A[m][ii] = tr2(pA[ii] + off + m * 12288 + kk * 6144, 768);
      };
      auto mm = [&](u32x4 (&A)[2][3], u32x4 (&B)[4]) {
#pragma unroll
        for (int e = 0; e < 24; ++e) { const int m = e / 12, ii = (e / 4) % 3, tb = e % 4; acc[m][ii][tb] = MFMA_BF(A[m][ii], B[tb], acc[m][ii][tb]); }
      };
      auto sched = [&]() {
#pragma unroll
        for (int k = 0; k < 16; ++k) { SGB(0x8, 1); SGB(0x100, 1); }
        SGB(0x8, 8);
      };
      BAR_L();
      STAMP(2);
      rd(A0, B0, SL0, 0);
      int s0 = SL0, s1 = SL1, s2 = SL2;
#pragma unroll 1
      for (int s = 0; s < 9; ++s) {
        SBAR();
        rd(A1, B1, s0, 1); mm(A0, B0); sched();
        BAR_L();
        rd(A0, B0, s1, 0); mm(A1, B1); sched();
        SBAR();
        const int t = s0; s0 = s1; s1 = s2; s2 = t;
      }
      rd(A1, B1, SL0, 1); mm(A0, B0); sched();
      SBAR();
      flag_add(fb, FL_G1, lane);
      mm(A1, B1);
      SBAR();
      if (EXP & 64) __builtin_amdgcn_s_setprio(0);
      STAMP(3);
      unsigned bo[4], ao[4];
#pragma unroll
      for (int c = 0; c < 4; ++c) { const int cc = wid * 4 + ((c + nb) & 3); bo[c] = WS_WD + (nb * 48 + cc * 3) * 5120; ao[c] = WS_H + (tg * 16 + cc) * 12288; }
      const unsigned l16 = lane * 16;
#define BFRAG(k, n) __builtin_amdgcn_raw_buffer_load_b128(rs, l16, bo[(k) < 8 ? (k) >> 1 : (k) - 8] + ((k) < 8 ? ((k) & 1) * 5120 : 10240) + (n) * 1024, SC1)
#define AFRAG(k, t) __builtin_amdgcn_raw_buffer_load_b128(rs, l16, ao[(k) < 8 ? (k) >> 1 : (k) - 8] + ((k) < 8 ? ((k) & 1) * 4096 : 8192) + (t) * 1024, (EXP & 8) ? 0 : SC1)
      u32x4 tp[20];
      flag_wait(fb, FL_EPI, 1);
      tag = uni64(*(volatile LAS u64*)(lds + L_FLG + FL_TAG * 4));
      STAMP(4);
      {
        float nmb[4], al[4], sd[4];
#pragma unroll
        for (int tb = 0; tb < 4; ++tb) { const f32x2 st = *(LAS f32x2*)(lds + L_STAT + (tw * 64 + tb * 16 + r) * 8); nmb[tb] = -st.x; sd[tb] = st.y; al[tb] = -1.44269504f * __builtin_amdgcn_rcpf(st.y); }
        const unsigned hoff = WS_H + ((rb * 2 + tw) * 16 + cb) * 12288 + lane * 16;
        auto hcalc = [&](const f32x4& g4, const f32x4& u4, const f32x4& cg, const f32x4& cu, int tb) {
          float hv[4];
#pragma unroll
          for (int j = 0; j < 4; ++j) {
            const float g = __builtin_fmaf(nmb[tb], cg[j], g4[j]) * al[tb];
            const float u = __builtin_fmaf(nmb[tb], cu[j], u4[j]);
            const float e = __builtin_amdgcn_exp2f(g);
            hv[j] = u * __builtin_amdgcn_rcpf(__builtin_fmaf(e, sd[tb], sd[tb]));
          }
          return u32x2{pkbf(hv[0], hv[1]), pkbf(hv[2], hv[3])};
        };
        lptr pcs = lds + L_CS + (fw * 48 + 4 * q) * 4;
        const int oa = fw ? 128 : 0, oc = fw ? 0 : 128;
        {
          const f32x4 cga = *(LAS f32x4*)(pcs + oa), cua = *(LAS f32x4*)(pcs + 384 + oa), cgb = *(LAS f32x4*)(pcs + 64), cub = *(LAS f32x4*)(pcs + 384 + 64);
#pragma unroll
          for (int tb = 0; tb < 4; ++tb) {
            tp[4 * tb] = BFRAG(8 + (4 * tb) / 5, (4 * tb) % 5); tp[4 * tb + 1] = BFRAG(8 + (4 * tb + 1) / 5, (4 * tb + 1) % 5);
            const u32x2 ha = hcalc(acc[0][0][tb], acc[1][0][tb], cga, cua, tb);
            SGB(0x20, 1); SGB(0x6, 18); SGB(0x20, 1); SBAR();
            tp[4 * tb + 2] = BFRAG(8 + (4 * tb + 2) / 5, (4 * tb + 2) % 5); tp[4 * tb + 3] = BFRAG(8 + (4 * tb + 3) / 5, (4 * tb + 3) % 5);
            const u32x2 hb = hcalc(acc[0][1][tb], acc[1][1][tb], cgb, cub, tb);
            SGB(0x20, 1); SGB(0x6, 18); SGB(0x20, 1); SBAR();
            ST_SC1((u32x4{ha[0], ha[1], hb[0], hb[1]}), hoff + fw * 4096 + tb * 1024);
            SBAR();
          }
        }
        {
          const f32x4 cg = *(LAS f32x4*)(pcs + oc), cu = *(LAS f32x4*)(pcs + 384 + oc);
#pragma unroll
          for (int t2 = 0; t2 < 2; ++t2) {
            tp[16 + 2 * t2] = BFRAG(11, 1 + 2 * t2);
            const u32x2 ha = hcalc(acc[0][2][2 * t2], acc[1][2][2 * t2], cg, cu, 2 * t2);
            SBAR();
            tp[17 + 2 * t2] = BFRAG(11, 2 + 2 * t2);
            const u32x2 hb = hcalc(acc[0][2][2 * t2 + 1], acc[1][2][2 * t2 + 1], cg, cu, 2 * t2 + 1);
            SBAR();
            ST_SC1((u32x4{ha[0], ha[1], hb[0], hb[1]}), hoff + 8192 + fw * 2048 + t2 * 1024);
            SBAR();
          }
        }
      }
      if (EXP & 128) __builtin_amdgcn_s_setprio(0);
      STAMP(5);
      lptr park = lds + SL2 + wid * 20480 + lane * 16;
      waitv<6>();
      if (lane < 8) __hip_atomic_store((GAS u64*)(ws + WS_MB + (rb * 16 + tw * 8 + lane) * MBS + cb * 16 + fw * 8), tag, RLX_AGENT);
      flag_wait(fb, FL_G1, 4);
#pragma unroll
      for (int j = 0; j < 16; ++j) st16(park + j * 1024, tp[j]);
      SBAR();
      waitv<0>();
      if (lane < 8) __hip_atomic_store((GAS u64*)(ws + WS_MB + (rb * 16 + tw * 8 + lane) * MBS + 256 + cb * 16 + fw * 8), tag, RLX_AGENT);
#pragma unroll
      for (int j = 16; j < 20; ++j) st16(park + j * 1024, tp[j]);
      STAMP(6);
      u32x4 Bw[4][5];
#pragma unroll
      for (int k = 0; k < 4; ++k)
#pragma unroll
        for (int n = 0; n < 5; ++n) Bw[k][n] = BFRAG(k, n);
      STAMP(7);
      const unsigned fo = WS_MB + (tg * 8 + nb) * MBS + (wid * 8 + (lane & 7)) * 8;
      _Pragma("nounroll") for (int sp = 0; sp < SPIN_GLB; ++sp) { if (wall(gflag_ok(ws, fo, tag))) break; __builtin_amdgcn_s_sleep(2); }
      asm volatile("" ::: "memory");
      if (EXP & 1024) __builtin_amdgcn_s_sleep(127);
      STAMP(8);
      f32x4 a2[5][4];
#pragma unroll
      for (int n = 0; n < 5; ++n)
#pragma unroll
        for (int tb = 0; tb < 4; ++tb) a2[n][tb] = f32x4{0, 0, 0, 0};
      u32x4 Ah[4][4];
#pragma unroll
      for (int k = 0; k < 3; ++k)
#pragma unroll
        for (int tb = 0; tb < 4; ++tb) Ah[k][tb] = AFRAG(k, tb);
      u64 f2 = 0;
      SBAR();
#pragma unroll
      for (int k = 0; k < 12; ++k) {
        if (k == 3) { f2 = __hip_atomic_load((GAS u64*)(ws + fo + 256), RLX_AGENT); SBAR(); }
        if (k == 5) {
          if (!wall(f2 == tag)) { _Pragma("nounroll") for (int sp = 0; sp < SPIN_GLB; ++sp) { if (wall(gflag_ok(ws, fo + 256, tag))) break; __builtin_amdgcn_s_sleep(2); } }
          asm volatile("" ::: "memory");
        }
        u32x4 hf[4];
        if (k >= 8) {
          const u32x4 X0 = Ah[k & 3][0], X1 = Ah[k & 3][1], Y0 = Ah[k & 3][2], Y1 = Ah[k & 3][3];
          hf[0] = u32x4{X0[0], X0[1], Y0[0], Y0[1]}; hf[1] = u32x4{X0[2], X0[3], Y0[2], Y0[3]};
          hf[2] = u32x4{X1[0], X1[1], Y1[0], Y1[1]}; hf[3] = u32x4{X1[2], X1[3], Y1[2], Y1[3]};
        } else {
#pragma unroll
          for (int tb = 0; tb < 4; ++tb) hf[tb] = Ah[k & 3][tb];
        }
#pragma unroll
        for (int e = 0; e < 20; ++e) {
          const int n = e >> 2, tb = e & 3;
          a2[n][tb] = MFMA_BF(Bw[k & 3][n], hf[tb], a2[n][tb]);
          if (e & 1) {
            const int j = e >> 1;
            if (j < 4) { if (k + 3 < 12) Ah[(k + 3) & 3][j] = AFRAG(k + 3, j); }
            else if (j < 9 && k >= 1 && k + 3 < 12) {
              if (k + 3 < 8) { if (!(EXP & 16)) Bw[(k + 3) & 3][j - 4] = BFRAG(k + 3, j - 4); }
              else Bw[(k + 3) & 3][j - 4] = ld16(park + ((k - 5) * 5 + j - 4) * 1024);
            }
            SBAR();
          }
        }
#pragma unroll
        for (int n = 0; n < 5; ++n) asm volatile("" : "+v"(a2[n][0]), "+v"(a2[n][1]), "+v"(a2[n][2]), "+v"(a2[n][3]));
        if ((EXP & 512) && k < 11) ARR();
      }
      STAMP(9);
      {
        lptr red = lds + SL2 + wid * 20480;
        const int l2 = fresh_lane(), r = l2 & 15, q = l2 >> 4;
#pragma unroll
        for (int n = 0; n < 5; ++n)
#pragma unroll
          for (int tb = 0; tb < 4; ++tb) {
            *(LAS f32x4*)(red + (4 * n + q) * 1024 + ((tb * 16 + r + 3 * n) & 63) * 16) = a2[n][tb];
          }
      }
      __builtin_amdgcn_s_setprio(0);
    }
  }

  if (wid >= 4 && !(EXP & 1)) {
    const int sw = wid - 4;
    const int lane = fresh_lane(), r = lane & 15, q = lane >> 4;
    if (EXP & 256) __builtin_amdgcn_s_setprio(3);
    const int hm = sw & 1;
    lptr am = lds + SA + hm * PCH;
    f32x4 ca[8];
#pragma unroll
    for (int t = 0; t < 8; ++t) ca[t] = f32x4{0, 0, 0, 0};
    lptr pmain = am + O_KL + 1312 + lane * 16;
    STAMP(4);
    if (sw < 2) flag_wait(fb, FL_KA + hm, 1);
    flag_wait(fb, FL_U, 2);
    STAMP(5);
    conv_units<0xff>(am + O_KREV, lds + L_U + (hm + odd) * UCH, lane, sw < 2 ? 5 : 0, sw < 2 ? 3 : 5, ca);
    if (sw < 2) {
      lptr pc = lds + L_PC;
#pragma unroll
      for (int t = 0; t < 8; ++t) { *(LAS f32x4*)(pmain + t * 1024) = ca[t]; ca[t] = f32x4{0, 0, 0, 0}; }
      flag_set(fb, FL_PA + hm, 1);
      flag_wait(fb, FL_KC, 1);
      lptr uc_ = lds + L_U + (odd ? 0 : 2) * UCH;
      if (odd) conv_units<0x3a>(lds + L_KC, uc_, lane, hm * 4, 4, ca);
      else conv_units<0xc5>(lds + L_KC, uc_, lane, hm * 4, 4, ca);
      __builtin_amdgcn_s_setprio(3);
      STAMP(9);
      f32x4 c4[4];
      c4[0] = odd ? ca[1] : ca[0]; c4[1] = odd ? ca[3] : ca[2]; c4[2] = odd ? ca[4] : ca[6]; c4[3] = odd ? ca[5] : ca[7];
      if (!hm) {
#pragma unroll
        for (int t = 0; t < 4; ++t) *(LAS f32x4*)(pc + t * 1024 + lane * 16) = c4[t];
        flag_set(fb, FL_PC, 1);
      } else {
        flag_wait(fb, FL_PC, 1);
#pragma unroll
        for (int t = 0; t < 4; ++t) c4[t] += *(LAS f32x4*)(pc + t * 1024 + lane * 16);
        waitl();
#pragma unroll
        for (int t = 0; t < 4; ++t) { *(LAS unsigned*)(pc + t * 512 + q * 128 + r * 4) = pkbf(c4[t][0], c4[t][1]); *(LAS unsigned*)(pc + t * 512 + q * 128 + r * 4 + 64) = pkbf(c4[t][2], c4[t][3]); }
#pragma unroll
        for (int i = 0; i < 2; ++i) {
          const int s4 = 2 * i + (lane >> 5);
          const int T = odd ? (s4 == 0 ? 1 : s4 + 2) : (s4 < 2 ? 2 * s4 : s4 + 4);
          ST_SC1(ld16(pc + i * 1024 + lane * 16), WS_Y + ((T * 4 + ((lane >> 3) & 3)) * DM + chC) * 128 + (lane & 7) * 16);
        }
      }
    } else {
      __builtin_amdgcn_s_setprio(3);
      STAMP(9);
      flag_wait(fb, FL_PA + hm, 1);
#pragma unroll
      for (int t = 0; t < 8; ++t) ca[t] += *(LAS f32x4*)(pmain + t * 1024);
#pragma unroll
      for (int t = 0; t < 8; ++t) { *(LAS unsigned*)(am + t * 512 + q * 128 + r * 4) = pkbf(ca[t][0], ca[t][1]); *(LAS unsigned*)(am + t * 512 + q * 128 + r * 4 + 64) = pkbf(ca[t][2], ca[t][3]); }
#pragma unroll
      for (int i = 0; i < 4; ++i) ST_SC1(ld16(am + i * 1024 + lane * 16), WS_Y + ((i * 8 + (lane >> 3)) * DM + chA + hm) * 128 + (lane & 7) * 16);
    }
    STAMP(6);
    waitv<0>();
    if (flag_add(fb, FL_YC, lane) == 3u) {
      if (lane < 32) __hip_atomic_store((GAS u64*)(ws + WS_MB + (lane * 8 + xcd) * MBS + 512 + bi * 8), tag, RLX_AGENT);
      _Pragma("nounroll") for (int sp = 0; sp < SPIN_GLB; ++sp) { if (wall(gflag_ok(ws, WS_MB + (tg * 8 + nb) * MBS + 512 + (lane & 31) * 8, tag))) break; __builtin_amdgcn_s_sleep(4); }
      flag_set(fb, FL_YOK, 1);
    }
    STAMP(7);
    flag_wait(fb, FL_YOK, 1);
    STAMP(8);
    u32x4 yv[3];
#pragma unroll
    for (int j = 0; j < 3; ++j) {
      const int p = umin(sw + 4 * j, 9), c = p * 64 + lane, row = (c * 6554) >> 16;
      glds1((const char*)x + (size_t)tg * 64 * 1280 + nb * 160, row * 1280 + (c - row * 10) * 16, lds0 + L_XT + p * 1024);
    }
#pragma unroll
    for (int j = 0; j < 3; ++j) { const int p = umin(sw + 4 * j, 9); yv[j] = LD_SC1(WS_Y + (tg * DM + nb * 80 + p * 8) * 128 + lane * 16); }
    lptr yb = lds + L_YB + sw * 3072;
#pragma unroll
    for (int j = 0; j < 3; ++j) st16(yb + j * 1024 + lane * 16, yv[j]);
#pragma unroll
    for (int j = 0; j < 3; ++j) {
      const u32x4 a = tr2(yb + j * 1024 + (r >> 2) * 128 + q * 32 + (lane & 3) * 8, 512);
      if (sw + 4 * j < 10) st16(lds + L_BASE + lane * 160 + (sw + 4 * j) * 16, a);
    }
  }
  STAMP(10);
  BAR_L();
  STAMP(11);
  if (wid < 5) {
    const int tid = wid * 64 + fresh_lane();
    const int tok = (tid * 13108) >> 16, g = tid - tok * 5;
    const int o = g * 4096 + ((tok + 3 * g) & 63) * 16;
    f32x4 s[4];
#pragma unroll
    for (int m = 0; m < 4; ++m)
      s[m] = (*(LAS f32x4*)(lds + SL0 + o + m * 1024) + *(LAS f32x4*)(lds + SL0 + 20480 + o + m * 1024)) +
             (*(LAS f32x4*)(lds + SL2 + o + m * 1024) + *(LAS f32x4*)(lds + SL2 + 20480 + o + m * 1024));
    u16* op = out + (size_t)(tg * 64 + tok) * DM + nb * 80 + g * 16;
#pragma unroll
    for (int hh = 0; hh < 2; ++hh) {
      const u32x4 bs = ld16(lds + L_BASE + tid * 32 + hh * 16), xs = ld16(lds + L_XT + tid * 32 + hh * 16);
      const f32x4 sa = s[2 * hh], sb = s[2 * hh + 1];
      *(u32x4*)(op + hh * 8) = u32x4{pkbf((bflo(xs[0]) + bflo(bs[0])) + sa[0], (bfhi(xs[0]) + bfhi(bs[0])) + sa[1]), pkbf((bflo(xs[1]) + bflo(bs[1])) + sa[2], (bfhi(xs[1]) + bfhi(bs[1])) + sa[3]),
                                     pkbf((bflo(xs[2]) + bflo(bs[2])) + sb[0], (bfhi(xs[2]) + bfhi(bs[2])) + sb[1]), pkbf((bflo(xs[3]) + bflo(bs[3])) + sb[2], (bfhi(xs[3]) + bfhi(bs[3])) + sb[3])};
    }
  }
}

#if defined(__HIP_DEVICE_COMPILE__)
#pragma clang attribute pop
#endif

extern "C" void kernel_launch(void* const* d_in, const int* in_sizes, int n_in, void* d_out, int out_size, void* d_ws, size_t ws_size, hipStream_t stream) {
  (void)in_sizes; (void)n_in; (void)out_size; (void)ws_size;
  (void)hipFuncSetAttribute((const void*)fused_kernel, hipFuncAttributeMaxDynamicSharedMemorySize, LDS_BYTES);
  fused_kernel<<<256, 512, LDS_BYTES, stream>>>((const u16*)d_in[0], (const u16*)d_in[1], (const u16*)d_in[2], (const u16*)d_in[3], (const u16*)d_in[4],
                                                (const u16*)d_in[5], (const u16*)d_in[6], (const u16*)d_in[8], (const u16*)d_in[9], (const u16*)d_in[10],
                                                (u16*)d_out, (char*)d_ws);
}
```

```cpp
#include <hip/hip_runtime.h>
#include <stdint.h>

#if defined(__HIP_DEVICE_COMPILE__)
#pragma clang attribute push(__attribute__((target("no-packed-fp32-ops"))), apply_to = function)
#endif
typedef unsigned short u16;
typedef unsigned long long u64;
typedef short bf16x8 __attribute__((ext_vector_type(8)));
typedef _Float16 f16x8 __attribute__((ext_vector_type(8)));
typedef short s16x4 __attribute__((ext_vector_type(4)));
typedef float f32x2 __attribute__((ext_vector_type(2)));
typedef float f32x4 __attribute__((ext_vector_type(4)));
typedef unsigned u32x2 __attribute__((ext_vector_type(2)));
typedef unsigned u32x4 __attribute__((ext_vector_type(4)));
typedef _Float16 h16x2 __attribute__((ext_vector_type(2)));
typedef __bf16 b16x2 __attribute__((ext_vector_type(2)));
#define LAS __attribute__((address_space(3)))
#define GAS __attribute__((address_space(1)))
typedef LAS char* lptr;
#define DI __device__ __forceinline__
#define SBAR() __builtin_amdgcn_sched_barrier(0)
#define SGB(m, n) __builtin_amdgcn_sched_group_barrier(m, n, 0)
#define MFMA_BF(a, b, c) __builtin_amdgcn_mfma_f32_16x16x32_bf16(__builtin_bit_cast(bf16x8, a), __builtin_bit_cast(bf16x8, b), c, 0, 0, 0)
#define MFMA_H(a, b, c) __builtin_amdgcn_mfma_f32_16x16x32_f16(__builtin_bit_cast(f16x8, a), __builtin_bit_cast(f16x8, b), c, 0, 0, 0)
#define SC1 16
#define HSPLAN 0, 3, 6, 9, 12, 15, 18, 21, 24
#define PACE 2
#define EXP 0

constexpr int DM = 640, NM = 192;
constexpr unsigned WS_EPOCH = 0, WS_F0 = 4096, F0REP = 4352;
constexpr unsigned WS_MB = 262144, MBS = 1280;
constexpr unsigned WS_ST = 65536, WS_CS = 98304, WS_XB = 655360, WS_WD = WS_XB + 2621440 + 4096;
constexpr unsigned WS_H = WS_WD + 1966080, WS_Y = WS_H + 6291456, WS_END = WS_Y + 2621440;
constexpr unsigned WS_STAMP = 16u << 20;
constexpr int STAGE = 40960, SL1 = 0, SA = 40960, SL2 = 81920, SL0 = 122880, POOL_BIAS = SA;
constexpr int PROW = 784, PCH = 23 * PROW;
constexpr int KC1 = 4228, O_KREV = 128, O_KL = 8528;
constexpr int L_FLG = SA + 2 * PCH, L_SC = L_FLG + 128, L_STG = L_FLG + 192, STGW = 2304;
constexpr int L_STAT = L_STG, L_CS = L_STG + 1024;
constexpr int L_RTB = SL2 + 29696;
constexpr int L_LNT = L_STG;
constexpr int L_LNB = SL2 + 18432, LNR = 1344;
constexpr int UCH = 9232, UC1 = 4640;
constexpr int L_U = SL1 + 128, L_KC = L_U + 3 * UCH + 128, L_PC = L_KC + 8400;
constexpr int L_XT = SL1;
constexpr int L_BASE = SA, L_YB = SA + 10240;
constexpr int LDS_BYTES = 163840;
static_assert(O_KL + 1312 + 8192 <= PCH && L_LNB + 8 * LNR <= L_RTB && L_RTB + 10240 + 128 <= SL0 && L_PC + 4096 <= SA && L_STG + 2 * STGW <= SL0 && (L_KC & 15) == 0 && (L_PC & 15) == 0 && (L_STG & 15) == 0, "lds map");
enum { FL_F0 = 0, FL_EPI, FL_U, FL_KA, FL_KB, FL_KC, FL_KP, FL_PA, FL_PB, FL_PC, FL_YC, FL_YOK, FL_H1, FL_H2, FL_G1, FL_S2, FL_TAG = 16 };
constexpr int SPIN_LDS = 1 << 18, SPIN_GLB = 1 << 15;

DI bool wall(bool p) { return __builtin_amdgcn_ballot_w64(!p) == 0; }
DI unsigned umin(unsigned a, unsigned b) { return a < b ? a : b; }
DI unsigned umax(unsigned a, unsigned b) { return a > b ? a : b; }
DI float asf(unsigned v) { return __builtin_bit_cast(float, v); }
DI unsigned asu(float v) { return __builtin_bit_cast(unsigned, v); }
DI float bflo(unsigned v) { return asf(v << 16); }
DI float bfhi(unsigned v) { return asf(v & 0xffff0000u); }
DI float bf1(u16 v) { return asf((unsigned)v << 16); }
DI unsigned pkbf(float lo, float hi) { return __builtin_bit_cast(unsigned, __builtin_convertvector(f32x2{lo, hi}, b16x2)); }
DI unsigned pkh(f32x2 v) { return __builtin_bit_cast(unsigned, __builtin_convertvector(v, h16x2)); }
DI float rbf(float x) { return bflo(pkbf(x, x)); }
template <int CTRL> DI float dppf(float x) { return __builtin_bit_cast(float, __builtin_amdgcn_mov_dpp(__builtin_bit_cast(int, x), CTRL, 0xf, 0xf, true)); }
DI float row16_sum(float v) { v += dppf<0xB1>(v); v += dppf<0x4E>(v); v += dppf<0x124>(v); v += dppf<0x128>(v); return v; }

DI void glds4(const void* sb, unsigned v0, unsigned v1, unsigned v2, unsigned v3, unsigned lds) {
  asm volatile("s_mov_b32 m0, %5\n\ts_nop 0\n\tglobal_load_lds_dwordx4 %0, %4\n\tglobal_load_lds_dwordx4 %1, %4 offset:1024\n\t"
               "global_load_lds_dwordx4 %2, %4 offset:2048\n\tglobal_load_lds_dwordx4 %3, %4 offset:3072"
               :: "v"(v0), "v"(v1), "v"(v2), "v"(v3), "s"(sb), "s"(lds) : "memory");
}
DI void glds1(const void* sb, unsigned v0, unsigned lds) {
  asm volatile("s_mov_b32 m0, %2\n\ts_nop 0\n\tglobal_load_lds_dwordx4 %0, %1" :: "v"(v0), "s"(sb), "s"(lds) : "memory");
}
DI void glds2(const void* sb, unsigned v0, unsigned v1, unsigned lds) {
  asm volatile("s_mov_b32 m0, %3\n\ts_nop 0\n\tglobal_load_lds_dwordx4 %0, %2\n\tglobal_load_lds_dwordx4 %1, %2 offset:1024"
               :: "v"(v0), "v"(v1), "s"(sb), "s"(lds) : "memory");
}
template <int N> DI void waitv() { asm volatile("s_waitcnt vmcnt(%0)" :: "i"(N) : "memory"); }
DI void waitl() { asm volatile("s_waitcnt lgkmcnt(0)" ::: "memory"); }
DI u32x2 vtr(lptr p) { return __builtin_bit_cast(u32x2, __builtin_amdgcn_ds_read_tr16_b64_v4i16((LAS s16x4*)p)); }
DI u32x4 tr2(lptr p, int d) { u32x2 lo = vtr(p), hi = vtr(p + d); return u32x4{lo[0], lo[1], hi[0], hi[1]}; }
DI u32x4 ld16(lptr p) { return *(LAS u32x4*)p; }
DI void st16(lptr p, u32x4 v) { *(LAS u32x4*)p = v; }

DI unsigned flag_ld(unsigned addr) { unsigned r; asm volatile("ds_read_b32 %0, %1\n\ts_waitcnt lgkmcnt(0)" : "=v"(r) : "v"(addr) : "memory"); return __builtin_amdgcn_readfirstlane(r); }
DI void flag_wait(unsigned fb, int idx, unsigned need) { _Pragma("nounroll") for (int sp = 0; sp < SPIN_LDS && flag_ld(fb + idx * 4) < need; ++sp) __builtin_amdgcn_s_sleep(1); }
DI void flag_set(unsigned fb, int idx, unsigned v) {
  asm volatile("s_waitcnt lgkmcnt(0)\n\ts_mov_b64 exec, 1\n\tds_write_b32 %0, %1\n\ts_mov_b64 exec, -1" :: "v"(fb + idx * 4), "v"(v) : "memory"); }
DI unsigned flag_add(unsigned fb, int idx, int lane) {
  unsigned r; (void)lane;
  asm volatile("s_waitcnt lgkmcnt(0)\n\ts_mov_b64 exec, 1\n\tds_add_rtn_u32 %0, %1, %2\n\ts_mov_b64 exec, -1\n\ts_waitcnt lgkmcnt(0)" : "=&v"(r) : "v"(fb + idx * 4), "v"(1u) : "memory");
  return __builtin_amdgcn_readfirstlane(r);
}
#define RLX_AGENT __ATOMIC_RELAXED, __HIP_MEMORY_SCOPE_AGENT
DI bool gflag_ok(const char* ws, unsigned off, u64 tag) { return __hip_atomic_load((GAS u64*)(ws + off), RLX_AGENT) == tag; }

#define STAMP(i) do {} while (0)

DI f32x2 csq(f32x2 x) { const float t = x.x + x.x; return f32x2{__builtin_fmaf(x.x, x.x, -(x.y * x.y)), t * x.y}; }
DI h16x2 toh(f32x2 v) { return __builtin_convertvector(v, h16x2); }
DI h16x2 hmul(h16x2 x, h16x2 y) { return __builtin_elementwise_fma(h16x2{x.y, x.y}, h16x2{-y.y, y.x}, h16x2{x.x, x.x} * y); }
DI f32x2 cexp_p(float a2, float bh, float bl, float p) {
  const float e = __builtin_amdgcn_exp2f(a2 * p);
  const float ph = __builtin_fmaf(bl, p, __builtin_amdgcn_fractf(bh * p));
  return f32x2{e * __builtin_amdgcn_cosf(ph), e * __builtin_amdgcn_sinf(ph)};
}
template <int NS, int ROW0> DI void put4(const unsigned (&tb)[NS], const h16x2 (&a)[NS], const h16x2 (&b)[NS], const h16x2 (&c)[NS], const h16x2 (&d)[NS]) {
#pragma unroll
  for (int e = 0; e < NS; ++e)
    asm volatile("s_mov_b32 m0, %4\n\ts_nop 0\n\tds_write_addtid_b32 %0 offset:%5\n\tds_write_addtid_b32 %1 offset:%5+784\n\t"
                 "ds_write_addtid_b32 %2 offset:%5+1568\n\tds_write_addtid_b32 %3 offset:%5+2352"
                 :: "v"(a[e]), "v"(b[e]), "v"(c[e]), "v"(d[e]), "s"(tb[e]), "i"(ROW0 * PROW + POOL_BIAS) : "memory");
}
#define FORS _Pragma("unroll") for (int e = 0; e < NS; ++e)
template <int NS> DI void kpool(const unsigned (&tb)[NS], const float (&pv)[NS][4], const float (&ldt)[NS], bool join, float (&sc)[NS]) {
  float dt[NS];
  FORS dt[e] = rbf(__builtin_amdgcn_exp2f(ldt[e] * 1.44269504f));
  unsigned eb[NS]; float cs[NS];
  FORS {
    unsigned mi = umax(asu(pv[e][2]) & 0x7fffffffu, asu(pv[e][3]) & 0x7fffffffu);
    mi = umax(mi, asu(dppf<0xB1>(asf(mi)))); mi = umax(mi, asu(dppf<0x4E>(asf(mi)))); mi = umax(mi, asu(dppf<0x124>(asf(mi)))); mi = umax(mi, asu(dppf<0x128>(asf(mi))));
    eb[e] = umax(umax((unsigned)__builtin_amdgcn_readlane(mi, 0), (unsigned)__builtin_amdgcn_readlane(mi, 16)),
                 umax((unsigned)__builtin_amdgcn_readlane(mi, 32), (unsigned)__builtin_amdgcn_readlane(mi, 48))) >> 23;
  }
  if (join) eb[0] = eb[NS - 1] = umax(eb[0], eb[NS - 1]);
  FORS { eb[e] = umin(umax(eb[e], 2u), 252u); cs[e] = asf((254u - eb[e]) << 23); }
  float a2[NS], bh[NS], bl[NS];
  FORS {
    const float ea = rbf(__builtin_amdgcn_exp2f(pv[e][0] * 1.44269504f));
    a2[e] = -(ea * dt[e]) * 1.44269504f;
    const float b = pv[e][1] * dt[e];
    bh[e] = b * 0.15915494f;
    bl[e] = __builtin_fmaf(b, 6.4206382e-9f, __builtin_fmaf(b, 0.15915494f, -bh[e]));
  }
  h16x2 one[NS], w8[NS], w16[NS], w24[NS], w32[NS], w64[NS], w96[NS], w1[NS], w2[NS], w3[NS], w4[NS], C[NS], c4[NS];
  FORS {
    const f32x2 f1 = cexp_p(a2[e], bh[e], bl[e], 1.f), f2 = csq(f1), f4 = csq(f2);
    const f32x2 f8 = csq(f32x2{f4.x, -f4.y}), f16 = csq(f8), f32 = csq(f16), f64 = csq(f32);
    one[e] = h16x2{(_Float16)1.f, (_Float16)0.f};
    w8[e] = toh(f8); w16[e] = toh(f16); w32[e] = toh(f32); w64[e] = toh(f64); w24[e] = hmul(w8[e], w16[e]); w96[e] = hmul(w32[e], w64[e]);
    w1[e] = toh(f1); w2[e] = toh(f2); w4[e] = toh(f4); w3[e] = hmul(w1[e], w2[e]);
    C[e] = toh(f32x2{pv[e][2] * cs[e], pv[e][3] * cs[e]}); c4[e] = hmul(C[e], w4[e]);
  }
  put4<NS, 0>(tb, one, w8, w16, w24);
  put4<NS, 4>(tb, w32, w64, w96, w1);
  put4<NS, 8>(tb, w2, w3, C, c4);
  h16x2 c128[NS], c132[NS], w256[NS], w258[NS], w512[NS], w768[NS], w1024[NS], w1280[NS], w1536[NS], w1792[NS], w1796[NS];
  FORS {
    const f32x2 f128 = cexp_p(a2[e], bh[e], bl[e], 128.f), f256 = csq(f128), f512 = csq(f256);
    const h16x2 w128 = toh(f128);
    w256[e] = toh(f256); w512[e] = toh(f512); c128[e] = hmul(C[e], w128); c132[e] = hmul(c4[e], w128);
    w258[e] = hmul(w256[e], w2[e]); w768[e] = hmul(w512[e], w256[e]);
    w1024[e] = toh(csq(f512)); w1280[e] = hmul(w1024[e], w256[e]); w1536[e] = hmul(w1024[e], w512[e]);
    w1792[e] = hmul(w1024[e], w768[e]); w1796[e] = hmul(w1792[e], w4[e]);
  }
  put4<NS, 12>(tb, c128, c132, w256, w258);
  put4<NS, 16>(tb, w512, w768, w1024, w1280);
  put4<NS, 19>(tb, w1280, w1536, w1792, w1796);
  FORS sc[e] = 2.f * dt[e] * asf(eb[e] << 23);
}
struct KPtr {
  lptr pbx, pby, py, px0, px2;
  DI void init(lptr pool, int lane) {
    const int r = lane & 15, x = r & 3, y = r >> 2;
    lptr p = pool + (lane >> 4) * 16;
    pbx = p + x * PROW; pby = p + (y ? 3 + y : 0) * PROW; py = p + (10 + y) * PROW; px0 = p + (x ? 6 + x : 0) * PROW; px2 = p + (y == 3 ? 22 : 18 + x) * PROW;
  }
  DI void fetch(u32x4 (&v)[6], int o) const {
    v[0] = ld16(pbx + o); v[1] = ld16(pby + o); v[2] = ld16(py + o); v[3] = ld16(px0 + o); v[4] = ld16(pbx + 14 * PROW + o); v[5] = ld16(px2 + o);
  }
  DI void advance() { pbx += 128; pby += 128; py += 128; px0 += 128; px2 += 128; }
};
DI void kprod(const u32x4 (&v)[6], u32x4 (&o)[4]) {
  unsigned t[16];
  if (EXP & 2048) { SBAR(); __builtin_amdgcn_s_setprio(3); SBAR(); }
#pragma unroll
  for (int i = 0; i < 16; ++i) { const unsigned xe = v[(i >> 2) ? 2 + (i >> 2) : 0][i & 3], ye = v[(i >> 2) ? 2 : 1][i & 3];
    asm("v_pk_mul_f16 %0, %1, %2 op_sel_hi:[0,1]" : "=v"(t[i]) : "v"(xe), "v"(ye)); }
  SBAR();
#pragma unroll
  for (int i = 0; i < 16; ++i) { const unsigned xe = v[(i >> 2) ? 2 + (i >> 2) : 0][i & 3], ye = v[(i >> 2) ? 2 : 1][i & 3]; unsigned z;
    asm("v_pk_fma_f16 %0, %1, %2, %3 op_sel:[1,1,0] op_sel_hi:[1,0,1] neg_lo:[0,1,0]" : "=v"(z) : "v"(xe), "v"(ye), "v"(t[i])); o[i >> 2][i & 3] = z; }
  SBAR();
}
template <int H> DI void kprodh(const u32x4 (&v)[6], u32x4 (&o)[4]) {
  unsigned t[8];
#pragma unroll
  for (int i = 8 * H; i < 8 * H + 8; ++i) { const unsigned xe = v[(i >> 2) ? 2 + (i >> 2) : 0][i & 3], ye = v[(i >> 2) ? 2 : 1][i & 3];
    asm("v_pk_mul_f16 %0, %1, %2 op_sel_hi:[0,1]" : "=v"(t[i & 7]) : "v"(xe), "v"(ye)); }
  SBAR();
#pragma unroll
  for (int i = 8 * H; i < 8 * H + 8; ++i) { const unsigned xe = v[(i >> 2) ? 2 + (i >> 2) : 0][i & 3], ye = v[(i >> 2) ? 2 : 1][i & 3]; unsigned z;
    asm("v_pk_fma_f16 %0, %1, %2, %3 op_sel:[1,1,0] op_sel_hi:[1,0,1] neg_lo:[0,1,0]" : "=v"(z) : "v"(xe), "v"(ye), "v"(t[i & 7])); o[i >> 2][i & 3] = z; }
  asm volatile("" : "+v"(o[2 * H]), "+v"(o[2 * H + 1]));
  SBAR();
}
DI void kmma1(const u32x4 (&o0)[4], f32x4 (&acc)[3], bool first) {
  SBAR(); asm volatile("s_setprio 3\n\ts_nop 1"); SBAR();
#pragma unroll
  for (int i = 0; i < 3; ++i) acc[i] = MFMA_H(o0[1 + i], o0[0], (first ? f32x4{0, 0, 0, 0} : acc[i]));
  SBAR(); __builtin_amdgcn_s_setprio(0); SBAR();
}
DI void kcombine(f32x4 (&ko)[3], const f32x4 (&ka)[3][3], float s0, float s1, float s2) {
#pragma unroll
  for (int i = 0; i < 3; ++i)
#pragma unroll
    for (int j = 0; j < 4; ++j) ko[i][j] = __builtin_fmaf(ka[2][i][j], s2, __builtin_fmaf(ka[1][i][j], s1, ka[0][i][j] * s0));
}
DI float ubf(const u16* p, int i) { const unsigned w = ((const __attribute__((address_space(4))) unsigned*)(uintptr_t)p)[i >> 1]; return (i & 1) ? bfhi(w) : bflo(w); }
DI void kmma2(const u32x4 (&o0)[4], const u32x4 (&o1)[4], f32x4 (&acc)[3]) {
  SBAR(); asm volatile("s_setprio 3\n\ts_nop 1"); SBAR();
#pragma unroll
  for (int i = 0; i < 3; ++i) acc[i] = MFMA_H(o0[1 + i], o0[0], acc[i]);
#pragma unroll
  for (int i = 0; i < 3; ++i) acc[i] = MFMA_H(o1[1 + i], o1[0], acc[i]);
  SBAR(); __builtin_amdgcn_s_setprio(0); SBAR();
}
DI void kstore(lptr kl, int lane, const f32x4 (&acc)[3], float dsk) {
  const int c = lane & 15, q = lane >> 4;
  LAS u16* t = (LAS u16*)kl;
  const float k0 = acc[0][0] + (lane == 0 ? dsk : 0.f);
  *(LAS u32x2*)(t + 128 * (q >> 1) + 8 * c + 4 * (q & 1)) = u32x2{pkbf(k0, acc[0][1]), pkbf(acc[0][2], acc[0][3])};
  *(LAS unsigned*)(t + 256 + 64 * (q >> 1) + 2 * (q & 1) + 4 * c) = pkbf(acc[1][0], acc[1][1]);
  const unsigned p1 = pkbf(acc[1][2], acc[1][3]);
  const int i1 = 384 + 32 * (q >> 1) + (q & 1) + 2 * c;
  t[i1] = (u16)p1; t[i1 + 64] = (u16)(p1 >> 16);
  const unsigned r0 = pkbf(acc[2][0], acc[2][1]), r1 = pkbf(acc[2][2], acc[2][3]);
  if (!(q & 1)) { const int i2 = 512 + 8 * q + c; t[i2] = (u16)r0; t[i2 + 32] = (u16)(r0 >> 16); t[i2 + 64] = (u16)r1; t[i2 + 96] = (u16)(r1 >> 16); }
  else if (lane == 63) t[640] = (u16)r0;
}
DI unsigned dup16(unsigned a) { return a | (a << 16); }
DI unsigned bsel(unsigned m, unsigned a, unsigned b) { asm("" : "+v"(m)); return (a & m) | (b & ~m); }
DI unsigned rot16(unsigned a) { return __builtin_amdgcn_alignbit(a, a, 16); }
DI void kexpand(lptr kr, lptr kl, int lane) {
  LAS u16* t = (LAS u16*)kl;
  auto put = [&](int ch, unsigned dp, unsigned d0, unsigned d1, unsigned d2, unsigned d3) {
    st16(kr + 16 * ch, u32x4{d0, d1, d2, d3}); st16(kr + KC1 - 4 + 16 * ch, u32x4{dp, d0, d1, d2}); };
  const int n = 63 - lane, n0 = n & 31;
  const unsigned a0 = t[576 + n], b0 = t[577 + n], a1 = t[512 + n], b1 = t[513 + n];
  const unsigned ab = *(LAS unsigned*)(t + 384 + 2 * n), c2 = t[386 + 2 * n];
  const u32x2 v1 = *(LAS u32x2*)(t + 256 + 4 * n0); const unsigned q4 = t[260 + 4 * n0];
  const u32x4 v0 = ld16((lptr)(t + 8 * n0)); const unsigned w = *(LAS unsigned*)(t + 8 * n0 + 8);
  const unsigned mo = (n & 1) ? ~0u : 0u, lo = lane >= 32 ? ~0u : 0u, m31 = n == 31 ? ~0u : 0u;
  put(lane, dup16(b0), dup16(b0), bsel(mo, dup16(b0), b0 | (a0 << 16)), dup16(a0), dup16(a0));
  put(64 + lane, dup16(b1), dup16(b1), bsel(mo, dup16(b1), b1 | (a1 << 16)), dup16(a1), dup16(a1));
  put(128 + lane, dup16(c2), dup16(c2), dup16(ab >> 16), rot16(ab), dup16(ab & 0xffffu));
  put(192 + lane, bsel(lo, bsel(m31, dup16(w & 0xffffu), rot16(w)), dup16(q4)), bsel(lo, rot16(v0[3]), q4 | (v1[1] & 0xffff0000u)),
      bsel(lo, rot16(v0[2]), dup16(v1[1] & 0xffffu)), bsel(lo, rot16(v0[1]), (v1[1] & 0xffffu) | (v1[0] & 0xffff0000u)), bsel(lo, rot16(v0[0]), dup16(v1[0] & 0xffffu)));
  if (lane >= 60) put(319 - lane, lane == 63 ? rot16(v0[0]) : 0u, 0u, 0u, 0u, 0u);
}

struct ConvPlan { int n; int item[36][3]; };
constexpr ConvPlan conv_plan(int tmask, int maxt) {
  ConvPlan p{};
  int fifo[64] = {}, head = 0, tail = 0, carry[16] = {}, nc = 0, per = (tmask == 0xff) ? 1 : 2;
  for (int pass = 0; pass < 2; ++pass) {
    head = tail = 0;
    for (int i = 0; i < nc; ++i) fifo[tail++] = carry[i] + 32;
    int e = 0;
    for (int m = maxt; m >= 0; --m) {
      for (int j = 0; j + m <= maxt; ++j) {
        const bool on = (tmask >> (m + j)) & 1;
        int k = 0;
        if (m == 0) { if (on && j < maxt - 1) p.item[e][k++] = j; else fifo[tail++] = j; }
        if (j + m == maxt) { fifo[tail++] = 8 + 2 * m; fifo[tail++] = 9 + 2 * m; }
        if (on) { while (k < per && head < tail) p.item[e][k++] = fifo[head++]; while (k < 3) p.item[e][k++] = -1; ++e; }
      }
    }
    p.n = e; nc = 0;
    while (head < tail) carry[nc++] = fifo[head++];
  }
  return p;
}
template <int TMASK, class F>
DI void conv_units(lptr kr, lptr ub, int lane, int rho0, int nrho, f32x4 (&acc)[8], F hook) {
  constexpr int MAXT = (TMASK & 0x80) ? 7 : 5;
  constexpr ConvPlan plan = conv_plan(TMASK, MAXT);
  const int r = lane & 15, q = lane >> 4;
  lptr pk = kr + ((r & 1) ? 0 : KC1) + 4092 - 4 * r + 16 * q - 3584 - 64 * rho0 - 64;
  lptr pu = ub + ((r & 1) ? 0 : UC1) + 64 * (r >> 1) + 16 * q + 64 * (7 - rho0) - 64;
  lptr pk2 = pk + 8;
  u32x2 Kl[8], Kh[8]; u32x4 Uf[8];
  asm volatile("" : "+v"(pk), "+v"(pk2), "+v"(pu));
#pragma unroll
  for (int j = 0; j <= MAXT; ++j) { const int m = MAXT - j; Uf[j] = ld16(pu + 64 + 512 * j); Kl[m] = *(LAS u32x2*)(pk + 64 + 512 * (7 - m)); Kh[m] = *(LAS u32x2*)(pk2 + 64 + 512 * (7 - m)); SBAR(); }
#pragma unroll 1
  for (int it = 0; it < nrho; ++it) {
    int e = 0;
#pragma unroll
    for (int m = MAXT; m >= 0; --m) {
#pragma unroll
      for (int j = 0; j + m <= MAXT; ++j) {
        if ((TMASK >> (m + j)) & 1) {
          acc[m + j] = MFMA_BF(Uf[j], (u32x4{Kl[m][0], Kl[m][1], Kh[m][0], Kh[m][1]}), acc[m + j]);
#pragma unroll
          for (int k = 0; k < 3; ++k) {
            const int c = plan.item[e][k];
            if (c >= 0) {
              const int o = (c & 32) ? 64 : 0, cc = c & 31, mm = (cc - 8) >> 1;
              if (cc < 8) Uf[cc] = ld16(pu + o + 512 * cc);
              else if (cc & 1) Kh[mm] = *(LAS u32x2*)(pk2 + o + 512 * (7 - mm));
              else Kl[mm] = *(LAS u32x2*)(pk + o + 512 * (7 - mm));
            }
          }
          SBAR();
          ++e;
        }
      }
    }
    pk -= 64; pk2 -= 64; pu -= 64;
    asm volatile("" : "+v"(pk), "+v"(pk2), "+v"(pu));
    hook(it);
  }
}

template <int N> struct IC { static constexpr int value = N; };
DI u64 uni64(u64 v) { return (u64)(unsigned)__builtin_amdgcn_readfirstlane((unsigned)v) | ((u64)(unsigned)__builtin_amdgcn_readfirstlane((unsigned)(v >> 32)) << 32); }
DI int fresh_lane() { int l; asm volatile("v_mbcnt_lo_u32_b32 %0, -1, 0\n\tv_mbcnt_hi_u32_b32 %0, -1, %0" : "=v"(l)); return l; }
#define ARR() do {} while (0)
#define BAR() do { SBAR(); ARR(); asm volatile("s_barrier" ::: "memory"); SBAR(); } while (0)
#define BAR_L() do { SBAR(); asm volatile("s_waitcnt lgkmcnt(0)" ::: "memory"); SBAR(); ARR(); asm volatile("s_barrier" ::: "memory"); SBAR(); } while (0)
#define LD_SC1(off) __builtin_amdgcn_raw_buffer_load_b128(rs, (off), 0, SC1)
#define ST_SC1(v, off) __builtin_amdgcn_raw_buffer_store_b128((v), rs, (off), 0, SC1)

__global__ __launch_bounds__(512, 2) void fused_kernel(const u16* __restrict__ x, const u16* __restrict__ p_lar, const u16* __restrict__ p_aim,
                                                       const u16* __restrict__ p_cr, const u16* __restrict__ p_ci, const u16* __restrict__ p_ldt,
                                                       const u16* __restrict__ p_dsk, const u16* __restrict__ wg, const u16* __restrict__ wu,
                                                       const u16* __restrict__ wd, u16* __restrict__ out, char* ws) {
  extern __shared__ __attribute__((aligned(16))) char smem[];
  const int tid = __builtin_amdgcn_workitem_id_x(), bid = __builtin_amdgcn_workgroup_id_x();
  u64 pc_; asm volatile("s_getpc_b64 %0" : "=s"(pc_));
  lptr lds = (lptr)smem;
  const unsigned lds0 = (unsigned)(uintptr_t)smem;
  const unsigned fb = lds0 + L_FLG;
  const int wraw = __builtin_amdgcn_readfirstlane(tid >> 6), lane = tid & 63;
  const int wid = wraw ^ ((wraw >> 2) << 1);
  const int xcd = bid & 7, bi = bid >> 3;
  const int rb = (xcd >> 1) * 4 + (bi >> 3), cb = (xcd & 1) * 8 + (bi & 7);
  const int tg = (xcd >> 1) * 8 + (bi >> 2), nb = (xcd & 1) * 4 + (bi & 3);
  const int cw = xcd * 32 + bi, odd = cw & 1, c0 = 5 * (cw >> 1) + 2 * odd;
  const int chA = c0 + odd, chC = odd ? c0 : c0 + 2;
  const int rot = (EXP & 2) ? 0 : rb >= 10 ? rb - 10 : rb;
  const __amdgpu_buffer_rsrc_t rs = __builtin_amdgcn_make_buffer_rsrc(ws, 0, (int)WS_END, 0x00020000);
  const int r = lane & 15, q = lane >> 4;

  u64 tag = 0;
  const int lw = wid - 4, kw = wid - 6;
  unsigned vx[4], vw[12];
  auto issue = [&](int s, int sbase, int h, int P0 = 0, int P1 = 5) {
    int ks = s + rot; if (ks >= 10) ks -= 10;
    const unsigned base = lds0 + sbase;
    const char* sx = (const char*)x + (size_t)(rb * 128 + h * 64) * 1280 - 4096 + ks * 128;
    const char* sw = (const char*)(h ? wu : wg) + cb * 192 - 4096 + (size_t)ks * 64 * 3072;
    if (P0 <= 0 && 0 < P1) glds4(sx, vx[0], vx[1], vx[2], vx[3], base + h * 8192);
    if (P0 <= 1 && 1 < P1) glds4(sw, vw[0], vw[1], vw[2], vw[3], base + 16384 + h * 12288);
    if (P0 <= 2 && 2 < P1) glds4(sx + 32 * 1280, vx[0], vx[1], vx[2], vx[3], base + h * 8192 + 4096);
    if (P0 <= 3 && 3 < P1) glds4(sw, vw[4], vw[5], vw[6], vw[7], base + 16384 + h * 12288 + 4096);
    if (P0 <= 4 && 4 < P1) glds4(sw, vw[8], vw[9], vw[10], vw[11], base + 16384 + h * 12288 + 8192);
  };
  auto prep_dma = [&](int ln) {
#pragma unroll
    for (int j = 0; j < 4; ++j) { const int row = 8 * j + (ln >> 3); vx[j] = row * 1280 + (((ln & 7) ^ ((row >> 1) & 7)) << 4) + 4096 - 1024 * j; }
#pragma unroll
    for (int j = 0; j < 3; ++j) { const int c = j * 64 + ln, row = (c * 43691) >> 19, pos = c - row * 12;
      const unsigned v = row * 3072 + ((pos ^ (((row >> 3) & 1) << 1)) << 4) + 4096;
#pragma unroll
      for (int g = 0; g < 4; ++g) vw[3 * g + j] = v + g * 16 * 3072 - 1024 * ((3 * g + j) & 3); }
  };
  auto wd_dma = [&](int ln, int w, int j0, int j1) {
    unsigned v[5];
#pragma unroll
    for (int i = 0; i < 5; ++i) { const int c = i * 64 + ln, row = (c * 6554) >> 16; v[i] = row * 1280 + (c - row * 10) * 16 + 4096 - (i & 3) * 1024; }
#pragma unroll
    for (int j = j0; j < j1; ++j) {
      const char* sp = (const char*)wd + (size_t)((w * 4 + ((j + nb) & 3)) * 96 + 32) * 1280 + nb * 160 - 4096;
      const unsigned d = lds0 + ((j >> 1) ? SL0 : SL2) + w * 10240 + (j & 1) * 5120;
      glds4(sp, v[0], v[1], v[2], v[3], d); glds1(sp, v[4], d + 4096);
    }
  };
  const int lnb = (xcd >> 1) * 64 + (xcd & 1) * 32 + bi;
  const int ro0 = (0x100222 >> (4 * wid)) & 3, th0 = (0x000210 >> (4 * wid)) & 3, ro1 = (wid >> 1) & 1, th1 = 1 + (wid & 1);
  const int ch0 = c0 + ((0x0210 >> (4 * (ro0 + odd))) & 3), ch1 = chA + ro1;
  const int pt0 = ro0 * PCH + (ro0 >> 1) * (SL2 - SA - 2 * PCH) + th0 * 256, pt1 = ro1 * PCH + th1 * 256;
  unsigned o = (lane < 32 ? ch0 * 384 + th0 * 128 : ch1 * 384 + th1 * 128) + (lane & 31) * 4;
  unsigned xo0 = lane * 16, xo1 = q * 1280 + r * 16 + 1024;
  asm volatile("" : "+v"(o), "+v"(xo0), "+v"(xo1));
  asm volatile("" :: "s"(x), "s"(p_lar), "s"(p_aim), "s"(p_cr), "s"(p_ci), "s"(p_ldt), "s"(p_dsk), "s"(wg), "s"(wu), "s"(wd), "s"(out), "s"(ws));
  const int ptp = wid == 5 ? pt0 : pt1;
  if ((0x2f >> wid) & 1) {
    asm volatile("s_mov_b32 m0, %5\n\ts_nop 0\n\tglobal_load_lds_dword %0, %1\n\tglobal_load_lds_dword %0, %2 offset:784\n\t"
                 "global_load_lds_dword %0, %3 offset:1568\n\tglobal_load_lds_dword %0, %4 offset:2352"
                 :: "v"(o), "s"(p_lar), "s"((const char*)p_aim - 784), "s"((const char*)p_cr - 1568), "s"((const char*)p_ci - 2352), "s"(lds0 + SA + ptp) : "memory");
  }
  if (wid >= 6) {
    const char* xr = (const char*)x + (size_t)(lnb * 8 + kw * 4) * 1280;
#pragma unroll
    for (int j = 0; j < 4; ++j) glds1(xr + j * 1280, xo0, lds0 + L_LNB + (kw * 4 + j) * LNR);
    glds1(xr, xo1, lds0 + L_LNT + kw * 1024);
  } else if (wid >= 4) { if (lane < 16) *(LAS unsigned*)(lds + L_FLG + lane * 4) = 0; prep_dma(lane); }
  SBAR(); asm volatile("s_waitcnt lgkmcnt(0)\n\ts_barrier" ::: "memory"); SBAR();
  const float ldt0 = ubf(p_ldt, ch0), ldt1 = ubf(p_ldt, ch1);
  if (wid == 4 || wid == 5) {
    asm volatile("s_mov_b32 m0, %2\n\ts_mov_b64 exec, 0x1ff\n\tglobal_load_lds_dword %0, %1\n\ts_mov_b64 exec, -1"
                 :: "v"(((bi & 15) * 18 + lw * 9 + lane) * 128), "s"(pc_ & ~127ull), "s"(lds0 + L_RTB + 10240 + lw * 64) : "memory");
    issue(0, SL0, lw);
  }
  auto lds_params = [&](float (&pv)[4], int m) {
#pragma unroll
    for (int e = 0; e < 4; ++e) pv[e] = bf1(*(LAS u16*)(lds + SA + ptp + e * PROW + m * 2));
  };
  if (wid >= 6) {
    waitv<0>();
    STAMP(0);
    lptr xp = lds + L_LNB + (kw * 4 + q) * LNR + r * 16;
    u32x4 xv[5];
#pragma unroll
    for (int j = 0; j < 4; ++j) xv[j] = ld16(xp + 256 * j);
    xv[4] = ld16(lds + L_LNT + kw * 1024 + lane * 16);
    float v[40]; float s0 = 0.f, s1 = 0.f;
#pragma unroll
    for (int j = 0; j < 20; ++j) { const unsigned w = xv[j >> 2][j & 3]; v[2 * j] = bflo(w); v[2 * j + 1] = bfhi(w); s0 += v[2 * j]; s1 += v[2 * j + 1]; }
    const float mean = row16_sum(s0 + s1) * (1.f / DM);
    float q0 = 0.f, q1 = 0.f;
#pragma unroll
    for (int j = 0; j < 20; ++j) { v[2 * j] -= mean; v[2 * j + 1] -= mean; q0 = __builtin_fmaf(v[2 * j], v[2 * j], q0); q1 = __builtin_fmaf(v[2 * j + 1], v[2 * j + 1], q1); }
    const float var = rbf(row16_sum(q0 + q1) * (1.f / DM));
    const float mb = rbf(mean), sd = rbf(__builtin_amdgcn_sqrtf(rbf(var + 1e-5f)));
    const float rsd = __builtin_amdgcn_rcpf(sd), off = (mean - mb) * rsd;
#pragma unroll
    for (int j = 0; j < 20; ++j) xv[j >> 2][j & 3] = pkbf(__builtin_fmaf(v[2 * j], rsd, off), __builtin_fmaf(v[2 * j + 1], rsd, off));
#pragma unroll
    for (int j = 0; j < 5; ++j) st16(xp + 256 * j, xv[j]);
    if (r == 0) __builtin_amdgcn_raw_buffer_store_b64(u32x2{asu(mb), asu(sd)}, rs, WS_ST + (lnb * 8 + kw * 4 + q) * 8, 0, SC1);
  } else if (wid == 4) {
    const int un = umin(cw, 191) * 2, ukb = un >> 3, unb = un & 7;
    const int c3 = (ukb * 43) >> 7, kt = ukb - 3 * c3;
    const int t0 = kt == 0 ? 0 : kt == 1 ? 80 : 32, t1 = kt == 0 ? 0 : kt == 1 ? 48 : 32;
#pragma unroll
    for (int j = 0; j < 5; ++j) { const int c = j * 64 + lane, row = (c * 6554) >> 16, pos = c - row * 10;
      const unsigned v = (c3 * 96 + row + (row < 16 ? t0 : t1)) * 1280 + unb * 160 + pos * 16;
      glds1(wd, v, lds0 + L_RTB + j * 1024); glds1((const char*)wd + 160, v, lds0 + L_RTB + 5120 + j * 1024); }
  } else {
    if (wid == 5) waitv<21>(); else waitv<0>();
    STAMP(0);
    float pv[1][4], pn[4];
    lds_params(pv[0], lane); lds_params(pn, 64 + lane);
    unsigned tb[1] = {lds0 + pt0}, sa_ = lds0 + L_SC + (ro0 * 3 + th0) * 4, inc = wid < 3;
    float ldt[1] = {ldt0};
#pragma unroll 1
    for (int rnd = wid == 5; rnd < 2; ++rnd) {
      float sc[1];
      kpool<1>(tb, pv, ldt, false, sc);
      asm volatile("s_mov_b64 exec, 1\n\tds_write_b32 %0, %1\n\tds_add_u32 %2, %3\n\ts_mov_b64 exec, -1" :: "v"(sa_), "v"(sc[0]), "v"(fb + FL_KP * 4), "v"(inc) : "memory");
#pragma unroll
      for (int e = 0; e < 4; ++e) pv[0][e] = pn[e];
      tb[0] = lds0 + pt1; sa_ = lds0 + L_SC + (ro1 * 3 + th1) * 4; inc = 0; ldt[0] = ldt1;
    }
  }
  STAMP(1);

  if (wid >= 6) {
    const int lane = fresh_lane(), r = lane & 15, q = lane >> 4;
    const int un = umin(cw * 2 + kw, 383), ukb = un >> 3, unb = un & 7;
    const float dsk = ubf(p_dsk, chA + kw);
    u32x4 xv[5];
    u64 ep = __hip_atomic_load((GAS u64*)(ws + WS_EPOCH), RLX_AGENT);
    BAR_L();
    u32x4 wt[5], wf[12];
#pragma unroll
    for (int j = 0; j < 5; ++j) xv[j] = tr2(lds + L_LNB + (r >> 2) * LNR + (kw * 5 + j) * 128 + q * 32 + (lane & 3) * 8, 4 * LNR);
    {
      const int sg = (q & 1) * 32;
      lptr pa = lds + SL0 + 16384 + kw * 12288 + (8 * q + (r >> 2)) * 192 + 8 * (lane & 3);
#pragma unroll
      for (int t = 0; t < 12; ++t) wf[t] = tr2(pa + (t / 6) * 6144 + (t % 6) * 32 + ((t & 1) ? -sg : sg), 768);
    }
    lptr pool = lds + SA + kw * PCH;
    KPtr kp; kp.init(pool, lane);
    u32x4 kv[2][6], kq[4];
    kp.fetch(kv[0], 0); kp.fetch(kv[1], 64);
    SBAR();
#pragma unroll
    for (int j = 0; j < 5; ++j) wt[j] = tr2(lds + L_RTB + kw * 5120 + (4 * q + (r >> 2)) * 160 + j * 32 + (lane & 3) * 8, 2560);
    BAR_L();
    tag = uni64(ep) + 1;
    *(volatile LAS u64*)(lds + L_FLG + FL_TAG * 4) = tag;
    unsigned pf_sink = 0;
    f32x4 ko[3], ka[3][3];
    u32x4 f0 = {0, 0, 0, 0}, f1 = {0, 0, 0, 0};
    constexpr int HS0[9] = {HSPLAN};
#pragma unroll
    for (int sl = 0; sl < 8; ++sl) {
      if (sl == 6 && kw == 0) { f0 = LD_SC1(WS_F0 + xcd * F0REP + lane * 16); f1 = LD_SC1(WS_F0 + xcd * F0REP + 1024 + lane * 16); SBAR(); }
      if (sl == 3 && kw == 0 && lane < 8) __hip_atomic_store((GAS u64*)(ws + WS_F0 + lane * F0REP + cw * 8), tag, RLX_AGENT);
#pragma unroll
      for (int hs = HS0[sl]; hs < HS0[sl + 1]; ++hs) {
        const int k = hs >> 1;
        if (!(hs & 1)) kprodh<0>(kv[k & 1], kq);
        else { kprodh<1>(kv[k & 1], kq); if (k + 2 < 12) kp.fetch(kv[k & 1], 64 * (k + 2)); kmma1(kq, ka[k >> 2], (k & 3) == 0); }
      }
      if (sl == 0) {
#pragma unroll
        for (int j = 0; j < 5; ++j) ST_SC1(xv[j], WS_XB + lnb * 10240 + (kw * 5 + j) * 1024 + lane * 16);
      }
      if (sl == 1) {
        if (rb < 10) {
          const u32x4 ones = {0x3f803f80u, 0x3f803f80u, 0x3f803f80u, 0x3f803f80u};
          f32x4 cs[6];
          SBAR(); __builtin_amdgcn_s_setprio(3); SBAR();
#pragma unroll
          for (int t = 0; t < 6; ++t) cs[t] = MFMA_BF(ones, wf[t], (f32x4{0, 0, 0, 0}));
#pragma unroll
          for (int t = 0; t < 6; ++t) cs[t] = MFMA_BF(ones, wf[6 + t], cs[t]);
          SBAR(); __builtin_amdgcn_s_setprio(0); SBAR();
          const float cs0 = q == 0 ? cs[0][0] : q == 1 ? cs[1][0] : q == 2 ? cs[2][0] : cs[3][0], cs1 = q == 0 ? cs[4][0] : cs[5][0];
          const unsigned co = WS_CS + (cb * 10 + rb) * 768 + kw * 384 + lane * 4;
          __builtin_amdgcn_raw_buffer_store_b32(asu(cs0), rs, co, 0, SC1);
          if (lane < 32) __builtin_amdgcn_raw_buffer_store_b32(asu(cs1), rs, co + 256, 0, SC1);
        }
        if (cw < 192) {
#pragma unroll
          for (int f = 0; f < 5; ++f) ST_SC1(wt[f], WS_WD + (unb * 48 + ukb) * 5120 + f * 1024 + lane * 16);
        }
      }
      if (sl == 2) waitv<0>();
      if (sl == 6 && kw == 0) {
        const unsigned tl = (unsigned)tag, th = (unsigned)(tag >> 32);
        _Pragma("nounroll") for (int sp = 0; sp < SPIN_GLB; ++sp) {
          if (wall(((f0[0] ^ tl) | (f0[1] ^ th) | (f0[2] ^ tl) | (f0[3] ^ th) | (f1[0] ^ tl) | (f1[1] ^ th) | (f1[2] ^ tl) | (f1[3] ^ th)) == 0u)) break;
          f0 = LD_SC1(WS_F0 + xcd * F0REP + lane * 16); f1 = LD_SC1(WS_F0 + xcd * F0REP + 1024 + lane * 16);
          __builtin_amdgcn_s_sleep(2);
        }
      }
      if (sl >= 6) {
        const unsigned po = WS_WD + nb * 245760 + (bi >> 2) * 30720 + umin(((sl - 6) * 2 + kw) * 64 + lane, 239u) * 128;
        asm volatile("global_load_dword %0, %1, %2 sc1" : "+v"(pf_sink) : "v"(po), "s"(ws) : "memory");
      }
      BAR();
    }
    STAMP(2);
    kcombine(ko, ka, *(LAS float*)(lds + L_SC + kw * 12), *(LAS float*)(lds + L_SC + kw * 12 + 4), *(LAS float*)(lds + L_SC + kw * 12 + 8));
    kstore(pool + O_KL, lane, ko, dsk);
    kexpand(pool + O_KREV, pool + O_KL, lane);
    flag_set(fb, FL_KA + kw, 1);
    asm volatile("s_waitcnt vmcnt(0)" : "+v"(pf_sink) :: "memory");
    STAMP(3);
  } else {
    if (wid >= 4) {
      const int lane = fresh_lane();
      u32x4 cv[6][6];
      {
        KPtr kp; kp.init(lds + SL2 + lw * 384, lane);
        flag_wait(fb, FL_KP, 3);
#pragma unroll
        for (int i = 0; i < 6; ++i) kp.fetch(cv[i], 64 * i);
      }
      const float sca = *(LAS float*)(lds + L_SC + 24 + lw * 4), scb = *(LAS float*)(lds + L_SC + 24 + lw * 8), scc = *(LAS float*)(lds + L_SC + 28 + lw * 4);
      SBAR();
      prep_dma(fresh_lane());
      if (!lw) { issue(1, SL1, 0); issue(1, SL1, 1); waitv<40>(); } else waitv<0>();
      BAR_L();
      issue(2, SL2, lw, 0, 1); issue(2, SL2, lw, 2, 3);
      f32x4 ka[3][3], kc[3];
#pragma unroll
      for (int s = 0; s < 7; ++s) {
        if (s == 0) waitv<8>(); else waitv<20>();
        BAR();
        if (s == 0) { issue(2, SL2, lw, 1, 2); issue(2, SL2, lw, 3, 5); }
        issue(s + 3, s % 3 == 0 ? SL0 : s % 3 == 1 ? SL1 : SL2, lw);
        if (s >= 1 && !(EXP & 4096)) { u32x4 o[4]; kprod(cv[s - 1], o); kmma1(o, ka[(s - 1) >> 1], s & 1); }
      }
      kcombine(kc, ka, sca, scb, scc);
      if (lw == 0) {
#pragma unroll
        for (int i = 0; i < 3; ++i) *(LAS f32x4*)(lds + L_STG + i * 1024 + lane * 16) = kc[i];
      }
      waitv<20>();
      BAR_L();
      STAMP(2);
      tag = uni64(*(volatile LAS u64*)(lds + L_FLG + FL_TAG * 4));
      u32x4 uc[8]; unsigned uprev;
      const int n0 = lw * 128 + (lane >> 2) * 8;
      const unsigned uo = WS_XB + n0 * 10240 + (c0 + (lane & 3)) * 16;
      u32x4 sv, cp[10];
      if (lw == 0) {
        sv = LD_SC1(WS_ST + rb * 1024 + lane * 16);
#pragma unroll
        for (int j = 0; j < 10; ++j) cp[j] = LD_SC1(WS_CS + (cb * 10 + j) * 768 + (lane < 48 ? lane : 0) * 16);
      }
#pragma unroll
      for (int j = 0; j < 8; ++j) uc[j] = LD_SC1(uo + j * 10240);
      uprev = __builtin_amdgcn_raw_buffer_load_b32(rs, uo - 10240 + 12, 0, SC1);
      SBAR();
      if (lw) {
#pragma unroll
        for (int i = 0; i < 3; ++i) kc[i] += *(LAS f32x4*)(lds + L_STG + i * 1024 + lane * 16);
        kstore(lds + L_PC, lane, kc, ubf(p_dsk, chC));
        waitv<9>();
      } else waitv<20>();
      BAR();
      STAMP(3);
      if (lw == 0) {
        f32x4 cs = {0, 0, 0, 0};
#pragma unroll
        for (int j = 0; j < 10; ++j) cs += __builtin_bit_cast(f32x4, cp[j]);
        st16(lds + L_STAT + lane * 16, sv);
        if (lane < 48) *(LAS f32x4*)(lds + L_CS + lane * 16) = cs;
        flag_set(fb, FL_EPI, 1);
        if (bid == 0 && lane == 0) __hip_atomic_store((GAS u64*)(ws + WS_EPOCH), tag, RLX_AGENT);
      }
      if (n0 == 0) uprev = 0;
      if ((lane & 3) < 3) {
        lptr ud = lds + L_U + (lane & 3) * UCH + 448 + n0 * 16;
#pragma unroll
        for (int j = 0; j < 8; ++j) {
          st16(ud + j * 16, uc[j]);
          const unsigned pw_ = j ? uc[j - 1][3] : uprev;
          st16(ud + UC1 + j * 16, u32x4{__builtin_amdgcn_alignbit(uc[j][0], pw_, 16), __builtin_amdgcn_alignbit(uc[j][1], uc[j][0], 16),
                                        __builtin_amdgcn_alignbit(uc[j][2], uc[j][1], 16), __builtin_amdgcn_alignbit(uc[j][3], uc[j][2], 16)});
        }
      }
      if (lane < 28) {
#pragma unroll
        for (int c = 0; c < 3; ++c) st16(lds + L_U + c * UCH + lw * UC1 + lane * 16, u32x4{0, 0, 0, 0});
      }
      flag_add(fb, FL_U, lane);
      if (lw) { kexpand(lds + L_KC, lds + L_PC, lane); flag_set(fb, FL_KC, 1); }

    } else {
      f32x4 acc[2][3][4];
      if (!(EXP & 4)) __builtin_amdgcn_s_setprio(2);
      const int tw = wid >> 1, fw = wid & 1;
      lptr pB0 = lds + (tw * 64 + r) * 128 + ((q ^ (r >> 1)) << 4);
      lptr pB1 = lds + (tw * 64 + r) * 128 + (((q ^ (r >> 1)) ^ 4) << 4);
      const int sg = (q & 1) * 32;
      lptr pAb = lds + 16384 + (8 * q + (r >> 2)) * 192 + 8 * (lane & 3);
      lptr pA[3] = {pAb + (fw ? 160 - sg : sg), pAb + (fw ? 128 + sg : 32 - sg), pAb + (fw ? 96 - sg : 64 + sg)};
      u32x4 A0[2][3], B0[4], A1[2][3], B1[4];
      auto rd = [&](u32x4 (&A)[2][3], u32x4 (&B)[4], int off, int kk) {
#pragma unroll
        for (int tb = 0; tb < 4; ++tb) B[tb] = ld16((kk ? pB1 : pB0) + off + tb * 2048);
#pragma unroll
        for (int m = 0; m < 2; ++m)
#pragma unroll
          for (int ii = 0; ii < 3; ++ii) A[m][ii] = tr2(pA[ii] + off + m * 12288 + kk * 6144, 768);
      };
      auto mm = [&](u32x4 (&A)[2][3], u32x4 (&B)[4]) {
#pragma unroll
        for (int e = 0; e < 24; ++e) { const int m = e / 12, ii = (e / 4) % 3, tb = e % 4; acc[m][ii][tb] = MFMA_BF(A[m][ii], B[tb], acc[m][ii][tb]); }
      };
      auto sched = [&]() {
#pragma unroll
        for (int k = 0; k < 16; ++k) { SGB(0x8, 1); SGB(0x100, 1); }
        SGB(0x8, 8);
      };
      BAR_L();
      STAMP(2);
      rd(A0, B0, SL0, 0);
      SBAR();
      rd(A1, B1, SL0, 1);
#pragma unroll
      for (int e = 0; e < 24; ++e) { const int m = e / 12, ii = (e / 4) % 3, tb = e % 4; acc[m][ii][tb] = MFMA_BF(A0[m][ii], B0[tb], (f32x4{0, 0, 0, 0})); }
      sched();
      int s0 = SL1, s1 = SL2, s2 = SL0;
#pragma unroll 1
      for (int s = 0; s < 9; ++s) {
        BAR_L();
        rd(A0, B0, s0, 0); mm(A1, B1); sched();
        SBAR();
        rd(A1, B1, s0, 1); mm(A0, B0); sched();
        const int t = s0; s0 = s1; s1 = s2; s2 = t;
      }
      SBAR();
      flag_add(fb, FL_G1, lane);
      mm(A1, B1);
      SBAR();
      if (EXP & 64) __builtin_amdgcn_s_setprio(0);
      STAMP(3);
      flag_wait(fb, FL_EPI, 1);
      tag = uni64(*(volatile LAS u64*)(lds + L_FLG + FL_TAG * 4));
      STAMP(4);
      {
        float nmb[4], al[4], sd[4];
#pragma unroll
        for (int tb = 0; tb < 4; ++tb) { const f32x2 st = *(LAS f32x2*)(lds + L_STAT + (tw * 64 + tb * 16 + r) * 8); nmb[tb] = -st.x; sd[tb] = st.y; al[tb] = -1.44269504f * __builtin_amdgcn_rcpf(st.y); }
        const unsigned hoff = WS_H + ((rb * 2 + tw) * 16 + cb) * 12288 + lane * 16;
        auto hcalc = [&](const f32x4& g4, const f32x4& u4, const f32x4& cg, const f32x4& cu, int tb) {
          float hv[4];
#pragma unroll
          for (int j = 0; j < 4; ++j) {
            const float g = __builtin_fmaf(nmb[tb], cg[j], g4[j]) * al[tb];
            const float u = __builtin_fmaf(nmb[tb], cu[j], u4[j]);
            const float e = __builtin_amdgcn_exp2f(g);
            hv[j] = u * __builtin_amdgcn_rcpf(__builtin_fmaf(e, sd[tb], sd[tb]));
          }
          return u32x2{pkbf(hv[0], hv[1]), pkbf(hv[2], hv[3])};
        };
        lptr pcs = lds + L_CS + (fw * 48 + 4 * q) * 4;
        const int oa = fw ? 128 : 0, oc = fw ? 0 : 128;
        {
          const f32x4 cga = *(LAS f32x4*)(pcs + oa), cua = *(LAS f32x4*)(pcs + 384 + oa), cgb = *(LAS f32x4*)(pcs + 64), cub = *(LAS f32x4*)(pcs + 384 + 64);
#pragma unroll
          for (int tb = 0; tb < 4; ++tb) {
            const u32x2 ha = hcalc(acc[0][0][tb], acc[1][0][tb], cga, cua, tb);
            const u32x2 hb = hcalc(acc[0][1][tb], acc[1][1][tb], cgb, cub, tb);
            ST_SC1((u32x4{ha[0], ha[1], hb[0], hb[1]}), hoff + fw * 4096 + tb * 1024);
            SBAR();
          }
        }
        {
          const f32x4 cg = *(LAS f32x4*)(pcs + oc), cu = *(LAS f32x4*)(pcs + 384 + oc);
#pragma unroll
          for (int t2 = 0; t2 < 2; ++t2) {
            const u32x2 ha = hcalc(acc[0][2][2 * t2], acc[1][2][2 * t2], cg, cu, 2 * t2);
            SBAR();
            const u32x2 hb = hcalc(acc[0][2][2 * t2 + 1], acc[1][2][2 * t2 + 1], cg, cu, 2 * t2 + 1);
            SBAR();
            ST_SC1((u32x4{ha[0], ha[1], hb[0], hb[1]}), hoff + 8192 + fw * 2048 + t2 * 1024);
            SBAR();
          }
        }
      }
      if (EXP & 128) __builtin_amdgcn_s_setprio(0);
      STAMP(5);
      waitv<2>();
      if (lane < 8) __hip_atomic_store((GAS u64*)(ws + WS_MB + (rb * 16 + tw * 8 + lane) * MBS + cb * 16 + fw * 8), tag, RLX_AGENT);
    {
    const int sw = wid ^ 2, hm = sw & 1;
    lptr am = lds + SA + hm * PCH;
    f32x4 ca[8];
#pragma unroll
    for (int t = 0; t < 8; ++t) ca[t] = f32x4{0, 0, 0, 0};
    lptr pmain = am + O_KL + 1312 + lane * 16;

    flag_wait(fb, FL_KA + hm, 1);
    flag_wait(fb, FL_U, 2);
    STAMP(6);
    conv_units<0xff>(am + O_KREV, lds + L_U + (hm + odd) * UCH, lane, sw < 2 ? 5 : 0, sw < 2 ? 3 : 5, ca, [&](int it) {
      if (it == 0) { waitv<1>(); if (lane < 8) __hip_atomic_store((GAS u64*)(ws + WS_MB + (rb * 16 + tw * 8 + lane) * MBS + 256 + cb * 16 + fw * 8), tag, RLX_AGENT); } });
    if (sw < 2) {
      lptr pc = lds + L_PC;
#pragma unroll
      for (int t = 0; t < 8; ++t) { *(LAS f32x4*)(pmain + t * 1024) = ca[t]; ca[t] = f32x4{0, 0, 0, 0}; }
      flag_set(fb, FL_PA + hm, 1);
      flag_wait(fb, FL_KC, 1);
      lptr uc_ = lds + L_U + (odd ? 0 : 2) * UCH;
      if (odd) conv_units<0x3a>(lds + L_KC, uc_, lane, hm * 4, 4, ca, [](int) {});
      else conv_units<0xc5>(lds + L_KC, uc_, lane, hm * 4, 4, ca, [](int) {});
      STAMP(7);
      f32x4 c4[4];
      c4[0] = odd ? ca[1] : ca[0]; c4[1] = odd ? ca[3] : ca[2]; c4[2] = odd ? ca[4] : ca[6]; c4[3] = odd ? ca[5] : ca[7];
      if (!hm) {
#pragma unroll
        for (int t = 0; t < 4; ++t) *(LAS f32x4*)(pc + t * 1024 + lane * 16) = c4[t];
        flag_set(fb, FL_PC, 1);
      } else {
        flag_wait(fb, FL_PC, 1);
#pragma unroll
        for (int t = 0; t < 4; ++t) c4[t] += *(LAS f32x4*)(pc + t * 1024 + lane * 16);
        waitl();
#pragma unroll
        for (int t = 0; t < 4; ++t) { *(LAS unsigned*)(pc + t * 512 + q * 128 + r * 4) = pkbf(c4[t][0], c4[t][1]); *(LAS unsigned*)(pc + t * 512 + q * 128 + r * 4 + 64) = pkbf(c4[t][2], c4[t][3]); }
#pragma unroll
        for (int i = 0; i < 2; ++i) {
          const int s4 = 2 * i + (lane >> 5);
          const int T = odd ? (s4 == 0 ? 1 : s4 + 2) : (s4 < 2 ? 2 * s4 : s4 + 4);
          ST_SC1(ld16(pc + i * 1024 + lane * 16), WS_Y + ((T * 4 + ((lane >> 3) & 3)) * DM + chC) * 128 + (lane & 7) * 16);
        }
      }
    } else {
      STAMP(7);
      flag_wait(fb, FL_PA + hm, 1);
#pragma unroll
      for (int t = 0; t < 8; ++t) ca[t] += *(LAS f32x4*)(pmain + t * 1024);
#pragma unroll
      for (int t = 0; t < 8; ++t) { *(LAS unsigned*)(am + t * 512 + q * 128 + r * 4) = pkbf(ca[t][0], ca[t][1]); *(LAS unsigned*)(am + t * 512 + q * 128 + r * 4 + 64) = pkbf(ca[t][2], ca[t][3]); }
#pragma unroll
      for (int i = 0; i < 4; ++i) ST_SC1(ld16(am + i * 1024 + lane * 16), WS_Y + ((i * 8 + (lane >> 3)) * DM + chA + hm) * 128 + (lane & 7) * 16);
    }
    waitv<0>();
    if (flag_add(fb, FL_YC, lane) == 3u) {
      if (lane < 32) __hip_atomic_store((GAS u64*)(ws + WS_MB + (lane * 8 + xcd) * MBS + 512 + bi * 8), tag, RLX_AGENT);
      _Pragma("nounroll") for (int sp = 0; sp < SPIN_GLB; ++sp) { if (wall(gflag_ok(ws, WS_MB + (tg * 8 + nb) * MBS + 512 + (lane & 31) * 8, tag))) break; __builtin_amdgcn_s_sleep(4); }
      flag_set(fb, FL_YOK, 1);
    }

    flag_wait(fb, FL_YOK, 1);
    STAMP(8);
    u32x4 yv[3];
#pragma unroll
    for (int j = 0; j < 3; ++j) {
      const int p = umin(sw + 4 * j, 9), c = p * 64 + lane, row = (c * 6554) >> 16;
      glds1((const char*)x + (size_t)tg * 64 * 1280 + nb * 160, row * 1280 + (c - row * 10) * 16, lds0 + L_XT + p * 1024);
    }
#pragma unroll
    for (int j = 0; j < 3; ++j) { const int p = umin(sw + 4 * j, 9); yv[j] = LD_SC1(WS_Y + (tg * DM + nb * 80 + p * 8) * 128 + lane * 16); }
    lptr yb = lds + L_YB + sw * 3072;
#pragma unroll
    for (int j = 0; j < 3; ++j) st16(yb + j * 1024 + lane * 16, yv[j]);
#pragma unroll
    for (int j = 0; j < 3; ++j) {
      const u32x4 a = tr2(yb + j * 1024 + (r >> 2) * 128 + q * 32 + (lane & 3) * 8, 512);
      if (sw + 4 * j < 10) st16(lds + L_BASE + lane * 160 + (sw + 4 * j) * 16, a);
    }
    }
      __builtin_amdgcn_s_setprio(0);
    }
  }

  if (wid >= 4) {
    const int sw = wid - 4;
    const int lane = fresh_lane();
    {
      unsigned bo[4], ao[4];
#pragma unroll
      for (int c = 0; c < 4; ++c) { const int cc = sw * 4 + ((c + nb) & 3); bo[c] = WS_WD + (nb * 48 + cc * 3) * 5120; ao[c] = WS_H + (tg * 16 + cc) * 12288; }
      const unsigned l16 = lane * 16;
#define BFRAG(k, n) __builtin_amdgcn_raw_buffer_load_b128(rs, l16, bo[(k) < 8 ? (k) >> 1 : (k) - 8] + ((k) < 8 ? ((k) & 1) * 5120 : 10240) + (n) * 1024, SC1)
#define AFRAG(k, t) __builtin_amdgcn_raw_buffer_load_b128(rs, l16, ao[(k) < 8 ? (k) >> 1 : (k) - 8] + ((k) < 8 ? ((k) & 1) * 4096 : 8192) + (t) * 1024, (EXP & 8) ? 0 : SC1)
      u32x4 Bw[4][5];
#pragma unroll
      for (int k = 0; k < 4; ++k)
#pragma unroll
        for (int n = 0; n < 5; ++n) Bw[k][n] = BFRAG(k, n);
      STAMP(4);
      wd_dma(lane, sw, 0, 2);
      flag_wait(fb, FL_G1, 4);
      wd_dma(lane, sw, 2, 4);
      lptr park = lds + SL2 + sw * 10240 + (lane >> 2) * 160 + (lane & 3) * 8;
      asm volatile("" : "+v"(park));
      STAMP(7);
      const unsigned fo = WS_MB + (tg * 8 + nb) * MBS + (sw * 8 + (lane & 7)) * 8;
      _Pragma("nounroll") for (int sp = 0; sp < SPIN_GLB; ++sp) { if (wall(gflag_ok(ws, fo, tag))) break; __builtin_amdgcn_s_sleep(2); }
      asm volatile("" ::: "memory");
      if (EXP & 1024) __builtin_amdgcn_s_sleep(127);
      STAMP(8);
      f32x4 a2[5][4];
#pragma unroll
      for (int n = 0; n < 5; ++n)
#pragma unroll
        for (int tb = 0; tb < 4; ++tb) a2[n][tb] = f32x4{0, 0, 0, 0};
      u32x4 Ah[4][4];
#pragma unroll
      for (int k = 0; k < 3; ++k)
#pragma unroll
        for (int tb = 0; tb < 4; ++tb) Ah[k][tb] = AFRAG(k, tb);
      u64 f2 = 0;
      SBAR();
#pragma unroll
      for (int k = 0; k < 12; ++k) {
        if (k == 3) { f2 = __hip_atomic_load((GAS u64*)(ws + fo + 256), RLX_AGENT); SBAR(); }
        if (k == 5) {
          unsigned tc;
          asm volatile("s_mov_b32 %1, %5\n\t1:\n\tv_cmp_ne_u64 vcc, %2, %0\n\ts_cbranch_vccz 2f\n\ts_sleep 2\n\t"
                       "global_load_dwordx2 %0, %3, %4 offset:256 sc1\n\ts_sub_u32 %1, %1, 1\n\ts_waitcnt vmcnt(0)\n\ts_cbranch_scc0 1b\n\t2:"
                       : "+v"(f2), "=&s"(tc) : "s"(tag), "v"(fo), "s"(ws), "i"(SPIN_GLB) : "memory", "vcc", "scc");
          SBAR();
        }
        u32x4 hf[4];
        if (k >= 8) {
          const u32x4 X0 = Ah[k & 3][0], X1 = Ah[k & 3][1], Y0 = Ah[k & 3][2], Y1 = Ah[k & 3][3];
          hf[0] = u32x4{X0[0], X0[1], Y0[0], Y0[1]}; hf[1] = u32x4{X0[2], X0[3], Y0[2], Y0[3]};
          hf[2] = u32x4{X1[0], X1[1], Y1[0], Y1[1]}; hf[3] = u32x4{X1[2], X1[3], Y1[2], Y1[3]};
        } else {
#pragma unroll
          for (int tb = 0; tb < 4; ++tb) hf[tb] = Ah[k & 3][tb];
        }
#pragma unroll
        for (int e = 0; e < 20; ++e) {
          const int n = e >> 2, tb = e & 3;
          a2[n][tb] = MFMA_BF(Bw[k & 3][n], hf[tb], a2[n][tb]);
          if (e & 1) {
            const int j = e >> 1;
            if (j < 4) { if (k + 3 < 12) Ah[(k + 3) & 3][j] = AFRAG(k + 3, j); }
            else if (j < 9 && k >= 1 && k + 3 < 12) {
              if (k + 3 < 8) Bw[(k + 3) & 3][j - 4] = BFRAG(k + 3, j - 4);
              else Bw[(k + 3) & 3][j - 4] = tr2(park + ((k - 5) >> 1) * (SL0 - SL2) + ((k - 5) & 1) * 5120 + (j - 4) * 32, 2560);
            }
            SBAR();
          }
        }
#pragma unroll
        for (int n = 0; n < 5; ++n) asm volatile("" : "+v"(a2[n][0]), "+v"(a2[n][1]), "+v"(a2[n][2]), "+v"(a2[n][3]));
        if ((EXP & 512) && k < 11) ARR();
      }
      STAMP(9);
      {
        lptr red = lds + SL2 + sw * 10240;
        const int l2 = fresh_lane(), r = l2 & 15, q = l2 >> 4;
#pragma unroll
        for (int n = 0; n < 5; ++n)
#pragma unroll
          for (int tb = 0; tb < 4; ++tb) {
            *(LAS f32x4*)(red + (q >> 1) * (SL0 - SL2) + (2 * n + (q & 1)) * 1024 + ((tb * 16 + r + 3 * n) & 63) * 16) = a2[n][tb];
          }
      }
    }
  }
  STAMP(10);
  BAR_L();
  STAMP(11);
  if (wid < 5) {
    const int tid = wid * 64 + fresh_lane();
    const int tok = (tid * 13108) >> 16, g = tid - tok * 5;
    lptr rp = lds + SL2 + g * 2048 + ((tok + 3 * g) & 63) * 16;
    f32x4 s[4];
#pragma unroll
    for (int m = 0; m < 4; ++m) {
      lptr p = rp + (m >> 1) * (SL0 - SL2) + (m & 1) * 1024;
      s[m] = (*(LAS f32x4*)(p) + *(LAS f32x4*)(p + 10240)) + (*(LAS f32x4*)(p + 20480) + *(LAS f32x4*)(p + 30720));
    }
    u16* op = out + (size_t)(tg * 64 + tok) * DM + nb * 80 + g * 16;
#pragma unroll
    for (int hh = 0; hh < 2; ++hh) {
      const u32x4 bs = ld16(lds + L_BASE + tid * 32 + hh * 16), xs = ld16(lds + L_XT + tid * 32 + hh * 16);
      const f32x4 sa = s[2 * hh], sb = s[2 * hh + 1];
      *(u32x4*)(op + hh * 8) = u32x4{pkbf((bflo(xs[0]) + bflo(bs[0])) + sa[0], (bfhi(xs[0]) + bfhi(bs[0])) + sa[1]), pkbf((bflo(xs[1]) + bflo(bs[1])) + sa[2], (bfhi(xs[1]) + bfhi(bs[1])) + sa[3]),
                                     pkbf((bflo(xs[2]) + bflo(bs[2])) + sb[0], (bfhi(xs[2]) + bfhi(bs[2])) + sb[1]), pkbf((bflo(xs[3]) + bflo(bs[3])) + sb[2], (bfhi(xs[3]) + bfhi(bs[3])) + sb[3])};
    }
  }
}

#if defined(__HIP_DEVICE_COMPILE__)
#pragma clang attribute pop
#endif

extern "C" void kernel_launch(void* const* d_in, const int* in_sizes, int n_in, void* d_out, int out_size, void* d_ws, size_t ws_size, hipStream_t stream) {
  (void)in_sizes; (void)n_in; (void)out_size; (void)ws_size;
  (void)hipFuncSetAttribute((const void*)fused_kernel, hipFuncAttributeMaxDynamicSharedMemorySize, LDS_BYTES);
  fused_kernel<<<256, 512, LDS_BYTES, stream>>>((const u16*)d_in[0], (const u16*)d_in[1], (const u16*)d_in[2], (const u16*)d_in[3], (const u16*)d_in[4],
                                                (const u16*)d_in[5], (const u16*)d_in[6], (const u16*)d_in[8], (const u16*)d_in[9], (const u16*)d_in[10],
                                                (u16*)d_out, (char*)d_ws);
}
```

```cpp
#include <hip/hip_runtime.h>
#include <stdint.h>

#if defined(__HIP_DEVICE_COMPILE__)
#pragma clang attribute push(__attribute__((target("no-packed-fp32-ops"))), apply_to = function)
#endif
typedef unsigned short u16;
typedef unsigned long long u64;
typedef short bf16x8 __attribute__((ext_vector_type(8)));
typedef _Float16 f16x8 __attribute__((ext_vector_type(8)));
typedef short s16x4 __attribute__((ext_vector_type(4)));
typedef float f32x2 __attribute__((ext_vector_type(2)));
typedef float f32x4 __attribute__((ext_vector_type(4)));
typedef unsigned u32x2 __attribute__((ext_vector_type(2)));
typedef unsigned u32x4 __attribute__((ext_vector_type(4)));
typedef _Float16 h16x2 __attribute__((ext_vector_type(2)));
typedef __bf16 b16x2 __attribute__((ext_vector_type(2)));
#define LAS __attribute__((address_space(3)))
#define GAS __attribute__((address_space(1)))
typedef LAS char* lptr;
#define DI __device__ __forceinline__
#define SBAR() __builtin_amdgcn_sched_barrier(0)
#define SGB(m, n) __builtin_amdgcn_sched_group_barrier(m, n, 0)
#define MFMA_BF(a, b, c) __builtin_amdgcn_mfma_f32_16x16x32_bf16(__builtin_bit_cast(bf16x8, a), __builtin_bit_cast(bf16x8, b), c, 0, 0, 0)
#define MFMA_H(a, b, c) __builtin_amdgcn_mfma_f32_16x16x32_f16(__builtin_bit_cast(f16x8, a), __builtin_bit_cast(f16x8, b), c, 0, 0, 0)
#define SC1 16
#define YS 8
#define YS0 16
#define HS 2
#define HSPLAN 0, 3, 6, 9, 12, 15, 18, 21, 24
#define PACE 2
#define EXP 0

constexpr int DM = 640, NM = 192;
constexpr unsigned WS_EPOCH = 0, WS_F0 = 4096, F0REP = 4352;
constexpr unsigned WS_MB = 262144, MBS = 1280;
constexpr unsigned WS_ST = 65536, WS_CS = 98304, WS_XB = 655360, WS_WD = WS_XB + 2621440 + 4096;
constexpr unsigned WS_H = WS_WD + 1966080, WS_Y = WS_H + 6291456, WS_END = WS_Y + 2621440;
constexpr unsigned WS_STAMP = 16u << 20;
constexpr int STAGE = 40960, SL1 = 0, SA = 40960, SL2 = 81920, SL0 = 122880, POOL_BIAS = SA;
constexpr int PROW = 784, PCH = 23 * PROW;
constexpr int KC1 = 4228, O_KREV = 128, O_KL = 8528;
constexpr int L_FLG = SA + 2 * PCH, L_SC = L_FLG + 128, L_STG = L_FLG + 192, STGW = 2304;
constexpr int L_STAT = L_STG, L_CS = L_STG + 1024;
constexpr int L_RTB = SL2 + 29696;
constexpr int L_LNT = L_STG;
constexpr int L_LNB = SL2 + 18432, LNR = 1344;
constexpr int UCH = 9232, UC1 = 4640;
constexpr int L_U = SL1 + 128, L_KC = L_U + 3 * UCH + 128, L_PC = L_KC + 8400;
constexpr int L_XT = SL1;
constexpr int L_BASE = SA, L_YB = SA + 10240;
constexpr int LDS_BYTES = 163840;
static_assert(O_KL + 1312 + 8192 <= PCH && L_LNB + 8 * LNR <= L_RTB && L_RTB + 10240 + 128 <= SL0 && L_PC + 4096 <= SA && L_STG + 2 * STGW <= SL0 && (L_KC & 15) == 0 && (L_PC & 15) == 0 && (L_STG & 15) == 0, "lds map");
enum { FL_F0 = 0, FL_EPI, FL_U, FL_KA, FL_KB, FL_KC, FL_KP, FL_PA, FL_PB, FL_PC, FL_YC, FL_YOK, FL_H1, FL_H2, FL_G1, FL_S2, FL_TAG = 16 };
constexpr int SPIN_LDS = 1 << 18, SPIN_GLB = 1 << 15;

DI bool wall(bool p) { return __builtin_amdgcn_ballot_w64(!p) == 0; }
DI unsigned umin(unsigned a, unsigned b) { return a < b ? a : b; }
DI unsigned umax(unsigned a, unsigned b) { return a > b ? a : b; }
DI float asf(unsigned v) { return __builtin_bit_cast(float, v); }
DI unsigned asu(float v) { return __builtin_bit_cast(unsigned, v); }
DI float bflo(unsigned v) { return asf(v << 16); }
DI float bfhi(unsigned v) { return asf(v & 0xffff0000u); }
DI float bf1(u16 v) { return asf((unsigned)v << 16); }
DI unsigned pkbf(float lo, float hi) { return __builtin_bit_cast(unsigned, __builtin_convertvector(f32x2{lo, hi}, b16x2)); }
DI unsigned pkh(f32x2 v) { return __builtin_bit_cast(unsigned, __builtin_convertvector(v, h16x2)); }
DI float rbf(float x) { return bflo(pkbf(x, x)); }
template <int CTRL> DI float dppf(float x) { return __builtin_bit_cast(float, __builtin_amdgcn_mov_dpp(__builtin_bit_cast(int, x), CTRL, 0xf, 0xf, true)); }
DI float row16_sum(float v) { v += dppf<0xB1>(v); v += dppf<0x4E>(v); v += dppf<0x124>(v); v += dppf<0x128>(v); return v; }

DI void glds4(const void* sb, unsigned v0, unsigned v1, unsigned v2, unsigned v3, unsigned lds) {
  asm volatile("s_mov_b32 m0, %5\n\ts_nop 0\n\tglobal_load_lds_dwordx4 %0, %4\n\tglobal_load_lds_dwordx4 %1, %4 offset:1024\n\t"
               "global_load_lds_dwordx4 %2, %4 offset:2048\n\tglobal_load_lds_dwordx4 %3, %4 offset:3072"
               :: "v"(v0), "v"(v1), "v"(v2), "v"(v3), "s"(sb), "s"(lds) : "memory");
}
DI void glds1(const void* sb, unsigned v0, unsigned lds) {
  asm volatile("s_mov_b32 m0, %2\n\ts_nop 0\n\tglobal_load_lds_dwordx4 %0, %1" :: "v"(v0), "s"(sb), "s"(lds) : "memory");
}
DI void glds2(const void* sb, unsigned v0, unsigned v1, unsigned lds) {
  asm volatile("s_mov_b32 m0, %3\n\ts_nop 0\n\tglobal_load_lds_dwordx4 %0, %2\n\tglobal_load_lds_dwordx4 %1, %2 offset:1024"
               :: "v"(v0), "v"(v1), "s"(sb), "s"(lds) : "memory");
}
template <int N> DI void waitv() { asm volatile("s_waitcnt vmcnt(%0)" :: "i"(N) : "memory"); }
DI void waitl() { asm volatile("s_waitcnt lgkmcnt(0)" ::: "memory"); }
DI u32x2 vtr(lptr p) { return __builtin_bit_cast(u32x2, __builtin_amdgcn_ds_read_tr16_b64_v4i16((LAS s16x4*)p)); }
DI u32x4 tr2(lptr p, int d) { u32x2 lo = vtr(p), hi = vtr(p + d); return u32x4{lo[0], lo[1], hi[0], hi[1]}; }
DI u32x4 ld16(lptr p) { return *(LAS u32x4*)p; }
DI void st16(lptr p, u32x4 v) { *(LAS u32x4*)p = v; }

DI unsigned flag_ld(unsigned addr) { unsigned r; asm volatile("ds_read_b32 %0, %1\n\ts_waitcnt lgkmcnt(0)" : "=v"(r) : "v"(addr) : "memory"); return __builtin_amdgcn_readfirstlane(r); }
DI void flag_wait(unsigned fb, int idx, unsigned need) { _Pragma("nounroll") for (int sp = 0; sp < SPIN_LDS && flag_ld(fb + idx * 4) < need; ++sp) __builtin_amdgcn_s_sleep(1); }
DI void flag_set(unsigned fb, int idx, unsigned v) {
  asm volatile("s_waitcnt lgkmcnt(0)\n\ts_mov_b64 exec, 1\n\tds_write_b32 %0, %1\n\ts_mov_b64 exec, -1" :: "v"(fb + idx * 4), "v"(v) : "memory"); }
DI unsigned flag_add(unsigned fb, int idx, int lane) {
  unsigned r; (void)lane;
  asm volatile("s_waitcnt lgkmcnt(0)\n\ts_mov_b64 exec, 1\n\tds_add_rtn_u32 %0, %1, %2\n\ts_mov_b64 exec, -1\n\ts_waitcnt lgkmcnt(0)" : "=&v"(r) : "v"(fb + idx * 4), "v"(1u) : "memory");
  return __builtin_amdgcn_readfirstlane(r);
}
#define RLX_AGENT __ATOMIC_RELAXED, __HIP_MEMORY_SCOPE_AGENT
DI bool gflag_ok(const char* ws, unsigned off, u64 tag) { return __hip_atomic_load((GAS u64*)(ws + off), RLX_AGENT) == tag; }

#define STAMP(i) do {} while (0)

DI f32x2 csq(f32x2 x) { const float t = x.x + x.x; return f32x2{__builtin_fmaf(x.x, x.x, -(x.y * x.y)), t * x.y}; }
DI h16x2 toh(f32x2 v) { return __builtin_convertvector(v, h16x2); }
DI h16x2 hmul(h16x2 x, h16x2 y) { return __builtin_elementwise_fma(h16x2{x.y, x.y}, h16x2{-y.y, y.x}, h16x2{x.x, x.x} * y); }
DI f32x2 cexp_p(float a2, float bh, float bl, float p) {
  const float e = __builtin_amdgcn_exp2f(a2 * p);
  const float ph = __builtin_fmaf(bl, p, __builtin_amdgcn_fractf(bh * p));
  return f32x2{e * __builtin_amdgcn_cosf(ph), e * __builtin_amdgcn_sinf(ph)};
}
template <int NS, int ROW0> DI void put4(const unsigned (&tb)[NS], const h16x2 (&a)[NS], const h16x2 (&b)[NS], const h16x2 (&c)[NS], const h16x2 (&d)[NS]) {
#pragma unroll
  for (int e = 0; e < NS; ++e)
    asm volatile("s_mov_b32 m0, %4\n\ts_nop 0\n\tds_write_addtid_b32 %0 offset:%5\n\tds_write_addtid_b32 %1 offset:%5+784\n\t"
                 "ds_write_addtid_b32 %2 offset:%5+1568\n\tds_write_addtid_b32 %3 offset:%5+2352"
                 :: "v"(a[e]), "v"(b[e]), "v"(c[e]), "v"(d[e]), "s"(tb[e]), "i"(ROW0 * PROW + POOL_BIAS) : "memory");
}
#define FORS _Pragma("unroll") for (int e = 0; e < NS; ++e)
template <int NS> DI void kpool(const unsigned (&tb)[NS], const float (&pv)[NS][4], const float (&ldt)[NS], bool join, float (&sc)[NS]) {
  float dt[NS];
  FORS dt[e] = rbf(__builtin_amdgcn_exp2f(ldt[e] * 1.44269504f));
  unsigned eb[NS]; float cs[NS];
  FORS {
    unsigned mi = umax(asu(pv[e][2]) & 0x7fffffffu, asu(pv[e][3]) & 0x7fffffffu);
    mi = umax(mi, asu(dppf<0xB1>(asf(mi)))); mi = umax(mi, asu(dppf<0x4E>(asf(mi)))); mi = umax(mi, asu(dppf<0x124>(asf(mi)))); mi = umax(mi, asu(dppf<0x128>(asf(mi))));
    eb[e] = umax(umax((unsigned)__builtin_amdgcn_readlane(mi, 0), (unsigned)__builtin_amdgcn_readlane(mi, 16)),
                 umax((unsigned)__builtin_amdgcn_readlane(mi, 32), (unsigned)__builtin_amdgcn_readlane(mi, 48))) >> 23;
  }
  if (join) eb[0] = eb[NS - 1] = umax(eb[0], eb[NS - 1]);
  FORS { eb[e] = umin(umax(eb[e], 2u), 252u); cs[e] = asf((254u - eb[e]) << 23); }
  float a2[NS], bh[NS], bl[NS];
  FORS {
    const float ea = rbf(__builtin_amdgcn_exp2f(pv[e][0] * 1.44269504f));
    a2[e] = -(ea * dt[e]) * 1.44269504f;
    const float b = pv[e][1] * dt[e];
    bh[e] = b * 0.15915494f;
    bl[e] = __builtin_fmaf(b, 6.4206382e-9f, __builtin_fmaf(b, 0.15915494f, -bh[e]));
  }
  h16x2 one[NS], w8[NS], w16[NS], w24[NS], w32[NS], w64[NS], w96[NS], w1[NS], w2[NS], w3[NS], w4[NS], C[NS], c4[NS];
  FORS {
    const f32x2 f1 = cexp_p(a2[e], bh[e], bl[e], 1.f), f2 = csq(f1), f4 = csq(f2);
    const f32x2 f8 = csq(f32x2{f4.x, -f4.y}), f16 = csq(f8), f32 = csq(f16), f64 = csq(f32);
    one[e] = h16x2{(_Float16)1.f, (_Float16)0.f};
    w8[e] = toh(f8); w16[e] = toh(f16); w32[e] = toh(f32); w64[e] = toh(f64); w24[e] = hmul(w8[e], w16[e]); w96[e] = hmul(w32[e], w64[e]);
    w1[e] = toh(f1); w2[e] = toh(f2); w4[e] = toh(f4); w3[e] = hmul(w1[e], w2[e]);
    C[e] = toh(f32x2{pv[e][2] * cs[e], pv[e][3] * cs[e]}); c4[e] = hmul(C[e], w4[e]);
  }
  put4<NS, 0>(tb, one, w8, w16, w24);
  put4<NS, 4>(tb, w32, w64, w96, w1);
  put4<NS, 8>(tb, w2, w3, C, c4);
  h16x2 c128[NS], c132[NS], w256[NS], w258[NS], w512[NS], w768[NS], w1024[NS], w1280[NS], w1536[NS], w1792[NS], w1796[NS];
  FORS {
    const f32x2 f128 = cexp_p(a2[e], bh[e], bl[e], 128.f), f256 = csq(f128), f512 = csq(f256);
    const h16x2 w128 = toh(f128);
    w256[e] = toh(f256); w512[e] = toh(f512); c128[e] = hmul(C[e], w128); c132[e] = hmul(c4[e], w128);
    w258[e] = hmul(w256[e], w2[e]); w768[e] = hmul(w512[e], w256[e]);
    w1024[e] = toh(csq(f512)); w1280[e] = hmul(w1024[e], w256[e]); w1536[e] = hmul(w1024[e], w512[e]);
    w1792[e] = hmul(w1024[e], w768[e]); w1796[e] = hmul(w1792[e], w4[e]);
  }
  put4<NS, 12>(tb, c128, c132, w256, w258);
  put4<NS, 16>(tb, w512, w768, w1024, w1280);
  put4<NS, 19>(tb, w1280, w1536, w1792, w1796);
  FORS sc[e] = 2.f * dt[e] * asf(eb[e] << 23);
}
struct KPtr {
  lptr pbx, pby, py, px0, px2;
  DI void init(lptr pool, int lane) {
    const int r = lane & 15, x = r & 3, y = r >> 2;
    lptr p = pool + (lane >> 4) * 16;
    pbx = p + x * PROW; pby = p + (y ? 3 + y : 0) * PROW; py = p + (10 + y) * PROW; px0 = p + (x ? 6 + x : 0) * PROW; px2 = p + (y == 3 ? 22 : 18 + x) * PROW;
  }
  DI void fetch(u32x4 (&v)[6], int o) const {
    v[0] = ld16(pbx + o); v[1] = ld16(pby + o); v[2] = ld16(py + o); v[3] = ld16(px0 + o); v[4] = ld16(pbx + 14 * PROW + o); v[5] = ld16(px2 + o);
  }
  DI void advance() { pbx += 128; pby += 128; py += 128; px0 += 128; px2 += 128; }
};
DI void kprod(const u32x4 (&v)[6], u32x4 (&o)[4]) {
  unsigned t[16];
  if (EXP & 2048) { SBAR(); __builtin_amdgcn_s_setprio(3); SBAR(); }
#pragma unroll
  for (int i = 0; i < 16; ++i) { const unsigned xe = v[(i >> 2) ? 2 + (i >> 2) : 0][i & 3], ye = v[(i >> 2) ? 2 : 1][i & 3];
    asm("v_pk_mul_f16 %0, %1, %2 op_sel_hi:[0,1]" : "=v"(t[i]) : "v"(xe), "v"(ye)); }
  SBAR();
#pragma unroll
  for (int i = 0; i < 16; ++i) { const unsigned xe = v[(i >> 2) ? 2 + (i >> 2) : 0][i & 3], ye = v[(i >> 2) ? 2 : 1][i & 3]; unsigned z;
    asm("v_pk_fma_f16 %0, %1, %2, %3 op_sel:[1,1,0] op_sel_hi:[1,0,1] neg_lo:[0,1,0]" : "=v"(z) : "v"(xe), "v"(ye), "v"(t[i])); o[i >> 2][i & 3] = z; }
  SBAR();
}
template <int H> DI void kprodh(const u32x4 (&v)[6], u32x4 (&o)[4]) {
  unsigned t[8];
#pragma unroll
  for (int i = 8 * H; i < 8 * H + 8; ++i) { const unsigned xe = v[(i >> 2) ? 2 + (i >> 2) : 0][i & 3], ye = v[(i >> 2) ? 2 : 1][i & 3];
    asm("v_pk_mul_f16 %0, %1, %2 op_sel_hi:[0,1]" : "=v"(t[i & 7]) : "v"(xe), "v"(ye)); }
  SBAR();
#pragma unroll
  for (int i = 8 * H; i < 8 * H + 8; ++i) { const unsigned xe = v[(i >> 2) ? 2 + (i >> 2) : 0][i & 3], ye = v[(i >> 2) ? 2 : 1][i & 3]; unsigned z;
    asm("v_pk_fma_f16 %0, %1, %2, %3 op_sel:[1,1,0] op_sel_hi:[1,0,1] neg_lo:[0,1,0]" : "=v"(z) : "v"(xe), "v"(ye), "v"(t[i & 7])); o[i >> 2][i & 3] = z; }
  asm volatile("" : "+v"(o[2 * H]), "+v"(o[2 * H + 1]));
  SBAR();
}
DI void kmma1(const u32x4 (&o0)[4], f32x4 (&acc)[3], bool first) {
  SBAR(); asm volatile("s_setprio 3\n\ts_nop 1"); SBAR();
#pragma unroll
  for (int i = 0; i < 3; ++i) acc[i] = MFMA_H(o0[1 + i], o0[0], (first ? f32x4{0, 0, 0, 0} : acc[i]));
  SBAR(); __builtin_amdgcn_s_setprio(0); SBAR();
}
DI void kcombine(f32x4 (&ko)[3], const f32x4 (&ka)[3][3], float s0, float s1, float s2) {
#pragma unroll
  for (int i = 0; i < 3; ++i)
#pragma unroll
    for (int j = 0; j < 4; ++j) ko[i][j] = __builtin_fmaf(ka[2][i][j], s2, __builtin_fmaf(ka[1][i][j], s1, ka[0][i][j] * s0));
}
DI float ubf(const u16* p, int i) { const unsigned w = ((const __attribute__((address_space(4))) unsigned*)(uintptr_t)p)[i >> 1]; return (i & 1) ? bfhi(w) : bflo(w); }
DI void kmma2(const u32x4 (&o0)[4], const u32x4 (&o1)[4], f32x4 (&acc)[3]) {
  SBAR(); asm volatile("s_setprio 3\n\ts_nop 1"); SBAR();
#pragma unroll
  for (int i = 0; i < 3; ++i) acc[i] = MFMA_H(o0[1 + i], o0[0], acc[i]);
#pragma unroll
  for (int i = 0; i < 3; ++i) acc[i] = MFMA_H(o1[1 + i], o1[0], acc[i]);
  SBAR(); __builtin_amdgcn_s_setprio(0); SBAR();
}
DI void kstore(lptr kl, int lane, const f32x4 (&acc)[3], float dsk) {
  const int c = lane & 15, q = lane >> 4;
  LAS u16* t = (LAS u16*)kl;
  const float k0 = acc[0][0] + (lane == 0 ? dsk : 0.f);
  *(LAS u32x2*)(t + 128 * (q >> 1) + 8 * c + 4 * (q & 1)) = u32x2{pkbf(k0, acc[0][1]), pkbf(acc[0][2], acc[0][3])};
  *(LAS unsigned*)(t + 256 + 64 * (q >> 1) + 2 * (q & 1) + 4 * c) = pkbf(acc[1][0], acc[1][1]);
  const unsigned p1 = pkbf(acc[1][2], acc[1][3]);
  const int i1 = 384 + 32 * (q >> 1) + (q & 1) + 2 * c;
  t[i1] = (u16)p1; t[i1 + 64] = (u16)(p1 >> 16);
  const unsigned r0 = pkbf(acc[2][0], acc[2][1]), r1 = pkbf(acc[2][2], acc[2][3]);
  if (!(q & 1)) { const int i2 = 512 + 8 * q + c; t[i2] = (u16)r0; t[i2 + 32] = (u16)(r0 >> 16); t[i2 + 64] = (u16)r1; t[i2 + 96] = (u16)(r1 >> 16); }
  else if (lane == 63) t[640] = (u16)r0;
}
DI unsigned dup16(unsigned a) { return a | (a << 16); }
DI unsigned bsel(unsigned m, unsigned a, unsigned b) { asm("" : "+v"(m)); return (a & m) | (b & ~m); }
DI unsigned rot16(unsigned a) { return __builtin_amdgcn_alignbit(a, a, 16); }
DI void kexpand(lptr kr, lptr kl, int lane) {
  LAS u16* t = (LAS u16*)kl;
  auto put = [&](int ch, unsigned dp, unsigned d0, unsigned d1, unsigned d2, unsigned d3) {
    st16(kr + 16 * ch, u32x4{d0, d1, d2, d3}); st16(kr + KC1 - 4 + 16 * ch, u32x4{dp, d0, d1, d2}); };
  const int n = 63 - lane, n0 = n & 31;
  const unsigned a0 = t[576 + n], b0 = t[577 + n], a1 = t[512 + n], b1 = t[513 + n];
  const unsigned ab = *(LAS unsigned*)(t + 384 + 2 * n), c2 = t[386 + 2 * n];
  const u32x2 v1 = *(LAS u32x2*)(t + 256 + 4 * n0); const unsigned q4 = t[260 + 4 * n0];
  const u32x4 v0 = ld16((lptr)(t + 8 * n0)); const unsigned w = *(LAS unsigned*)(t + 8 * n0 + 8);
  const unsigned mo = (n & 1) ? ~0u : 0u, lo = lane >= 32 ? ~0u : 0u, m31 = n == 31 ? ~0u : 0u;
  put(lane, dup16(b0), dup16(b0), bsel(mo, dup16(b0), b0 | (a0 << 16)), dup16(a0), dup16(a0));
  put(64 + lane, dup16(b1), dup16(b1), bsel(mo, dup16(b1), b1 | (a1 << 16)), dup16(a1), dup16(a1));
  put(128 + lane, dup16(c2), dup16(c2), dup16(ab >> 16), rot16(ab), dup16(ab & 0xffffu));
  put(192 + lane, bsel(lo, bsel(m31, dup16(w & 0xffffu), rot16(w)), dup16(q4)), bsel(lo, rot16(v0[3]), q4 | (v1[1] & 0xffff0000u)),
      bsel(lo, rot16(v0[2]), dup16(v1[1] & 0xffffu)), bsel(lo, rot16(v0[1]), (v1[1] & 0xffffu) | (v1[0] & 0xffff0000u)), bsel(lo, rot16(v0[0]), dup16(v1[0] & 0xffffu)));
  if (lane >= 60) put(319 - lane, lane == 63 ? rot16(v0[0]) : 0u, 0u, 0u, 0u, 0u);
}

struct ConvPlan { int n; int item[36][3]; };
constexpr ConvPlan conv_plan(int tmask, int maxt) {
  ConvPlan p{};
  int fifo[64] = {}, head = 0, tail = 0, carry[16] = {}, nc = 0, per = (tmask == 0xff) ? 1 : 2;
  for (int pass = 0; pass < 2; ++pass) {
    head = tail = 0;
    for (int i = 0; i < nc; ++i) fifo[tail++] = carry[i] + 32;
    int e = 0;
    for (int m = maxt; m >= 0; --m) {
      for (int j = 0; j + m <= maxt; ++j) {
        const bool on = (tmask >> (m + j)) & 1;
        int k = 0;
        if (m == 0) { if (on && j < maxt - 1) p.item[e][k++] = j; else fifo[tail++] = j; }
        if (j + m == maxt) { fifo[tail++] = 8 + 2 * m; fifo[tail++] = 9 + 2 * m; }
        if (on) { while (k < per && head < tail) p.item[e][k++] = fifo[head++]; while (k < 3) p.item[e][k++] = -1; ++e; }
      }
    }
    p.n = e; nc = 0;
    while (head < tail) carry[nc++] = fifo[head++];
  }
  return p;
}
template <int TMASK, class F>
DI void conv_units(lptr kr, lptr ub, int lane, int rho0, int nrho, f32x4 (&acc)[8], F hook) {
  constexpr int MAXT = (TMASK & 0x80) ? 7 : 5;
  constexpr ConvPlan plan = conv_plan(TMASK, MAXT);
  const int r = lane & 15, q = lane >> 4;
  lptr pk = kr + ((r & 1) ? 0 : KC1) + 4092 - 4 * r + 16 * q - 3584 - 64 * rho0 - 64;
  lptr pu = ub + ((r & 1) ? 0 : UC1) + 64 * (r >> 1) + 16 * q + 64 * (7 - rho0) - 64;
  lptr pk2 = pk + 8;
  u32x2 Kl[8], Kh[8]; u32x4 Uf[8];
  asm volatile("" : "+v"(pk), "+v"(pk2), "+v"(pu));
#pragma unroll
  for (int j = 0; j <= MAXT; ++j) { const int m = MAXT - j; Uf[j] = ld16(pu + 64 + 512 * j); Kl[m] = *(LAS u32x2*)(pk + 64 + 512 * (7 - m)); Kh[m] = *(LAS u32x2*)(pk2 + 64 + 512 * (7 - m)); SBAR(); }
#pragma unroll 1
  for (int it = 0; it < nrho; ++it) {
    int e = 0;
#pragma unroll
    for (int m = MAXT; m >= 0; --m) {
#pragma unroll
      for (int j = 0; j + m <= MAXT; ++j) {
        if ((TMASK >> (m + j)) & 1) {
          acc[m + j] = MFMA_BF(Uf[j], (u32x4{Kl[m][0], Kl[m][1], Kh[m][0], Kh[m][1]}), acc[m + j]);
#pragma unroll
          for (int k = 0; k < 3; ++k) {
            const int c = plan.item[e][k];
            if (c >= 0) {
              const int o = (c & 32) ? 64 : 0, cc = c & 31, mm = (cc - 8) >> 1;
              if (cc < 8) Uf[cc] = ld16(pu + o + 512 * cc);
              else if (cc & 1) Kh[mm] = *(LAS u32x2*)(pk2 + o + 512 * (7 - mm));
              else Kl[mm] = *(LAS u32x2*)(pk + o + 512 * (7 - mm));
            }
          }
          SBAR();
          ++e;
        }
      }
    }
    pk -= 64; pk2 -= 64; pu -= 64;
    asm volatile("" : "+v"(pk), "+v"(pk2), "+v"(pu));
    hook(it);
  }
}

template <int N> struct IC { static constexpr int value = N; };
DI u64 uni64(u64 v) { return (u64)(unsigned)__builtin_amdgcn_readfirstlane((unsigned)v) | ((u64)(unsigned)__builtin_amdgcn_readfirstlane((unsigned)(v >> 32)) << 32); }
DI int fresh_lane() { int l; asm volatile("v_mbcnt_lo_u32_b32 %0, -1, 0\n\tv_mbcnt_hi_u32_b32 %0, -1, %0" : "=v"(l)); return l; }
#define ARR() do {} while (0)
#define BAR() do { SBAR(); ARR(); asm volatile("s_barrier" ::: "memory"); SBAR(); } while (0)
#define BAR_L() do { SBAR(); asm volatile("s_waitcnt lgkmcnt(0)" ::: "memory"); SBAR(); ARR(); asm volatile("s_barrier" ::: "memory"); SBAR(); } while (0)
#define LD_SC1(off) __builtin_amdgcn_raw_buffer_load_b128(rs, (off), 0, SC1)
#define ST_SC1(v, off) __builtin_amdgcn_raw_buffer_store_b128((v), rs, (off), 0, SC1)

__global__ __launch_bounds__(512, 2) void fused_kernel(const u16* __restrict__ x, const u16* __restrict__ p_lar, const u16* __restrict__ p_aim,
                                                       const u16* __restrict__ p_cr, const u16* __restrict__ p_ci, const u16* __restrict__ p_ldt,
                                                       const u16* __restrict__ p_dsk, const u16* __restrict__ wg, const u16* __restrict__ wu,
                                                       const u16* __restrict__ wd, u16* __restrict__ out, char* ws) {
  extern __shared__ __attribute__((aligned(16))) char smem[];
  const int tid = __builtin_amdgcn_workitem_id_x(), bid = __builtin_amdgcn_workgroup_id_x();
  u64 pc_; asm volatile("s_getpc_b64 %0" : "=s"(pc_));
  lptr lds = (lptr)smem;
  const unsigned lds0 = (unsigned)(uintptr_t)smem;
  const unsigned fb = lds0 + L_FLG;
  const int wraw = __builtin_amdgcn_readfirstlane(tid >> 6), lane = tid & 63;
  const int wid = wraw ^ ((wraw >> 2) << 1);
  const int xcd = bid & 7, bi = bid >> 3;
  const int rb = (xcd >> 2) * 8 + (bi >> 2), cb = (xcd & 3) * 4 + (bi & 3);
  const int tg = (xcd >> 1) * 8 + (bi >> 2), nb = (xcd & 1) * 4 + (bi & 3);
  const int cw = xcd * 32 + bi, odd = cw & 1, c0 = 5 * (cw >> 1) + 2 * odd;
  const int chA = c0 + odd, chC = odd ? c0 : c0 + 2;
  const int rm = rb & 7, rot = 2 * (rm >= 5 ? rm - 5 : rm) + (rb >> 3);
  const bool csum = rm < 5;
  const __amdgpu_buffer_rsrc_t rs = __builtin_amdgcn_make_buffer_rsrc(ws, 0, (int)WS_END, 0x00020000);
  const int r = lane & 15, q = lane >> 4;

  u64 tag = 0;
  const int lw = wid - 4, kw = wid - 6;
  unsigned vx[4], vw[12];
  auto issue = [&](int s, int sbase, int h, int P0 = 0, int P1 = 5) {
    int ks = s + rot; if (ks >= 10) ks -= 10;
    const unsigned base = lds0 + sbase;
    const char* sx = (const char*)x + (size_t)(rb * 128 + h * 64) * 1280 - 4096 + ks * 128;
    const char* sw = (const char*)(h ? wu : wg) + cb * 192 - 4096 + (size_t)ks * 64 * 3072;
    if (P0 <= 0 && 0 < P1) glds4(sx, vx[0], vx[1], vx[2], vx[3], base + h * 8192);
    if (P0 <= 1 && 1 < P1) glds4(sw, vw[0], vw[1], vw[2], vw[3], base + 16384 + h * 12288);
    if (P0 <= 2 && 2 < P1) glds4(sx + 32 * 1280, vx[0], vx[1], vx[2], vx[3], base + h * 8192 + 4096);
    if (P0 <= 3 && 3 < P1) glds4(sw, vw[4], vw[5], vw[6], vw[7], base + 16384 + h * 12288 + 4096);
    if (P0 <= 4 && 4 < P1) glds4(sw, vw[8], vw[9], vw[10], vw[11], base + 16384 + h * 12288 + 8192);
  };
  auto prep_dma = [&](int ln) {
#pragma unroll
    for (int j = 0; j < 4; ++j) { const int row = 8 * j + (ln >> 3); vx[j] = row * 1280 + (((ln & 7) ^ ((row >> 1) & 7)) << 4) + 4096 - 1024 * j; }
#pragma unroll
    for (int j = 0; j < 3; ++j) { const int c = j * 64 + ln, row = (c * 43691) >> 19, pos = c - row * 12;
      const unsigned v = row * 3072 + ((pos ^ (((row >> 3) & 1) << 1)) << 4) + 4096;
#pragma unroll
      for (int g = 0; g < 4; ++g) vw[3 * g + j] = v + g * 16 * 3072 - 1024 * ((3 * g + j) & 3); }
  };
  auto wd_dma = [&](int ln, int w, int j0, int j1) {
    unsigned v[5];
#pragma unroll
    for (int i = 0; i < 5; ++i) { const int c = i * 64 + ln, row = (c * 6554) >> 16; v[i] = row * 1280 + (c - row * 10) * 16 + 4096 - (i & 3) * 1024; }
#pragma unroll
    for (int j = j0; j < j1; ++j) {
      const char* sp = (const char*)wd + (size_t)((w * 4 + ((j + nb) & 3)) * 96 + 32) * 1280 + nb * 160 - 4096;
      const unsigned d = lds0 + ((j >> 1) ? SL0 : SL2) + w * 10240 + (j & 1) * 5120;
      glds4(sp, v[0], v[1], v[2], v[3], d); glds1(sp, v[4], d + 4096);
    }
  };
  const int lnb = cw;
  const int ro0 = (0x100222 >> (4 * wid)) & 3, th0 = (0x000210 >> (4 * wid)) & 3, ro1 = (wid >> 1) & 1, th1 = 1 + (wid & 1);
  const int ch0 = c0 + ((0x0210 >> (4 * (ro0 + odd))) & 3), ch1 = chA + ro1;
  const int pt0 = ro0 * PCH + (ro0 >> 1) * (SL2 - SA - 2 * PCH) + th0 * 256, pt1 = ro1 * PCH + th1 * 256;
  unsigned o = (lane < 32 ? ch0 * 384 + th0 * 128 : ch1 * 384 + th1 * 128) + (lane & 31) * 4;
  unsigned xo0 = lane * 16, xo1 = q * 1280 + r * 16 + 1024;
  asm volatile("" : "+v"(o), "+v"(xo0), "+v"(xo1));
  asm volatile("" :: "s"(x), "s"(p_lar), "s"(p_aim), "s"(p_cr), "s"(p_ci), "s"(p_ldt), "s"(p_dsk), "s"(wg), "s"(wu), "s"(wd), "s"(out), "s"(ws));
  const int ptp = wid == 5 ? pt0 : pt1;
  if ((0x2f >> wid) & 1) {
    asm volatile("s_mov_b32 m0, %5\n\ts_nop 0\n\tglobal_load_lds_dword %0, %1\n\tglobal_load_lds_dword %0, %2 offset:784\n\t"
                 "global_load_lds_dword %0, %3 offset:1568\n\tglobal_load_lds_dword %0, %4 offset:2352"
                 :: "v"(o), "s"(p_lar), "s"((const char*)p_aim - 784), "s"((const char*)p_cr - 1568), "s"((const char*)p_ci - 2352), "s"(lds0 + SA + ptp) : "memory");
  }
  if (wid >= 6) {
    const char* xr = (const char*)x + (size_t)(lnb * 8 + kw * 4) * 1280;
#pragma unroll
    for (int j = 0; j < 4; ++j) glds1(xr + j * 1280, xo0, lds0 + L_LNB + (kw * 4 + j) * LNR);
    glds1(xr, xo1, lds0 + L_LNT + kw * 1024);
  } else if (wid >= 4) { if (lane < 16) *(LAS unsigned*)(lds + L_FLG + lane * 4) = 0; prep_dma(lane); }
  SBAR(); asm volatile("s_waitcnt lgkmcnt(0)\n\ts_barrier" ::: "memory"); SBAR();
  const float ldt0 = ubf(p_ldt, ch0), ldt1 = ubf(p_ldt, ch1);
  if (wid == 4 || wid == 5) {
    asm volatile("s_mov_b32 m0, %2\n\ts_mov_b64 exec, 0x1ff\n\tglobal_load_lds_dword %0, %1\n\ts_mov_b64 exec, -1"
                 :: "v"(((bi & 15) * 18 + lw * 9 + lane) * 128), "s"(pc_ & ~127ull), "s"(lds0 + L_RTB + 10240 + lw * 64) : "memory");
    issue(0, SL0, lw);
  }
  auto lds_params = [&](float (&pv)[4], int m) {
#pragma unroll
    for (int e = 0; e < 4; ++e) pv[e] = bf1(*(LAS u16*)(lds + SA + ptp + e * PROW + m * 2));
  };
  if (wid >= 6) {
    waitv<0>();
    STAMP(0);
    lptr xp = lds + L_LNB + (kw * 4 + q) * LNR + r * 16;
    u32x4 xv[5];
#pragma unroll
    for (int j = 0; j < 4; ++j) xv[j] = ld16(xp + 256 * j);
    xv[4] = ld16(lds + L_LNT + kw * 1024 + lane * 16);
    float v[40]; float s0 = 0.f, s1 = 0.f;
#pragma unroll
    for (int j = 0; j < 20; ++j) { const unsigned w = xv[j >> 2][j & 3]; v[2 * j] = bflo(w); v[2 * j + 1] = bfhi(w); s0 += v[2 * j]; s1 += v[2 * j + 1]; }
    const float mean = row16_sum(s0 + s1) * (1.f / DM);
    float q0 = 0.f, q1 = 0.f;
#pragma unroll
    for (int j = 0; j < 20; ++j) { v[2 * j] -= mean; v[2 * j + 1] -= mean; q0 = __builtin_fmaf(v[2 * j], v[2 * j], q0); q1 = __builtin_fmaf(v[2 * j + 1], v[2 * j + 1], q1); }
    const float var = rbf(row16_sum(q0 + q1) * (1.f / DM));
    const float mb = rbf(mean), sd = rbf(__builtin_amdgcn_sqrtf(rbf(var + 1e-5f)));
    const float rsd = __builtin_amdgcn_rcpf(sd), off = (mean - mb) * rsd;
#pragma unroll
    for (int j = 0; j < 20; ++j) xv[j >> 2][j & 3] = pkbf(__builtin_fmaf(v[2 * j], rsd, off), __builtin_fmaf(v[2 * j + 1], rsd, off));
#pragma unroll
    for (int j = 0; j < 5; ++j) st16(xp + 256 * j, xv[j]);
    if (r == 0) __builtin_amdgcn_raw_buffer_store_b64(u32x2{asu(mb), asu(sd)}, rs, WS_ST + (lnb * 8 + kw * 4 + q) * 8, 0, SC1);
  } else if (wid == 4) {
    const int un = umin(cw, 191) * 2, ukb = un >> 3, unb = un & 7;
    const int c3 = (ukb * 43) >> 7, kt = ukb - 3 * c3;
    const int t0 = kt == 0 ? 0 : kt == 1 ? 80 : 32, t1 = kt == 0 ? 0 : kt == 1 ? 48 : 32;
#pragma unroll
    for (int j = 0; j < 5; ++j) { const int c = j * 64 + lane, row = (c * 6554) >> 16, pos = c - row * 10;
      const unsigned v = (c3 * 96 + row + (row < 16 ? t0 : t1)) * 1280 + unb * 160 + pos * 16;
      glds1(wd, v, lds0 + L_RTB + j * 1024); glds1((const char*)wd + 160, v, lds0 + L_RTB + 5120 + j * 1024); }
  } else {
    if (wid == 5) waitv<21>(); else waitv<0>();
    STAMP(0);
    float pv[1][4], pn[4];
    lds_params(pv[0], lane); lds_params(pn, 64 + lane);
    unsigned tb[1] = {lds0 + pt0}, sa_ = lds0 + L_SC + (ro0 * 3 + th0) * 4, inc = wid < 3;
    float ldt[1] = {ldt0};
#pragma unroll 1
    for (int rnd = wid == 5; rnd < 2; ++rnd) {
      float sc[1];
      kpool<1>(tb, pv, ldt, false, sc);
      asm volatile("s_mov_b64 exec, 1\n\tds_write_b32 %0, %1\n\tds_add_u32 %2, %3\n\ts_mov_b64 exec, -1" :: "v"(sa_), "v"(sc[0]), "v"(fb + FL_KP * 4), "v"(inc) : "memory");
#pragma unroll
      for (int e = 0; e < 4; ++e) pv[0][e] = pn[e];
      tb[0] = lds0 + pt1; sa_ = lds0 + L_SC + (ro1 * 3 + th1) * 4; inc = 0; ldt[0] = ldt1;
    }
  }
  STAMP(1);

  if (wid >= 6) {
    const int lane = fresh_lane(), r = lane & 15, q = lane >> 4;
    const int un = umin(cw * 2 + kw, 383), ukb = un >> 3, unb = un & 7;
    const float dsk = ubf(p_dsk, chA + kw);
    u32x4 xv[5];
    u64 ep = __hip_atomic_load((GAS u64*)(ws + WS_EPOCH), RLX_AGENT);
    BAR_L();
    u32x4 wt[5], wf[12];
#pragma unroll
    for (int j = 0; j < 5; ++j) xv[j] = tr2(lds + L_LNB + (r >> 2) * LNR + (kw * 5 + j) * 128 + q * 32 + (lane & 3) * 8, 4 * LNR);
    {
      const int sg = (q & 1) * 32;
      lptr pa = lds + SL0 + 16384 + kw * 12288 + (8 * q + (r >> 2)) * 192 + 8 * (lane & 3);
#pragma unroll
      for (int t = 0; t < 12; ++t) wf[t] = tr2(pa + (t / 6) * 6144 + (t % 6) * 32 + ((t & 1) ? -sg : sg), 768);
    }
    lptr pool = lds + SA + kw * PCH;
    KPtr kp; kp.init(pool, lane);
    u32x4 kv[2][6], kq[4];
    kp.fetch(kv[0], 0); kp.fetch(kv[1], 64);
    SBAR();
#pragma unroll
    for (int j = 0; j < 5; ++j) wt[j] = tr2(lds + L_RTB + kw * 5120 + (4 * q + (r >> 2)) * 160 + j * 32 + (lane & 3) * 8, 2560);
    BAR_L();
    tag = uni64(ep) + 1;
    *(volatile LAS u64*)(lds + L_FLG + FL_TAG * 4) = tag;
    unsigned pf_sink = 0;
    f32x4 ko[3], ka[3][3];
    u32x4 f0 = {0, 0, 0, 0}, f1 = {0, 0, 0, 0};
    constexpr int HS0[9] = {HSPLAN};
#pragma unroll
    for (int sl = 0; sl < 8; ++sl) {
      if (sl == 6 && kw == 0) { f0 = LD_SC1(WS_F0 + xcd * F0REP + lane * 16); f1 = LD_SC1(WS_F0 + xcd * F0REP + 1024 + lane * 16); SBAR(); }
      if (sl == 3 && kw == 0 && lane < 8) __hip_atomic_store((GAS u64*)(ws + WS_F0 + lane * F0REP + cw * 8), tag, RLX_AGENT);
#pragma unroll
      for (int hs = HS0[sl]; hs < HS0[sl + 1]; ++hs) {
        const int k = hs >> 1;
        if (!(hs & 1)) kprodh<0>(kv[k & 1], kq);
        else { kprodh<1>(kv[k & 1], kq); if (k + 2 < 12) kp.fetch(kv[k & 1], 64 * (k + 2)); kmma1(kq, ka[k >> 2], (k & 3) == 0); }
      }
      if (sl == 0) {
#pragma unroll
        for (int j = 0; j < 5; ++j) ST_SC1(xv[j], WS_XB + lnb * 10240 + (kw * 5 + j) * 1024 + lane * 16);
      }
      if (sl == 1) {
        if (csum) {
          const u32x4 ones = {0x3f803f80u, 0x3f803f80u, 0x3f803f80u, 0x3f803f80u};
          f32x4 cs[6];
          SBAR(); __builtin_amdgcn_s_setprio(3); SBAR();
#pragma unroll
          for (int t = 0; t < 6; ++t) cs[t] = MFMA_BF(ones, wf[t], (f32x4{0, 0, 0, 0}));
#pragma unroll
          for (int t = 0; t < 6; ++t) cs[t] = MFMA_BF(ones, wf[6 + t], cs[t]);
          SBAR(); __builtin_amdgcn_s_setprio(0); SBAR();
          const float cs0 = q == 0 ? cs[0][0] : q == 1 ? cs[1][0] : q == 2 ? cs[2][0] : cs[3][0], cs1 = q == 0 ? cs[4][0] : cs[5][0];
          const unsigned co = WS_CS + (cb * 10 + rot) * 768 + kw * 384 + lane * 4;
          __builtin_amdgcn_raw_buffer_store_b32(asu(cs0), rs, co, 0, SC1);
          if (lane < 32) __builtin_amdgcn_raw_buffer_store_b32(asu(cs1), rs, co + 256, 0, SC1);
        }
        if (cw < 192) {
#pragma unroll
          for (int f = 0; f < 5; ++f) ST_SC1(wt[f], WS_WD + (unb * 48 + ukb) * 5120 + f * 1024 + lane * 16);
        }
      }
      if (sl == 2) waitv<0>();
      if (sl == 6 && kw == 0) {
        const unsigned tl = (unsigned)tag, th = (unsigned)(tag >> 32);
        _Pragma("nounroll") for (int sp = 0; sp < SPIN_GLB; ++sp) {
          if (wall(((f0[0] ^ tl) | (f0[1] ^ th) | (f0[2] ^ tl) | (f0[3] ^ th) | (f1[0] ^ tl) | (f1[1] ^ th) | (f1[2] ^ tl) | (f1[3] ^ th)) == 0u)) break;
          f0 = LD_SC1(WS_F0 + xcd * F0REP + lane * 16); f1 = LD_SC1(WS_F0 + xcd * F0REP + 1024 + lane * 16);
          __builtin_amdgcn_s_sleep(2);
        }
      }
      if (false) {
        const unsigned po = WS_WD + nb * 245760 + (bi >> 2) * 30720 + umin(((sl - 6) * 2 + kw) * 64 + lane, 239u) * 128;
        asm volatile("global_load_dword %0, %1, %2 sc1" : "+v"(pf_sink) : "v"(po), "s"(ws) : "memory");
      }
      BAR();
    }
    STAMP(2);
    kcombine(ko, ka, *(LAS float*)(lds + L_SC + kw * 12), *(LAS float*)(lds + L_SC + kw * 12 + 4), *(LAS float*)(lds + L_SC + kw * 12 + 8));
    kstore(pool + O_KL, lane, ko, dsk);
    kexpand(pool + O_KREV, pool + O_KL, lane);
    flag_set(fb, FL_KA + kw, 1);
    asm volatile("s_waitcnt vmcnt(0)" : "+v"(pf_sink) :: "memory");
    STAMP(3);
  } else {
    if (wid >= 4) {
      const int lane = fresh_lane();
      u32x4 cv[6][6];
      {
        KPtr kp; kp.init(lds + SL2 + lw * 384, lane);
        flag_wait(fb, FL_KP, 3);
#pragma unroll
        for (int i = 0; i < 6; ++i) kp.fetch(cv[i], 64 * i);
      }
      const float sca = *(LAS float*)(lds + L_SC + 24 + lw * 4), scb = *(LAS float*)(lds + L_SC + 24 + lw * 8), scc = *(LAS float*)(lds + L_SC + 28 + lw * 4);
      SBAR();
      prep_dma(fresh_lane());
      if (!lw) { issue(1, SL1, 0); issue(1, SL1, 1); waitv<40>(); } else waitv<0>();
      BAR_L();
      issue(2, SL2, lw, 0, 1); issue(2, SL2, lw, 2, 3);
      f32x4 ka[3][3], kc[3];
#pragma unroll
      for (int s = 0; s < 7; ++s) {
        if (s == 0) waitv<8>(); else waitv<20>();
        BAR();
        if (s == 0) { issue(2, SL2, lw, 1, 2); issue(2, SL2, lw, 3, 5); }
        issue(s + 3, s % 3 == 0 ? SL0 : s % 3 == 1 ? SL1 : SL2, lw);
        if (s >= 1 && !(EXP & 4096)) { u32x4 o[4]; kprod(cv[s - 1], o); kmma1(o, ka[(s - 1) >> 1], s & 1); }
      }
      kcombine(kc, ka, sca, scb, scc);
      if (lw == 0) {
#pragma unroll
        for (int i = 0; i < 3; ++i) *(LAS f32x4*)(lds + L_STG + i * 1024 + lane * 16) = kc[i];
      }
      waitv<20>();
      BAR_L();
      STAMP(2);
      tag = uni64(*(volatile LAS u64*)(lds + L_FLG + FL_TAG * 4));
      u32x4 uc[8]; unsigned uprev;
      const int n0 = lw * 128 + (lane >> 2) * 8;
      const unsigned uo = WS_XB + n0 * 10240 + (c0 + (lane & 3)) * 16;
      u32x4 sv, cp[10];
      if (lw == 0) {
        sv = LD_SC1(WS_ST + rb * 1024 + lane * 16);
#pragma unroll
        for (int j = 0; j < 10; ++j) cp[j] = LD_SC1(WS_CS + (cb * 10 + j) * 768 + (lane < 48 ? lane : 0) * 16);
      }
#pragma unroll
      for (int j = 0; j < 8; ++j) uc[j] = LD_SC1(uo + j * 10240);
      uprev = __builtin_amdgcn_raw_buffer_load_b32(rs, uo - 10240 + 12, 0, SC1);
      SBAR();
      if (lw) {
#pragma unroll
        for (int i = 0; i < 3; ++i) kc[i] += *(LAS f32x4*)(lds + L_STG + i * 1024 + lane * 16);
        kstore(lds + L_PC, lane, kc, ubf(p_dsk, chC));
        waitv<9>();
      } else waitv<20>();
      BAR();
      STAMP(3);
      if (lw == 0) {
        f32x4 cs = {0, 0, 0, 0};
#pragma unroll
        for (int j = 0; j < 10; ++j) cs += __builtin_bit_cast(f32x4, cp[j]);
        st16(lds + L_STAT + lane * 16, sv);
        if (lane < 48) *(LAS f32x4*)(lds + L_CS + lane * 16) = cs;
        flag_set(fb, FL_EPI, 1);
        if (bid == 0 && lane == 0) __hip_atomic_store((GAS u64*)(ws + WS_EPOCH), tag, RLX_AGENT);
      }
      if (n0 == 0) uprev = 0;
      if ((lane & 3) < 3) {
        lptr ud = lds + L_U + (lane & 3) * UCH + 448 + n0 * 16;
#pragma unroll
        for (int j = 0; j < 8; ++j) {
          st16(ud + j * 16, uc[j]);
          const unsigned pw_ = j ? uc[j - 1][3] : uprev;
          st16(ud + UC1 + j * 16, u32x4{__builtin_amdgcn_alignbit(uc[j][0], pw_, 16), __builtin_amdgcn_alignbit(uc[j][1], uc[j][0], 16),
                                        __builtin_amdgcn_alignbit(uc[j][2], uc[j][1], 16), __builtin_amdgcn_alignbit(uc[j][3], uc[j][2], 16)});
        }
      }
      if (lane < 28) {
#pragma unroll
        for (int c = 0; c < 3; ++c) st16(lds + L_U + c * UCH + lw * UC1 + lane * 16, u32x4{0, 0, 0, 0});
      }
      flag_add(fb, FL_U, lane);
      if (lw) { kexpand(lds + L_KC, lds + L_PC, lane); flag_set(fb, FL_KC, 1); }

    } else {
      f32x4 acc[2][3][4];
      if (!(EXP & 4)) __builtin_amdgcn_s_setprio(2);
      const int tw = wid >> 1, fw = wid & 1;
      lptr pB0 = lds + (tw * 64 + r) * 128 + ((q ^ (r >> 1)) << 4);
      lptr pB1 = lds + (tw * 64 + r) * 128 + (((q ^ (r >> 1)) ^ 4) << 4);
      const int sg = (q & 1) * 32;
      lptr pAb = lds + 16384 + (8 * q + (r >> 2)) * 192 + 8 * (lane & 3);
      lptr pA[3] = {pAb + (fw ? 160 - sg : sg), pAb + (fw ? 128 + sg : 32 - sg), pAb + (fw ? 96 - sg : 64 + sg)};
      u32x4 A0[2][3], B0[4], A1[2][3], B1[4];
      auto rd = [&](u32x4 (&A)[2][3], u32x4 (&B)[4], int off, int kk) {
#pragma unroll
        for (int tb = 0; tb < 4; ++tb) B[tb] = ld16((kk ? pB1 : pB0) + off + tb * 2048);
#pragma unroll
        for (int m = 0; m < 2; ++m)
#pragma unroll
          for (int ii = 0; ii < 3; ++ii) A[m][ii] = tr2(pA[ii] + off + m * 12288 + kk * 6144, 768);
      };
      auto mm = [&](u32x4 (&A)[2][3], u32x4 (&B)[4]) {
#pragma unroll
        for (int e = 0; e < 24; ++e) { const int m = e / 12, ii = (e / 4) % 3, tb = e % 4; acc[m][ii][tb] = MFMA_BF(A[m][ii], B[tb], acc[m][ii][tb]); }
      };
      auto sched = [&]() {
#pragma unroll
        for (int k = 0; k < 16; ++k) { SGB(0x8, 1); SGB(0x100, 1); }
        SGB(0x8, 8);
      };
      BAR_L();
      STAMP(2);
      rd(A0, B0, SL0, 0);
      SBAR();
      rd(A1, B1, SL0, 1);
#pragma unroll
      for (int e = 0; e < 24; ++e) { const int m = e / 12, ii = (e / 4) % 3, tb = e % 4; acc[m][ii][tb] = MFMA_BF(A0[m][ii], B0[tb], (f32x4{0, 0, 0, 0})); }
      sched();
      int s0 = SL1, s1 = SL2, s2 = SL0;
#pragma unroll 1
      for (int s = 0; s < 9; ++s) {
        BAR_L();
        rd(A0, B0, s0, 0); mm(A1, B1); sched();
        SBAR();
        rd(A1, B1, s0, 1); mm(A0, B0); sched();
        const int t = s0; s0 = s1; s1 = s2; s2 = t;
      }
      SBAR();
      flag_add(fb, FL_G1, lane);
      mm(A1, B1);
      SBAR();
      if (EXP & 64) __builtin_amdgcn_s_setprio(0);
      STAMP(3);
      flag_wait(fb, FL_EPI, 1);
      tag = uni64(*(volatile LAS u64*)(lds + L_FLG + FL_TAG * 4));
      STAMP(4);
      {
        float nmb[4], al[4], sd[4];
#pragma unroll
        for (int tb = 0; tb < 4; ++tb) { const f32x2 st = *(LAS f32x2*)(lds + L_STAT + (tw * 64 + tb * 16 + r) * 8); nmb[tb] = -st.x; sd[tb] = st.y; al[tb] = -1.44269504f * __builtin_amdgcn_rcpf(st.y); }
        const unsigned hoff = WS_H + ((rb * 2 + tw) * 16 + cb) * 12288 + lane * 16;
        auto hcalc = [&](const f32x4& g4, const f32x4& u4, const f32x4& cg, const f32x4& cu, int tb) {
          float hv[4];
#pragma unroll
          for (int j = 0; j < 4; ++j) {
            const float g = __builtin_fmaf(nmb[tb], cg[j], g4[j]) * al[tb];
            const float u = __builtin_fmaf(nmb[tb], cu[j], u4[j]);
            const float e = __builtin_amdgcn_exp2f(g);
            hv[j] = u * __builtin_amdgcn_rcpf(__builtin_fmaf(e, sd[tb], sd[tb]));
          }
          return u32x2{pkbf(hv[0], hv[1]), pkbf(hv[2], hv[3])};
        };
        lptr pcs = lds + L_CS + (fw * 48 + 4 * q) * 4;
        const int oa = fw ? 128 : 0, oc = fw ? 0 : 128;
        {
          const f32x4 cga = *(LAS f32x4*)(pcs + oa), cua = *(LAS f32x4*)(pcs + 384 + oa), cgb = *(LAS f32x4*)(pcs + 64), cub = *(LAS f32x4*)(pcs + 384 + 64);
#pragma unroll
          for (int tb = 0; tb < 4; ++tb) {
            const u32x2 ha = hcalc(acc[0][0][tb], acc[1][0][tb], cga, cua, tb);
            const u32x2 hb = hcalc(acc[0][1][tb], acc[1][1][tb], cgb, cub, tb);
            ST_SC1((u32x4{ha[0], ha[1], hb[0], hb[1]}), hoff + fw * 4096 + tb * 1024);
            SBAR();
          }
        }
        {
          const f32x4 cg = *(LAS f32x4*)(pcs + oc), cu = *(LAS f32x4*)(pcs + 384 + oc);
#pragma unroll
          for (int t2 = 0; t2 < 2; ++t2) {
            const u32x2 ha = hcalc(acc[0][2][2 * t2], acc[1][2][2 * t2], cg, cu, 2 * t2);
            SBAR();
            const u32x2 hb = hcalc(acc[0][2][2 * t2 + 1], acc[1][2][2 * t2 + 1], cg, cu, 2 * t2 + 1);
            SBAR();
            ST_SC1((u32x4{ha[0], ha[1], hb[0], hb[1]}), hoff + 8192 + fw * 2048 + t2 * 1024);
            SBAR();
          }
        }
      }
      if (EXP & 128) __builtin_amdgcn_s_setprio(0);
      STAMP(5);
      waitv<2>();
      if (lane < 8) __hip_atomic_store((GAS u64*)(ws + WS_MB + (rb * 16 + tw * 8 + lane) * MBS + cb * 16 + fw * 8), tag, RLX_AGENT);
    {
    const int sw = wid ^ 2, hm = sw & 1;
    lptr am = lds + SA + hm * PCH;
    f32x4 ca[8];
#pragma unroll
    for (int t = 0; t < 8; ++t) ca[t] = f32x4{0, 0, 0, 0};
    lptr pmain = am + O_KL + 1312 + lane * 16;

    flag_wait(fb, FL_KA + hm, 1);
    flag_wait(fb, FL_U, 2);
    STAMP(6);
    conv_units<0xff>(am + O_KREV, lds + L_U + (hm + odd) * UCH, lane, sw < 2 ? 5 : 0, sw < 2 ? 3 : 5, ca, [&](int it) {
      if (it == 0) { waitv<1>(); if (lane < 8) __hip_atomic_store((GAS u64*)(ws + WS_MB + (rb * 16 + tw * 8 + lane) * MBS + 256 + cb * 16 + fw * 8), tag, RLX_AGENT); } });
    if (sw < 2) {
      lptr pc = lds + L_PC;
#pragma unroll
      for (int t = 0; t < 8; ++t) { *(LAS f32x4*)(pmain + t * 1024) = ca[t]; ca[t] = f32x4{0, 0, 0, 0}; }
      flag_set(fb, FL_PA + hm, 1);
      flag_wait(fb, FL_KC, 1);
      lptr uc_ = lds + L_U + (odd ? 0 : 2) * UCH;
      if (odd) conv_units<0x3a>(lds + L_KC, uc_, lane, hm * 4, 4, ca, [](int) {});
      else conv_units<0xc5>(lds + L_KC, uc_, lane, hm * 4, 4, ca, [](int) {});
      STAMP(7);
      f32x4 c4[4];
      c4[0] = odd ? ca[1] : ca[0]; c4[1] = odd ? ca[3] : ca[2]; c4[2] = odd ? ca[4] : ca[6]; c4[3] = odd ? ca[5] : ca[7];
      if (!hm) {
#pragma unroll
        for (int t = 0; t < 4; ++t) *(LAS f32x4*)(pc + t * 1024 + lane * 16) = c4[t];
        flag_set(fb, FL_PC, 1);
      } else {
        flag_wait(fb, FL_PC, 1);
#pragma unroll
        for (int t = 0; t < 4; ++t) c4[t] += *(LAS f32x4*)(pc + t * 1024 + lane * 16);
        waitl();
#pragma unroll
        for (int t = 0; t < 4; ++t) { *(LAS unsigned*)(pc + t * 512 + q * 128 + r * 4) = pkbf(c4[t][0], c4[t][1]); *(LAS unsigned*)(pc + t * 512 + q * 128 + r * 4 + 64) = pkbf(c4[t][2], c4[t][3]); }
#pragma unroll
        for (int i = 0; i < 2; ++i) {
          const int s4 = 2 * i + (lane >> 5);
          const int T = odd ? (s4 == 0 ? 1 : s4 + 2) : (s4 < 2 ? 2 * s4 : s4 + 4);
          ST_SC1(ld16(pc + i * 1024 + lane * 16), WS_Y + ((T * 4 + ((lane >> 3) & 3)) * DM + chC) * 128 + (lane & 7) * 16);
        }
      }
    } else {
      STAMP(7);
      flag_wait(fb, FL_PA + hm, 1);
#pragma unroll
      for (int t = 0; t < 8; ++t) ca[t] += *(LAS f32x4*)(pmain + t * 1024);
#pragma unroll
      for (int t = 0; t < 8; ++t) { *(LAS unsigned*)(am + t * 512 + q * 128 + r * 4) = pkbf(ca[t][0], ca[t][1]); *(LAS unsigned*)(am + t * 512 + q * 128 + r * 4 + 64) = pkbf(ca[t][2], ca[t][3]); }
#pragma unroll
      for (int i = 0; i < 4; ++i) ST_SC1(ld16(am + i * 1024 + lane * 16), WS_Y + ((i * 8 + (lane >> 3)) * DM + chA + hm) * 128 + (lane & 7) * 16);
    }
    waitv<0>();
    if (flag_add(fb, FL_YC, lane) == 3u) {
      if (lane < 32) __hip_atomic_store((GAS u64*)(ws + WS_MB + (lane * 8 + xcd) * MBS + 512 + bi * 8), tag, RLX_AGENT);
      if (YS0) __builtin_amdgcn_s_sleep(YS0);
      _Pragma("nounroll") for (int sp = 0; sp < SPIN_GLB; ++sp) { if (wall(gflag_ok(ws, WS_MB + (tg * 8 + nb) * MBS + 512 + (lane & 31) * 8, tag))) break; __builtin_amdgcn_s_sleep(YS); }
      flag_set(fb, FL_YOK, 1);
    }

    flag_wait(fb, FL_YOK, 1);
    STAMP(8);
    u32x4 yv[3];
#pragma unroll
    for (int j = 0; j < 3; ++j) {
      const int p = umin(sw + 4 * j, 9), c = p * 64 + lane, row = (c * 6554) >> 16;
      glds1((const char*)x + (size_t)tg * 64 * 1280 + nb * 160, row * 1280 + (c - row * 10) * 16, lds0 + L_XT + p * 1024);
    }
#pragma unroll
    for (int j = 0; j < 3; ++j) { const int p = umin(sw + 4 * j, 9); yv[j] = LD_SC1(WS_Y + (tg * DM + nb * 80 + p * 8) * 128 + lane * 16); }
    lptr yb = lds + L_YB + sw * 3072;
#pragma unroll
    for (int j = 0; j < 3; ++j) st16(yb + j * 1024 + lane * 16, yv[j]);
#pragma unroll
    for (int j = 0; j < 3; ++j) {
      const u32x4 a = tr2(yb + j * 1024 + (r >> 2) * 128 + q * 32 + (lane & 3) * 8, 512);
      if (sw + 4 * j < 10) st16(lds + L_BASE + lane * 160 + (sw + 4 * j) * 16, a);
    }
    }
      __builtin_amdgcn_s_setprio(0);
    }
  }

  if (wid >= 4) {
    const int sw = wid - 4;
    const int lane = fresh_lane();
    {
      unsigned bo[4], ao[4];
#pragma unroll
      for (int c = 0; c < 4; ++c) { const int cc = sw * 4 + ((c + nb) & 3); bo[c] = WS_WD + (nb * 48 + cc * 3) * 5120; ao[c] = WS_H + (tg * 16 + cc) * 12288; }
      const unsigned l16 = lane * 16;
#define BFRAG(k, n) __builtin_amdgcn_raw_buffer_load_b128(rs, l16, bo[(k) < 8 ? (k) >> 1 : (k) - 8] + ((k) < 8 ? ((k) & 1) * 5120 : 10240) + (n) * 1024, SC1)
#define AFRAG(k, t) __builtin_amdgcn_raw_buffer_load_b128(rs, l16, ao[(k) < 8 ? (k) >> 1 : (k) - 8] + ((k) < 8 ? ((k) & 1) * 4096 : 8192) + (t) * 1024, (EXP & 8) ? 0 : SC1)
      u32x4 Bw[4][5];
#pragma unroll
      for (int k = 0; k < 4; ++k)
#pragma unroll
        for (int n = 0; n < 5; ++n) Bw[k][n] = BFRAG(k, n);
      STAMP(4);
      wd_dma(lane, sw, 0, 2);
      flag_wait(fb, FL_G1, 4);
      wd_dma(lane, sw, 2, 4);
      lptr park = lds + SL2 + sw * 10240 + (lane >> 2) * 160 + (lane & 3) * 8;
      asm volatile("" : "+v"(park));
      STAMP(7);
      const unsigned fo = WS_MB + (tg * 8 + nb) * MBS + (sw * 8 + (lane & 7)) * 8;
      _Pragma("nounroll") for (int sp = 0; sp < SPIN_GLB; ++sp) { if (wall(gflag_ok(ws, fo, tag))) break; __builtin_amdgcn_s_sleep(HS); }
      asm volatile("" ::: "memory");
      if (EXP & 1024) __builtin_amdgcn_s_sleep(127);
      STAMP(8);
      f32x4 a2[5][4];
#pragma unroll
      for (int n = 0; n < 5; ++n)
#pragma unroll
        for (int tb = 0; tb < 4; ++tb) a2[n][tb] = f32x4{0, 0, 0, 0};
      u32x4 Ah[4][4];
#pragma unroll
      for (int k = 0; k < 3; ++k)
#pragma unroll
        for (int tb = 0; tb < 4; ++tb) Ah[k][tb] = AFRAG(k, tb);
      u64 f2 = 0;
      SBAR();
#pragma unroll
      for (int k = 0; k < 12; ++k) {
        if (k == 3) { f2 = __hip_atomic_load((GAS u64*)(ws + fo + 256), RLX_AGENT); SBAR(); }
        if (k == 5) {
          unsigned tc;
          asm volatile("s_mov_b32 %1, %5\n\t1:\n\tv_cmp_ne_u64 vcc, %2, %0\n\ts_cbranch_vccz 2f\n\ts_sleep 2\n\t"
                       "global_load_dwordx2 %0, %3, %4 offset:256 sc1\n\ts_sub_u32 %1, %1, 1\n\ts_waitcnt vmcnt(0)\n\ts_cbranch_scc0 1b\n\t2:"
                       : "+v"(f2), "=&s"(tc) : "s"(tag), "v"(fo), "s"(ws), "i"(SPIN_GLB) : "memory", "vcc", "scc");
          SBAR();
        }
        u32x4 hf[4];
        if (k >= 8) {
          const u32x4 X0 = Ah[k & 3][0], X1 = Ah[k & 3][1], Y0 = Ah[k & 3][2], Y1 = Ah[k & 3][3];
          hf[0] = u32x4{X0[0], X0[1], Y0[0], Y0[1]}; hf[1] = u32x4{X0[2], X0[3], Y0[2], Y0[3]};
          hf[2] = u32x4{X1[0], X1[1], Y1[0], Y1[1]}; hf[3] = u32x4{X1[2], X1[3], Y1[2], Y1[3]};
        } else {
#pragma unroll
          for (int tb = 0; tb < 4; ++tb) hf[tb] = Ah[k & 3][tb];
        }
#pragma unroll
        for (int e = 0; e < 20; ++e) {
          const int n = e >> 2, tb = e & 3;
          a2[n][tb] = MFMA_BF(Bw[k & 3][n], hf[tb], a2[n][tb]);
          if (e & 1) {
            const int j = e >> 1;
            if (j < 4) { if (k + 3 < 12) Ah[(k + 3) & 3][j] = AFRAG(k + 3, j); }
            else if (j < 9 && k >= 1 && k + 3 < 12) {
              if (k + 3 < 8) Bw[(k + 3) & 3][j - 4] = BFRAG(k + 3, j - 4);
              else Bw[(k + 3) & 3][j - 4] = tr2(park + ((k - 5) >> 1) * (SL0 - SL2) + ((k - 5) & 1) * 5120 + (j - 4) * 32, 2560);
            }
            SBAR();
          }
        }
#pragma unroll
        for (int n = 0; n < 5; ++n) asm volatile("" : "+v"(a2[n][0]), "+v"(a2[n][1]), "+v"(a2[n][2]), "+v"(a2[n][3]));
        if ((EXP & 512) && k < 11) ARR();
      }
      STAMP(9);
      {
        lptr red = lds + SL2 + sw * 10240;
        const int l2 = fresh_lane(), r = l2 & 15, q = l2 >> 4;
#pragma unroll
        for (int n = 0; n < 5; ++n)
#pragma unroll
          for (int tb = 0; tb < 4; ++tb) {
            *(LAS f32x4*)(red + (q >> 1) * (SL0 - SL2) + (2 * n + (q & 1)) * 1024 + ((tb * 16 + r + 3 * n) & 63) * 16) = a2[n][tb];
          }
      }
    }
  }
  STAMP(10);
  BAR_L();
  STAMP(11);
  if (wid < 5) {
    const int tid = wid * 64 + fresh_lane();
    const int tok = (tid * 13108) >> 16, g = tid - tok * 5;
    lptr rp = lds + SL2 + g * 2048 + ((tok + 3 * g) & 63) * 16;
    f32x4 s[4];
#pragma unroll
    for (int m = 0; m < 4; ++m) {
      lptr p = rp + (m >> 1) * (SL0 - SL2) + (m & 1) * 1024;
      s[m] = (*(LAS f32x4*)(p) + *(LAS f32x4*)(p + 10240)) + (*(LAS f32x4*)(p + 20480) + *(LAS f32x4*)(p + 30720));
    }
    u16* op = out + (size_t)(tg * 64 + tok) * DM + nb * 80 + g * 16;
#pragma unroll
    for (int hh = 0; hh < 2; ++hh) {
      const u32x4 bs = ld16(lds + L_BASE + tid * 32 + hh * 16), xs = ld16(lds + L_XT + tid * 32 + hh * 16);
      const f32x4 sa = s[2 * hh], sb = s[2 * hh + 1];
      *(u32x4*)(op + hh * 8) = u32x4{pkbf((bflo(xs[0]) + bflo(bs[0])) + sa[0], (bfhi(xs[0]) + bfhi(bs[0])) + sa[1]), pkbf((bflo(xs[1]) + bflo(bs[1])) + sa[2], (bfhi(xs[1]) + bfhi(bs[1])) + sa[3]),
                                     pkbf((bflo(xs[2]) + bflo(bs[2])) + sb[0], (bfhi(xs[2]) + bfhi(bs[2])) + sb[1]), pkbf((bflo(xs[3]) + bflo(bs[3])) + sb[2], (bfhi(xs[3]) + bfhi(bs[3])) + sb[3])};
    }
  }
}

#if defined(__HIP_DEVICE_COMPILE__)
#pragma clang attribute pop
#endif

extern "C" void kernel_launch(void* const* d_in, const int* in_sizes, int n_in, void* d_out, int out_size, void* d_ws, size_t ws_size, hipStream_t stream) {
  (void)in_sizes; (void)n_in; (void)out_size; (void)ws_size;
  (void)hipFuncSetAttribute((const void*)fused_kernel, hipFuncAttributeMaxDynamicSharedMemorySize, LDS_BYTES);
  fused_kernel<<<256, 512, LDS_BYTES, stream>>>((const u16*)d_in[0], (const u16*)d_in[1], (const u16*)d_in[2], (const u16*)d_in[3], (const u16*)d_in[4],
                                                (const u16*)d_in[5], (const u16*)d_in[6], (const u16*)d_in[8], (const u16*)d_in[9], (const u16*)d_in[10],
                                                (u16*)d_out, (char*)d_ws);
}
```

```cpp
#define CPW 0
#include <hip/hip_runtime.h>
#include <stdint.h>

#if defined(__HIP_DEVICE_COMPILE__)
#pragma clang attribute push(__attribute__((target("no-packed-fp32-ops"))), apply_to = function)
#endif
typedef unsigned short u16;
typedef unsigned long long u64;
typedef short bf16x8 __attribute__((ext_vector_type(8)));
typedef _Float16 f16x8 __attribute__((ext_vector_type(8)));
typedef short s16x4 __attribute__((ext_vector_type(4)));
typedef float f32x2 __attribute__((ext_vector_type(2)));
typedef float f32x4 __attribute__((ext_vector_type(4)));
typedef unsigned u32x2 __attribute__((ext_vector_type(2)));
typedef unsigned u32x4 __attribute__((ext_vector_type(4)));
typedef _Float16 h16x2 __attribute__((ext_vector_type(2)));
typedef __bf16 b16x2 __attribute__((ext_vector_type(2)));
#define LAS __attribute__((address_space(3)))
#define GAS __attribute__((address_space(1)))
typedef LAS char* lptr;
#define DI __device__ __forceinline__
#define SBAR() __builtin_amdgcn_sched_barrier(0)
#define SGB(m, n) __builtin_amdgcn_sched_group_barrier(m, n, 0)
#define MFMA_BF(a, b, c) __builtin_amdgcn_mfma_f32_16x16x32_bf16(__builtin_bit_cast(bf16x8, a), __builtin_bit_cast(bf16x8, b), c, 0, 0, 0)
#define MFMA_H(a, b, c) __builtin_amdgcn_mfma_f32_16x16x32_f16(__builtin_bit_cast(f16x8, a), __builtin_bit_cast(f16x8, b), c, 0, 0, 0)
#define SC1 16
#define CPS 16
constexpr unsigned CODE_LINES = 306;
#define YS 8
#define YS0 16
#define HS 2
#define HSPLAN 0, 3, 6, 9, 12, 15, 18, 21, 24
#define PACE 2
#define EXP 0

constexpr int DM = 640, NM = 192;
constexpr unsigned WS_EPOCH = 0, WS_F0 = 4096, F0REP = 4352;
constexpr unsigned WS_MB = 262144, MBS = 1280;
constexpr unsigned WS_ST = 65536, WS_CS = 98304, WS_XB = 655360, WS_WD = WS_XB + 2621440 + 4096;
constexpr unsigned WS_H = WS_WD + 1966080, WS_Y = WS_H + 6291456, WS_END = WS_Y + 2621440;
constexpr unsigned WS_STAMP = 16u << 20;
constexpr int STAGE = 40960, SL1 = 0, SA = 40960, SL2 = 81920, SL0 = 122880, POOL_BIAS = SA;
constexpr int PROW = 784, PCH = 23 * PROW;
constexpr int KC1 = 4228, O_KREV = 128, O_KL = 8528;
constexpr int L_FLG = SA + 2 * PCH, L_SC = L_FLG + 128, L_STG = L_FLG + 192, STGW = 2304;
constexpr int L_STAT = L_STG, L_CS = L_STG + 1024;
constexpr int L_RTB = SL2 + 29696;
constexpr int L_LNT = L_STG;
constexpr int L_LNB = SL2 + 18432, LNR = 1344;
constexpr int UCH = 9232, UC1 = 4640;
constexpr int L_U = SL1 + 128, L_KC = L_U + 3 * UCH + 128, L_PC = L_KC + 8400;
constexpr int L_XT = SL1;
constexpr int L_BASE = SA, L_YB = SA + 10240;
constexpr int LDS_BYTES = 163840;
static_assert(O_KL + 1312 + 8192 <= PCH && L_LNB + 8 * LNR <= L_RTB && L_RTB + 10240 + 256 <= SL0 && L_PC + 4096 <= SA && L_STG + 2 * STGW <= SL0 && (L_KC & 15) == 0 && (L_PC & 15) == 0 && (L_STG & 15) == 0, "lds map");
enum { FL_F0 = 0, FL_EPI, FL_U, FL_KA, FL_KB, FL_KC, FL_KP, FL_PA, FL_PB, FL_PC, FL_YC, FL_YOK, FL_H1, FL_H2, FL_G1, FL_S2, FL_TAG = 16 };
constexpr int SPIN_LDS = 1 << 20, SPIN_GLB = 1 << 17;

DI bool wall(bool p) { return __builtin_amdgcn_ballot_w64(!p) == 0; }
DI unsigned umin(unsigned a, unsigned b) { return a < b ? a : b; }
DI unsigned umax(unsigned a, unsigned b) { return a > b ? a : b; }
DI float asf(unsigned v) { return __builtin_bit_cast(float, v); }
DI unsigned asu(float v) { return __builtin_bit_cast(unsigned, v); }
DI float bflo(unsigned v) { return asf(v << 16); }
DI float bfhi(unsigned v) { return asf(v & 0xffff0000u); }
DI float bf1(u16 v) { return asf((unsigned)v << 16); }
DI unsigned pkbf(float lo, float hi) { return __builtin_bit_cast(unsigned, __builtin_convertvector(f32x2{lo, hi}, b16x2)); }
DI unsigned pkh(f32x2 v) { return __builtin_bit_cast(unsigned, __builtin_convertvector(v, h16x2)); }
DI float rbf(float x) { return bflo(pkbf(x, x)); }
template <int CTRL> DI float dppf(float x) { return __builtin_bit_cast(float, __builtin_amdgcn_mov_dpp(__builtin_bit_cast(int, x), CTRL, 0xf, 0xf, true)); }
DI float row16_sum(float v) { v += dppf<0xB1>(v); v += dppf<0x4E>(v); v += dppf<0x124>(v); v += dppf<0x128>(v); return v; }

DI void glds4(const void* sb, unsigned v0, unsigned v1, unsigned v2, unsigned v3, unsigned lds) {
  asm volatile("s_mov_b32 m0, %5\n\ts_nop 0\n\tglobal_load_lds_dwordx4 %0, %4\n\tglobal_load_lds_dwordx4 %1, %4 offset:1024\n\t"
               "global_load_lds_dwordx4 %2, %4 offset:2048\n\tglobal_load_lds_dwordx4 %3, %4 offset:3072"
               :: "v"(v0), "v"(v1), "v"(v2), "v"(v3), "s"(sb), "s"(lds) : "memory");
}
DI void glds1(const void* sb, unsigned v0, unsigned lds) {
  asm volatile("s_mov_b32 m0, %2\n\ts_nop 0\n\tglobal_load_lds_dwordx4 %0, %1" :: "v"(v0), "s"(sb), "s"(lds) : "memory");
}
DI void glds2(const void* sb, unsigned v0, unsigned v1, unsigned lds) {
  asm volatile("s_mov_b32 m0, %3\n\ts_nop 0\n\tglobal_load_lds_dwordx4 %0, %2\n\tglobal_load_lds_dwordx4 %1, %2 offset:1024"
               :: "v"(v0), "v"(v1), "s"(sb), "s"(lds) : "memory");
}
template <int N> DI void waitv() { asm volatile("s_waitcnt vmcnt(%0)" :: "i"(N) : "memory"); }
DI void waitl() { asm volatile("s_waitcnt lgkmcnt(0)" ::: "memory"); }
DI u32x2 vtr(lptr p) { return __builtin_bit_cast(u32x2, __builtin_amdgcn_ds_read_tr16_b64_v4i16((LAS s16x4*)p)); }
DI u32x4 tr2(lptr p, int d) { u32x2 lo = vtr(p), hi = vtr(p + d); return u32x4{lo[0], lo[1], hi[0], hi[1]}; }
DI u32x4 ld16(lptr p) { return *(LAS u32x4*)p; }
DI void st16(lptr p, u32x4 v) { *(LAS u32x4*)p = v; }

DI unsigned flag_ld(unsigned addr) { unsigned r; asm volatile("ds_read_b32 %0, %1\n\ts_waitcnt lgkmcnt(0)" : "=v"(r) : "v"(addr) : "memory"); return __builtin_amdgcn_readfirstlane(r); }
DI void flag_wait(unsigned fb, int idx, unsigned need) { _Pragma("nounroll") for (int sp = 0; sp < SPIN_LDS && flag_ld(fb + idx * 4) < need; ++sp) __builtin_amdgcn_s_sleep(1); }
DI void flag_set(unsigned fb, int idx, unsigned v) {
  asm volatile("s_waitcnt lgkmcnt(0)\n\ts_mov_b64 exec, 1\n\tds_write_b32 %0, %1\n\ts_mov_b64 exec, -1" :: "v"(fb + idx * 4), "v"(v) : "memory"); }
DI unsigned flag_add(unsigned fb, int idx, int lane) {
  unsigned r; (void)lane;
  asm volatile("s_waitcnt lgkmcnt(0)\n\ts_mov_b64 exec, 1\n\tds_add_rtn_u32 %0, %1, %2\n\ts_mov_b64 exec, -1\n\ts_waitcnt lgkmcnt(0)" : "=&v"(r) : "v"(fb + idx * 4), "v"(1u) : "memory");
  return __builtin_amdgcn_readfirstlane(r);
}
#define RLX_AGENT __ATOMIC_RELAXED, __HIP_MEMORY_SCOPE_AGENT
DI bool gflag_ok(const char* ws, unsigned off, u64 tag) { return __hip_atomic_load((GAS u64*)(ws + off), RLX_AGENT) == tag; }

#define STAMP(i) do {} while (0)

DI f32x2 csq(f32x2 x) { const float t = x.x + x.x; return f32x2{__builtin_fmaf(x.x, x.x, -(x.y * x.y)), t * x.y}; }
DI h16x2 toh(f32x2 v) { return __builtin_convertvector(v, h16x2); }
DI h16x2 hmul(h16x2 x, h16x2 y) { return __builtin_elementwise_fma(h16x2{x.y, x.y}, h16x2{-y.y, y.x}, h16x2{x.x, x.x} * y); }
DI f32x2 cexp_p(float a2, float bh, float bl, float p) {
  const float e = __builtin_amdgcn_exp2f(a2 * p);
  const float ph = __builtin_fmaf(bl, p, __builtin_amdgcn_fractf(bh * p));
  return f32x2{e * __builtin_amdgcn_cosf(ph), e * __builtin_amdgcn_sinf(ph)};
}
template <int NS, int ROW0> DI void put4(const unsigned (&tb)[NS], const h16x2 (&a)[NS], const h16x2 (&b)[NS], const h16x2 (&c)[NS], const h16x2 (&d)[NS]) {
#pragma unroll
  for (int e = 0; e < NS; ++e)
    asm volatile("s_mov_b32 m0, %4\n\ts_nop 0\n\tds_write_addtid_b32 %0 offset:%5\n\tds_write_addtid_b32 %1 offset:%5+784\n\t"
                 "ds_write_addtid_b32 %2 offset:%5+1568\n\tds_write_addtid_b32 %3 offset:%5+2352"
                 :: "v"(a[e]), "v"(b[e]), "v"(c[e]), "v"(d[e]), "s"(tb[e]), "i"(ROW0 * PROW + POOL_BIAS) : "memory");
}
#define FORS _Pragma("unroll") for (int e = 0; e < NS; ++e)
template <int NS> DI void kpool(const unsigned (&tb)[NS], const float (&pv)[NS][4], const float (&ldt)[NS], bool join, float (&sc)[NS]) {
  float dt[NS];
  FORS dt[e] = rbf(__builtin_amdgcn_exp2f(ldt[e] * 1.44269504f));
  unsigned eb[NS]; float cs[NS];
  FORS {
    unsigned mi = umax(asu(pv[e][2]) & 0x7fffffffu, asu(pv[e][3]) & 0x7fffffffu);
    mi = umax(mi, asu(dppf<0xB1>(asf(mi)))); mi = umax(mi, asu(dppf<0x4E>(asf(mi)))); mi = umax(mi, asu(dppf<0x124>(asf(mi)))); mi = umax(mi, asu(dppf<0x128>(asf(mi))));
    eb[e] = umax(umax((unsigned)__builtin_amdgcn_readlane(mi, 0), (unsigned)__builtin_amdgcn_readlane(mi, 16)),
                 umax((unsigned)__builtin_amdgcn_readlane(mi, 32), (unsigned)__builtin_amdgcn_readlane(mi, 48))) >> 23;
  }
  if (join) eb[0] = eb[NS - 1] = umax(eb[0], eb[NS - 1]);
  FORS { eb[e] = umin(umax(eb[e], 2u), 252u); cs[e] = asf((254u - eb[e]) << 23); }
  float a2[NS], bh[NS], bl[NS];
  FORS {
    const float ea = rbf(__builtin_amdgcn_exp2f(pv[e][0] * 1.44269504f));
    a2[e] = -(ea * dt[e]) * 1.44269504f;
    const float b = pv[e][1] * dt[e];
    bh[e] = b * 0.15915494f;
    bl[e] = __builtin_fmaf(b, 6.4206382e-9f, __builtin_fmaf(b, 0.15915494f, -bh[e]));
  }
  h16x2 one[NS], w8[NS], w16[NS], w24[NS], w32[NS], w64[NS], w96[NS], w1[NS], w2[NS], w3[NS], w4[NS], C[NS], c4[NS];
  FORS {
    const f32x2 f1 = cexp_p(a2[e], bh[e], bl[e], 1.f), f2 = csq(f1), f4 = csq(f2);
    const f32x2 f8 = csq(f32x2{f4.x, -f4.y}), f16 = csq(f8), f32 = csq(f16), f64 = csq(f32);
    one[e] = h16x2{(_Float16)1.f, (_Float16)0.f};
    w8[e] = toh(f8); w16[e] = toh(f16); w32[e] = toh(f32); w64[e] = toh(f64); w24[e] = hmul(w8[e], w16[e]); w96[e] = hmul(w32[e], w64[e]);
    w1[e] = toh(f1); w2[e] = toh(f2); w4[e] = toh(f4); w3[e] = hmul(w1[e], w2[e]);
    C[e] = toh(f32x2{pv[e][2] * cs[e], pv[e][3] * cs[e]}); c4[e] = hmul(C[e], w4[e]);
  }
  put4<NS, 0>(tb, one, w8, w16, w24);
  put4<NS, 4>(tb, w32, w64, w96, w1);
  put4<NS, 8>(tb, w2, w3, C, c4);
  h16x2 c128[NS], c132[NS], w256[NS], w258[NS], w512[NS], w768[NS], w1024[NS], w1280[NS], w1536[NS], w1792[NS], w1796[NS];
  FORS {
    const f32x2 f128 = cexp_p(a2[e], bh[e], bl[e], 128.f), f256 = csq(f128), f512 = csq(f256);
    const h16x2 w128 = toh(f128);
    w256[e] = toh(f256); w512[e] = toh(f512); c128[e] = hmul(C[e], w128); c132[e] = hmul(c4[e], w128);
    w258[e] = hmul(w256[e], w2[e]); w768[e] = hmul(w512[e], w256[e]);
    w1024[e] = toh(csq(f512)); w1280[e] = hmul(w1024[e], w256[e]); w1536[e] = hmul(w1024[e], w512[e]);
    w1792[e] = hmul(w1024[e], w768[e]); w1796[e] = hmul(w1792[e], w4[e]);
  }
  put4<NS, 12>(tb, c128, c132, w256, w258);
  put4<NS, 16>(tb, w512, w768, w1024, w1280);
  put4<NS, 19>(tb, w1280, w1536, w1792, w1796);
  FORS sc[e] = 2.f * dt[e] * asf(eb[e] << 23);
}
struct KPtr {
  lptr pbx, pby, py, px0, px2;
  DI void init(lptr pool, int lane) {
    const int r = lane & 15, x = r & 3, y = r >> 2;
    lptr p = pool + (lane >> 4) * 16;
    pbx = p + x * PROW; pby = p + (y ? 3 + y : 0) * PROW; py = p + (10 + y) * PROW; px0 = p + (x ? 6 + x : 0) * PROW; px2 = p + (y == 3 ? 22 : 18 + x) * PROW;
  }
  DI void fetch(u32x4 (&v)[6], int o) const {
    v[0] = ld16(pbx + o); v[1] = ld16(pby + o); v[2] = ld16(py + o); v[3] = ld16(px0 + o); v[4] = ld16(pbx + 14 * PROW + o); v[5] = ld16(px2 + o);
  }
  DI void advance() { pbx += 128; pby += 128; py += 128; px0 += 128; px2 += 128; }
};
DI void kprod(const u32x4 (&v)[6], u32x4 (&o)[4]) {
  unsigned t[16];
  if (EXP & 2048) { SBAR(); __builtin_amdgcn_s_setprio(3); SBAR(); }
#pragma unroll
  for (int i = 0; i < 16; ++i) { const unsigned xe = v[(i >> 2) ? 2 + (i >> 2) : 0][i & 3], ye = v[(i >> 2) ? 2 : 1][i & 3];
    asm("v_pk_mul_f16 %0, %1, %2 op_sel_hi:[0,1]" : "=v"(t[i]) : "v"(xe), "v"(ye)); }
  SBAR();
#pragma unroll
  for (int i = 0; i < 16; ++i) { const unsigned xe = v[(i >> 2) ? 2 + (i >> 2) : 0][i & 3], ye = v[(i >> 2) ? 2 : 1][i & 3]; unsigned z;
    asm("v_pk_fma_f16 %0, %1, %2, %3 op_sel:[1,1,0] op_sel_hi:[1,0,1] neg_lo:[0,1,0]" : "=v"(z) : "v"(xe), "v"(ye), "v"(t[i])); o[i >> 2][i & 3] = z; }
  SBAR();
}
template <int H> DI void kprodh(const u32x4 (&v)[6], u32x4 (&o)[4]) {
  unsigned t[8];
#pragma unroll
  for (int i = 8 * H; i < 8 * H + 8; ++i) { const unsigned xe = v[(i >> 2) ? 2 + (i >> 2) : 0][i & 3], ye = v[(i >> 2) ? 2 : 1][i & 3];
    asm("v_pk_mul_f16 %0, %1, %2 op_sel_hi:[0,1]" : "=v"(t[i & 7]) : "v"(xe), "v"(ye)); }
  SBAR();
#pragma unroll
  for (int i = 8 * H; i < 8 * H + 8; ++i) { const unsigned xe = v[(i >> 2) ? 2 + (i >> 2) : 0][i & 3], ye = v[(i >> 2) ? 2 : 1][i & 3]; unsigned z;
    asm("v_pk_fma_f16 %0, %1, %2, %3 op_sel:[1,1,0] op_sel_hi:[1,0,1] neg_lo:[0,1,0]" : "=v"(z) : "v"(xe), "v"(ye), "v"(t[i & 7])); o[i >> 2][i & 3] = z; }
  asm volatile("" : "+v"(o[2 * H]), "+v"(o[2 * H + 1]));
  SBAR();
}
DI void kmma1(const u32x4 (&o0)[4], f32x4 (&acc)[3], bool first) {
  SBAR(); asm volatile("s_setprio 3\n\ts_nop 1"); SBAR();
#pragma unroll
  for (int i = 0; i < 3; ++i) acc[i] = MFMA_H(o0[1 + i], o0[0], (first ? f32x4{0, 0, 0, 0} : acc[i]));
  SBAR(); __builtin_amdgcn_s_setprio(0); SBAR();
}
DI void kcombine(f32x4 (&ko)[3], const f32x4 (&ka)[3][3], float s0, float s1, float s2) {
#pragma unroll
  for (int i = 0; i < 3; ++i)
#pragma unroll
    for (int j = 0; j < 4; ++j) ko[i][j] = __builtin_fmaf(ka[2][i][j], s2, __builtin_fmaf(ka[1][i][j], s1, ka[0][i][j] * s0));
}
DI float ubf(const u16* p, int i) { const unsigned w = ((const __attribute__((address_space(4))) unsigned*)(uintptr_t)p)[i >> 1]; return (i & 1) ? bfhi(w) : bflo(w); }
DI void kmma2(const u32x4 (&o0)[4], const u32x4 (&o1)[4], f32x4 (&acc)[3]) {
  SBAR(); asm volatile("s_setprio 3\n\ts_nop 1"); SBAR();
#pragma unroll
  for (int i = 0; i < 3; ++i) acc[i] = MFMA_H(o0[1 + i], o0[0], acc[i]);
#pragma unroll
  for (int i = 0; i < 3; ++i) acc[i] = MFMA_H(o1[1 + i], o1[0], acc[i]);
  SBAR(); __builtin_amdgcn_s_setprio(0); SBAR();
}
DI void kstore(lptr kl, int lane, const f32x4 (&acc)[3], float dsk) {
  const int c = lane & 15, q = lane >> 4;
  LAS u16* t = (LAS u16*)kl;
  const float k0 = acc[0][0] + (lane == 0 ? dsk : 0.f);
  *(LAS u32x2*)(t + 128 * (q >> 1) + 8 * c + 4 * (q & 1)) = u32x2{pkbf(k0, acc[0][1]), pkbf(acc[0][2], acc[0][3])};
  *(LAS unsigned*)(t + 256 + 64 * (q >> 1) + 2 * (q & 1) + 4 * c) = pkbf(acc[1][0], acc[1][1]);
  const unsigned p1 = pkbf(acc[1][2], acc[1][3]);
  const int i1 = 384 + 32 * (q >> 1) + (q & 1) + 2 * c;
  t[i1] = (u16)p1; t[i1 + 64] = (u16)(p1 >> 16);
  const unsigned r0 = pkbf(acc[2][0], acc[2][1]), r1 = pkbf(acc[2][2], acc[2][3]);
  if (!(q & 1)) { const int i2 = 512 + 8 * q + c; t[i2] = (u16)r0; t[i2 + 32] = (u16)(r0 >> 16); t[i2 + 64] = (u16)r1; t[i2 + 96] = (u16)(r1 >> 16); }
  else if (lane == 63) t[640] = (u16)r0;
}
DI unsigned dup16(unsigned a) { return a | (a << 16); }
DI unsigned bsel(unsigned m, unsigned a, unsigned b) { asm("" : "+v"(m)); return (a & m) | (b & ~m); }
DI unsigned rot16(unsigned a) { return __builtin_amdgcn_alignbit(a, a, 16); }
DI void kexpand(lptr kr, lptr kl, int lane) {
  LAS u16* t = (LAS u16*)kl;
  auto put = [&](int ch, unsigned dp, unsigned d0, unsigned d1, unsigned d2, unsigned d3) {
    st16(kr + 16 * ch, u32x4{d0, d1, d2, d3}); st16(kr + KC1 - 4 + 16 * ch, u32x4{dp, d0, d1, d2}); };
  const int n = 63 - lane, n0 = n & 31;
  const unsigned a0 = t[576 + n], b0 = t[577 + n], a1 = t[512 + n], b1 = t[513 + n];
  const unsigned ab = *(LAS unsigned*)(t + 384 + 2 * n), c2 = t[386 + 2 * n];
  const u32x2 v1 = *(LAS u32x2*)(t + 256 + 4 * n0); const unsigned q4 = t[260 + 4 * n0];
  const u32x4 v0 = ld16((lptr)(t + 8 * n0)); const unsigned w = *(LAS unsigned*)(t + 8 * n0 + 8);
  const unsigned mo = (n & 1) ? ~0u : 0u, lo = lane >= 32 ? ~0u : 0u, m31 = n == 31 ? ~0u : 0u;
  put(lane, dup16(b0), dup16(b0), bsel(mo, dup16(b0), b0 | (a0 << 16)), dup16(a0), dup16(a0));
  put(64 + lane, dup16(b1), dup16(b1), bsel(mo, dup16(b1), b1 | (a1 << 16)), dup16(a1), dup16(a1));
  put(128 + lane, dup16(c2), dup16(c2), dup16(ab >> 16), rot16(ab), dup16(ab & 0xffffu));
  put(192 + lane, bsel(lo, bsel(m31, dup16(w & 0xffffu), rot16(w)), dup16(q4)), bsel(lo, rot16(v0[3]), q4 | (v1[1] & 0xffff0000u)),
      bsel(lo, rot16(v0[2]), dup16(v1[1] & 0xffffu)), bsel(lo, rot16(v0[1]), (v1[1] & 0xffffu) | (v1[0] & 0xffff0000u)), bsel(lo, rot16(v0[0]), dup16(v1[0] & 0xffffu)));
  if (lane >= 60) put(319 - lane, lane == 63 ? rot16(v0[0]) : 0u, 0u, 0u, 0u, 0u);
}

struct ConvPlan { int n; int item[36][3]; };
constexpr ConvPlan conv_plan(int tmask, int maxt) {
  ConvPlan p{};
  int fifo[64] = {}, head = 0, tail = 0, carry[16] = {}, nc = 0, per = (tmask == 0xff) ? 1 : 2;
  for (int pass = 0; pass < 2; ++pass) {
    head = tail = 0;
    for (int i = 0; i < nc; ++i) fifo[tail++] = carry[i] + 32;
    int e = 0;
    for (int m = maxt; m >= 0; --m) {
      for (int j = 0; j + m <= maxt; ++j) {
        const bool on = (tmask >> (m + j)) & 1;
        int k = 0;
        if (m == 0) { if (on && j < maxt - 1) p.item[e][k++] = j; else fifo[tail++] = j; }
        if (j + m == maxt) { fifo[tail++] = 8 + 2 * m; fifo[tail++] = 9 + 2 * m; }
        if (on) { while (k < per && head < tail) p.item[e][k++] = fifo[head++]; while (k < 3) p.item[e][k++] = -1; ++e; }
      }
    }
    p.n = e; nc = 0;
    while (head < tail) carry[nc++] = fifo[head++];
  }
  return p;
}
template <int TMASK, class F>
DI void conv_units(lptr kr, lptr ub, int lane, int rho0, int nrho, f32x4 (&acc)[8], F hook) {
  constexpr int MAXT = (TMASK & 0x80) ? 7 : 5;
  constexpr ConvPlan plan = conv_plan(TMASK, MAXT);
  const int r = lane & 15, q = lane >> 4;
  lptr pk = kr + ((r & 1) ? 0 : KC1) + 4092 - 4 * r + 16 * q - 3584 - 64 * rho0 - 64;
  lptr pu = ub + ((r & 1) ? 0 : UC1) + 64 * (r >> 1) + 16 * q + 64 * (7 - rho0) - 64;
  lptr pk2 = pk + 8;
  u32x2 Kl[8], Kh[8]; u32x4 Uf[8];
  asm volatile("" : "+v"(pk), "+v"(pk2), "+v"(pu));
#pragma unroll
  for (int j = 0; j <= MAXT; ++j) { const int m = MAXT - j; Uf[j] = ld16(pu + 64 + 512 * j); Kl[m] = *(LAS u32x2*)(pk + 64 + 512 * (7 - m)); Kh[m] = *(LAS u32x2*)(pk2 + 64 + 512 * (7 - m)); SBAR(); }
#pragma unroll 1
  for (int it = 0; it < nrho; ++it) {
    int e = 0;
#pragma unroll
    for (int m = MAXT; m >= 0; --m) {
#pragma unroll
      for (int j = 0; j + m <= MAXT; ++j) {
        if ((TMASK >> (m + j)) & 1) {
          acc[m + j] = MFMA_BF(Uf[j], (u32x4{Kl[m][0], Kl[m][1], Kh[m][0], Kh[m][1]}), acc[m + j]);
#pragma unroll
          for (int k = 0; k < 3; ++k) {
            const int c = plan.item[e][k];
            if (c >= 0) {
              const int o = (c & 32) ? 64 : 0, cc = c & 31, mm = (cc - 8) >> 1;
              if (cc < 8) Uf[cc] = ld16(pu + o + 512 * cc);
              else if (cc & 1) Kh[mm] = *(LAS u32x2*)(pk2 + o + 512 * (7 - mm));
              else Kl[mm] = *(LAS u32x2*)(pk + o + 512 * (7 - mm));
            }
          }
          SBAR();
          ++e;
        }
      }
    }
    pk -= 64; pk2 -= 64; pu -= 64;
    asm volatile("" : "+v"(pk), "+v"(pk2), "+v"(pu));
    hook(it);
  }
}

template <int N> struct IC { static constexpr int value = N; };
DI u64 uni64(u64 v) { return (u64)(unsigned)__builtin_amdgcn_readfirstlane((unsigned)v) | ((u64)(unsigned)__builtin_amdgcn_readfirstlane((unsigned)(v >> 32)) << 32); }
DI int fresh_lane() { int l; asm volatile("v_mbcnt_lo_u32_b32 %0, -1, 0\n\tv_mbcnt_hi_u32_b32 %0, -1, %0" : "=v"(l)); return l; }
#define ARR() do {} while (0)
#define BAR() do { SBAR(); ARR(); asm volatile("s_barrier" ::: "memory"); SBAR(); } while (0)
#define BAR_L() do { SBAR(); asm volatile("s_waitcnt lgkmcnt(0)" ::: "memory"); SBAR(); ARR(); asm volatile("s_barrier" ::: "memory"); SBAR(); } while (0)
#define LD_SC1(off) __builtin_amdgcn_raw_buffer_load_b128(rs, (off), 0, SC1)
#define ST_SC1(v, off) __builtin_amdgcn_raw_buffer_store_b128((v), rs, (off), 0, SC1)

__global__ __launch_bounds__(512, 2) void fused_kernel(const u16* __restrict__ x, const u16* __restrict__ p_lar, const u16* __restrict__ p_aim,
                                                       const u16* __restrict__ p_cr, const u16* __restrict__ p_ci, const u16* __restrict__ p_ldt,
                                                       const u16* __restrict__ p_dsk, const u16* __restrict__ wg, const u16* __restrict__ wu,
                                                       const u16* __restrict__ wd, u16* __restrict__ out, char* ws) {
  extern __shared__ __attribute__((aligned(16))) char smem[];
  const int tid = __builtin_amdgcn_workitem_id_x(), bid = __builtin_amdgcn_workgroup_id_x();
  if (__builtin_amdgcn_readfirstlane(tid >> 6) == CPW) {
    u64 pc_; asm volatile("s_getpc_b64 %0" : "=s"(pc_));
    asm volatile("s_mov_b32 m0, %2\n\ts_mov_b32 exec_hi, %3\n\ts_mov_b32 exec_lo, %4\n\tglobal_load_lds_dword %0, %1\n\ts_mov_b64 exec, -1"
                 :: "v"(umin(((bid >> 3) & (CPS - 1)) * (320 / CPS) + (tid & 63), CODE_LINES - 1) * 128), "s"(pc_ & ~127ull), "s"((unsigned)(uintptr_t)smem + L_RTB + 10240),
                    "i"(320 / CPS > 32 ? (1u << ((320 / CPS - 32) & 31)) - 1 : 0u), "i"(320 / CPS >= 32 ? -1 : (int)((1u << ((320 / CPS) & 31)) - 1)) : "memory");
  }
  lptr lds = (lptr)smem;
  const unsigned lds0 = (unsigned)(uintptr_t)smem;
  const unsigned fb = lds0 + L_FLG;
  const int wraw = __builtin_amdgcn_readfirstlane(tid >> 6), lane = tid & 63;
  const int wid = wraw ^ ((wraw >> 2) << 1);
  const int xcd = bid & 7, bi = bid >> 3;
  const int rb = (xcd >> 2) * 8 + (bi >> 2), cb = (xcd & 3) * 4 + (bi & 3);
  const int tg = (xcd >> 1) * 8 + (bi >> 2), nb = (xcd & 1) * 4 + (bi & 3);
  const int cw = xcd * 32 + bi, odd = cw & 1, c0 = 5 * (cw >> 1) + 2 * odd;
  const int chA = c0 + odd, chC = odd ? c0 : c0 + 2;
  const int rm = rb & 7, rot = 2 * (rm >= 5 ? rm - 5 : rm) + (rb >> 3);
  const bool csum = rm < 5;
  const __amdgpu_buffer_rsrc_t rs = __builtin_amdgcn_make_buffer_rsrc(ws, 0, (int)WS_END, 0x00020000);
  const int r = lane & 15, q = lane >> 4;

  u64 tag = 0;
  const int lw = wid - 4, kw = wid - 6;
  unsigned vx[4], vw[12];
  auto issue = [&](int s, int sbase, int h, int P0 = 0, int P1 = 5) {
    int ks = s + rot; if (ks >= 10) ks -= 10;
    const unsigned base = lds0 + sbase;
    const char* sx = (const char*)x + (size_t)(rb * 128 + h * 64) * 1280 - 4096 + ks * 128;
    const char* sw = (const char*)(h ? wu : wg) + cb * 192 - 4096 + (size_t)ks * 64 * 3072;
    if (P0 <= 0 && 0 < P1) glds4(sx, vx[0], vx[1], vx[2], vx[3], base + h * 8192);
    if (P0 <= 1 && 1 < P1) glds4(sw, vw[0], vw[1], vw[2], vw[3], base + 16384 + h * 12288);
    if (P0 <= 2 && 2 < P1) glds4(sx + 32 * 1280, vx[0], vx[1], vx[2], vx[3], base + h * 8192 + 4096);
    if (P0 <= 3 && 3 < P1) glds4(sw, vw[4], vw[5], vw[6], vw[7], base + 16384 + h * 12288 + 4096);
    if (P0 <= 4 && 4 < P1) glds4(sw, vw[8], vw[9], vw[10], vw[11], base + 16384 + h * 12288 + 8192);
  };
  auto prep_dma = [&](int ln) {
#pragma unroll
    for (int j = 0; j < 4; ++j) { const int row = 8 * j + (ln >> 3); vx[j] = row * 1280 + (((ln & 7) ^ ((row >> 1) & 7)) << 4) + 4096 - 1024 * j; }
#pragma unroll
    for (int j = 0; j < 3; ++j) { const int c = j * 64 + ln, row = (c * 43691) >> 19, pos = c - row * 12;
      const unsigned v = row * 3072 + ((pos ^ (((row >> 3) & 1) << 1)) << 4) + 4096;
#pragma unroll
      for (int g = 0; g < 4; ++g) vw[3 * g + j] = v + g * 16 * 3072 - 1024 * ((3 * g + j) & 3); }
  };
  auto wd_dma = [&](int ln, int w, int j0, int j1) {
    unsigned v[5];
#pragma unroll
    for (int i = 0; i < 5; ++i) { const int c = i * 64 + ln, row = (c * 6554) >> 16; v[i] = row * 1280 + (c - row * 10) * 16 + 4096 - (i & 3) * 1024; }
#pragma unroll
    for (int j = j0; j < j1; ++j) {
      const char* sp = (const char*)wd + (size_t)((w * 4 + ((j + nb) & 3)) * 96 + 32) * 1280 + nb * 160 - 4096;
      const unsigned d = lds0 + ((j >> 1) ? SL0 : SL2) + w * 10240 + (j & 1) * 5120;
      glds4(sp, v[0], v[1], v[2], v[3], d); glds1(sp, v[4], d + 4096);
    }
  };
  const int lnb = cw;
  const int ro0 = (0x100222 >> (4 * wid)) & 3, th0 = (0x000210 >> (4 * wid)) & 3, ro1 = (wid >> 1) & 1, th1 = 1 + (wid & 1);
  const int ch0 = c0 + ((0x0210 >> (4 * (ro0 + odd))) & 3), ch1 = chA + ro1;
  const int pt0 = ro0 * PCH + (ro0 >> 1) * (SL2 - SA - 2 * PCH) + th0 * 256, pt1 = ro1 * PCH + th1 * 256;
  unsigned o = (lane < 32 ? ch0 * 384 + th0 * 128 : ch1 * 384 + th1 * 128) + (lane & 31) * 4;
  unsigned xo0 = lane * 16, xo1 = q * 1280 + r * 16 + 1024;
  asm volatile("" : "+v"(o), "+v"(xo0), "+v"(xo1));
  asm volatile("" :: "s"(x), "s"(p_lar), "s"(p_aim), "s"(p_cr), "s"(p_ci), "s"(p_ldt), "s"(p_dsk), "s"(wg), "s"(wu), "s"(wd), "s"(out), "s"(ws));
  const int ptp = wid == 5 ? pt0 : pt1;
  if ((0x2f >> wid) & 1) {
    asm volatile("s_mov_b32 m0, %5\n\ts_nop 0\n\tglobal_load_lds_dword %0, %1\n\tglobal_load_lds_dword %0, %2 offset:784\n\t"
                 "global_load_lds_dword %0, %3 offset:1568\n\tglobal_load_lds_dword %0, %4 offset:2352"
                 :: "v"(o), "s"(p_lar), "s"((const char*)p_aim - 784), "s"((const char*)p_cr - 1568), "s"((const char*)p_ci - 2352), "s"(lds0 + SA + ptp) : "memory");
  }
  if (wid >= 6) {
    const char* xr = (const char*)x + (size_t)(lnb * 8 + kw * 4) * 1280;
#pragma unroll
    for (int j = 0; j < 4; ++j) glds1(xr + j * 1280, xo0, lds0 + L_LNB + (kw * 4 + j) * LNR);
    glds1(xr, xo1, lds0 + L_LNT + kw * 1024);
  } else if (wid >= 4) { if (lane < 16) *(LAS unsigned*)(lds + L_FLG + lane * 4) = 0; prep_dma(lane); }
  SBAR(); asm volatile("s_waitcnt lgkmcnt(0)\n\ts_barrier" ::: "memory"); SBAR();
  const float ldt0 = ubf(p_ldt, ch0), ldt1 = ubf(p_ldt, ch1);
  if (wid == 4 || wid == 5) {
    issue(0, SL0, lw);
  }
  auto lds_params = [&](float (&pv)[4], int m) {
#pragma unroll
    for (int e = 0; e < 4; ++e) pv[e] = bf1(*(LAS u16*)(lds + SA + ptp + e * PROW + m * 2));
  };
  if (wid >= 6) {
    waitv<0>();
    STAMP(0);
    lptr xp = lds + L_LNB + (kw * 4 + q) * LNR + r * 16;
    u32x4 xv[5];
#pragma unroll
    for (int j = 0; j < 4; ++j) xv[j] = ld16(xp + 256 * j);
    xv[4] = ld16(lds + L_LNT + kw * 1024 + lane * 16);
    float v[40]; float s0 = 0.f, s1 = 0.f;
#pragma unroll
    for (int j = 0; j < 20; ++j) { const unsigned w = xv[j >> 2][j & 3]; v[2 * j] = bflo(w); v[2 * j + 1] = bfhi(w); s0 += v[2 * j]; s1 += v[2 * j + 1]; }
    const float mean = row16_sum(s0 + s1) * (1.f / DM);
    float q0 = 0.f, q1 = 0.f;
#pragma unroll
    for (int j = 0; j < 20; ++j) { v[2 * j] -= mean; v[2 * j + 1] -= mean; q0 = __builtin_fmaf(v[2 * j], v[2 * j], q0); q1 = __builtin_fmaf(v[2 * j + 1], v[2 * j + 1], q1); }
    const float var = rbf(row16_sum(q0 + q1) * (1.f / DM));
    const float mb = rbf(mean), sd = rbf(__builtin_amdgcn_sqrtf(rbf(var + 1e-5f)));
    const float rsd = __builtin_amdgcn_rcpf(sd), off = (mean - mb) * rsd;
#pragma unroll
    for (int j = 0; j < 20; ++j) xv[j >> 2][j & 3] = pkbf(__builtin_fmaf(v[2 * j], rsd, off), __builtin_fmaf(v[2 * j + 1], rsd, off));
#pragma unroll
    for (int j = 0; j < 5; ++j) st16(xp + 256 * j, xv[j]);
    if (r == 0) __builtin_amdgcn_raw_buffer_store_b64(u32x2{asu(mb), asu(sd)}, rs, WS_ST + (lnb * 8 + kw * 4 + q) * 8, 0, SC1);
  } else if (wid == 4) {
    const int un = umin(cw, 191) * 2, ukb = un >> 3, unb = un & 7;
    const int c3 = (ukb * 43) >> 7, kt = ukb - 3 * c3;
    const int t0 = kt == 0 ? 0 : kt == 1 ? 80 : 32, t1 = kt == 0 ? 0 : kt == 1 ? 48 : 32;
#pragma unroll
    for (int j = 0; j < 5; ++j) { const int c = j * 64 + lane, row = (c * 6554) >> 16, pos = c - row * 10;
      const unsigned v = (c3 * 96 + row + (row < 16 ? t0 : t1)) * 1280 + unb * 160 + pos * 16;
      glds1(wd, v, lds0 + L_RTB + j * 1024); glds1((const char*)wd + 160, v, lds0 + L_RTB + 5120 + j * 1024); }
  } else {
    if (wid == 5) waitv<20>(); else waitv<0>();
    STAMP(0);
    float pv[1][4], pn[4];
    lds_params(pv[0], lane); lds_params(pn, 64 + lane);
    unsigned tb[1] = {lds0 + pt0}, sa_ = lds0 + L_SC + (ro0 * 3 + th0) * 4, inc = wid < 3;
    float ldt[1] = {ldt0};
#pragma unroll 1
    for (int rnd = wid == 5; rnd < 2; ++rnd) {
      float sc[1];
      kpool<1>(tb, pv, ldt, false, sc);
      asm volatile("s_mov_b64 exec, 1\n\tds_write_b32 %0, %1\n\tds_add_u32 %2, %3\n\ts_mov_b64 exec, -1" :: "v"(sa_), "v"(sc[0]), "v"(fb + FL_KP * 4), "v"(inc) : "memory");
#pragma unroll
      for (int e = 0; e < 4; ++e) pv[0][e] = pn[e];
      tb[0] = lds0 + pt1; sa_ = lds0 + L_SC + (ro1 * 3 + th1) * 4; inc = 0; ldt[0] = ldt1;
    }
  }
  STAMP(1);

  if (wid >= 6) {
    const int lane = fresh_lane(), r = lane & 15, q = lane >> 4;
    const int un = umin(cw * 2 + kw, 383), ukb = un >> 3, unb = un & 7;
    const float dsk = ubf(p_dsk, chA + kw);
    u32x4 xv[5];
    u64 ep = __hip_atomic_load((GAS u64*)(ws + WS_EPOCH), RLX_AGENT);
    BAR_L();
    u32x4 wt[5], wf[12];
#pragma unroll
    for (int j = 0; j < 5; ++j) xv[j] = tr2(lds + L_LNB + (r >> 2) * LNR + (kw * 5 + j) * 128 + q * 32 + (lane & 3) * 8, 4 * LNR);
    {
      const int sg = (q & 1) * 32;
      lptr pa = lds + SL0 + 16384 + kw * 12288 + (8 * q + (r >> 2)) * 192 + 8 * (lane & 3);
#pragma unroll
      for (int t = 0; t < 12; ++t) wf[t] = tr2(pa + (t / 6) * 6144 + (t % 6) * 32 + ((t & 1) ? -sg : sg), 768);
    }
    lptr pool = lds + SA + kw * PCH;
    KPtr kp; kp.init(pool, lane);
    u32x4 kv[2][6], kq[4];
    kp.fetch(kv[0], 0); kp.fetch(kv[1], 64);
    SBAR();
#pragma unroll
    for (int j = 0; j < 5; ++j) wt[j] = tr2(lds + L_RTB + kw * 5120 + (4 * q + (r >> 2)) * 160 + j * 32 + (lane & 3) * 8, 2560);
    BAR_L();
    tag = uni64(ep) + 1;
    *(volatile LAS u64*)(lds + L_FLG + FL_TAG * 4) = tag;
    unsigned pf_sink = 0;
    f32x4 ko[3], ka[3][3];
    u32x4 f0 = {0, 0, 0, 0}, f1 = {0, 0, 0, 0};
    constexpr int HS0[9] = {HSPLAN};
#pragma unroll
    for (int sl = 0; sl < 8; ++sl) {
      if (sl == 6 && kw == 0) { f0 = LD_SC1(WS_F0 + xcd * F0REP + lane * 16); f1 = LD_SC1(WS_F0 + xcd * F0REP + 1024 + lane * 16); SBAR(); }
      if (sl == 3 && kw == 0 && lane < 8) __hip_atomic_store((GAS u64*)(ws + WS_F0 + lane * F0REP + cw * 8), tag, RLX_AGENT);
#pragma unroll
      for (int hs = HS0[sl]; hs < HS0[sl + 1]; ++hs) {
        const int k = hs >> 1;
        if (!(hs & 1)) kprodh<0>(kv[k & 1], kq);
        else { kprodh<1>(kv[k & 1], kq); if (k + 2 < 12) kp.fetch(kv[k & 1], 64 * (k + 2)); kmma1(kq, ka[k >> 2], (k & 3) == 0); }
      }
      if (sl == 0) {
#pragma unroll
        for (int j = 0; j < 5; ++j) ST_SC1(xv[j], WS_XB + lnb * 10240 + (kw * 5 + j) * 1024 + lane * 16);
      }
      if (sl == 1) {
        if (csum) {
          const u32x4 ones = {0x3f803f80u, 0x3f803f80u, 0x3f803f80u, 0x3f803f80u};
          f32x4 cs[6];
          SBAR(); __builtin_amdgcn_s_setprio(3); SBAR();
#pragma unroll
          for (int t = 0; t < 6; ++t) cs[t] = MFMA_BF(ones, wf[t], (f32x4{0, 0, 0, 0}));
#pragma unroll
          for (int t = 0; t < 6; ++t) cs[t] = MFMA_BF(ones, wf[6 + t], cs[t]);
          SBAR(); __builtin_amdgcn_s_setprio(0); SBAR();
          const float cs0 = q == 0 ? cs[0][0] : q == 1 ? cs[1][0] : q == 2 ? cs[2][0] : cs[3][0], cs1 = q == 0 ? cs[4][0] : cs[5][0];
          const unsigned co = WS_CS + (cb * 10 + rot) * 768 + kw * 384 + lane * 4;
          __builtin_amdgcn_raw_buffer_store_b32(asu(cs0), rs, co, 0, SC1);
          if (lane < 32) __builtin_amdgcn_raw_buffer_store_b32(asu(cs1), rs, co + 256, 0, SC1);
        }
        if (cw < 192) {
#pragma unroll
          for (int f = 0; f < 5; ++f) ST_SC1(wt[f], WS_WD + (unb * 48 + ukb) * 5120 + f * 1024 + lane * 16);
        }
      }
      if (sl == 2) waitv<0>();
      if (sl == 6 && kw == 0) {
        const unsigned tl = (unsigned)tag, th = (unsigned)(tag >> 32);
        _Pragma("nounroll") for (int sp = 0; sp < SPIN_GLB; ++sp) {
          if (wall(((f0[0] ^ tl) | (f0[1] ^ th) | (f0[2] ^ tl) | (f0[3] ^ th) | (f1[0] ^ tl) | (f1[1] ^ th) | (f1[2] ^ tl) | (f1[3] ^ th)) == 0u)) break;
          f0 = LD_SC1(WS_F0 + xcd * F0REP + lane * 16); f1 = LD_SC1(WS_F0 + xcd * F0REP + 1024 + lane * 16);
          __builtin_amdgcn_s_sleep(2);
        }
      }
      if (false) {
        const unsigned po = WS_WD + nb * 245760 + (bi >> 2) * 30720 + umin(((sl - 6) * 2 + kw) * 64 + lane, 239u) * 128;
        asm volatile("global_load_dword %0, %1, %2 sc1" : "+v"(pf_sink) : "v"(po), "s"(ws) : "memory");
      }
      BAR();
    }
    STAMP(2);
    kcombine(ko, ka, *(LAS float*)(lds + L_SC + kw * 12), *(LAS float*)(lds + L_SC + kw * 12 + 4), *(LAS float*)(lds + L_SC + kw * 12 + 8));
    kstore(pool + O_KL, lane, ko, dsk);
    kexpand(pool + O_KREV, pool + O_KL, lane);
    flag_set(fb, FL_KA + kw, 1);
    asm volatile("s_waitcnt vmcnt(0)" : "+v"(pf_sink) :: "memory");
    STAMP(3);
  } else {
    if (wid >= 4) {
      const int lane = fresh_lane();
      u32x4 cv[6][6];
      {
        KPtr kp; kp.init(lds + SL2 + lw * 384, lane);
        flag_wait(fb, FL_KP, 3);
#pragma unroll
        for (int i = 0; i < 6; ++i) kp.fetch(cv[i], 64 * i);
      }
      const float sca = *(LAS float*)(lds + L_SC + 24 + lw * 4), scb = *(LAS float*)(lds + L_SC + 24 + lw * 8), scc = *(LAS float*)(lds + L_SC + 28 + lw * 4);
      SBAR();
      prep_dma(fresh_lane());
      if (!lw) { issue(1, SL1, 0); issue(1, SL1, 1); waitv<40>(); } else waitv<0>();
      BAR_L();
      issue(2, SL2, lw, 0, 1); issue(2, SL2, lw, 2, 3);
      f32x4 ka[3][3], kc[3];
#pragma unroll
      for (int s = 0; s < 7; ++s) {
        if (s == 0) waitv<8>(); else waitv<20>();
        BAR();
        if (s == 0) { issue(2, SL2, lw, 1, 2); issue(2, SL2, lw, 3, 5); }
        issue(s + 3, s % 3 == 0 ? SL0 : s % 3 == 1 ? SL1 : SL2, lw);
        if (s >= 1 && !(EXP & 4096)) { u32x4 o[4]; kprod(cv[s - 1], o); kmma1(o, ka[(s - 1) >> 1], s & 1); }
      }
      kcombine(kc, ka, sca, scb, scc);
      if (lw == 0) {
#pragma unroll
        for (int i = 0; i < 3; ++i) *(LAS f32x4*)(lds + L_STG + i * 1024 + lane * 16) = kc[i];
      }
      waitv<20>();
      BAR_L();
      STAMP(2);
      tag = uni64(*(volatile LAS u64*)(lds + L_FLG + FL_TAG * 4));
      u32x4 uc[8]; unsigned uprev;
      const int n0 = lw * 128 + (lane >> 2) * 8;
      const unsigned uo = WS_XB + n0 * 10240 + (c0 + (lane & 3)) * 16;
      u32x4 sv, cp[10];
      if (lw == 0) {
        sv = LD_SC1(WS_ST + rb * 1024 + lane * 16);
#pragma unroll
        for (int j = 0; j < 10; ++j) cp[j] = LD_SC1(WS_CS + (cb * 10 + j) * 768 + (lane < 48 ? lane : 0) * 16);
      }
#pragma unroll
      for (int j = 0; j < 8; ++j) uc[j] = LD_SC1(uo + j * 10240);
      uprev = __builtin_amdgcn_raw_buffer_load_b32(rs, uo - 10240 + 12, 0, SC1);
      SBAR();
      if (lw) {
#pragma unroll
        for (int i = 0; i < 3; ++i) kc[i] += *(LAS f32x4*)(lds + L_STG + i * 1024 + lane * 16);
        kstore(lds + L_PC, lane, kc, ubf(p_dsk, chC));
        waitv<9>();
      } else waitv<20>();
      BAR();
      STAMP(3);
      if (lw == 0) {
        f32x4 cs = {0, 0, 0, 0};
#pragma unroll
        for (int j = 0; j < 10; ++j) cs += __builtin_bit_cast(f32x4, cp[j]);
        st16(lds + L_STAT + lane * 16, sv);
        if (lane < 48) *(LAS f32x4*)(lds + L_CS + lane * 16) = cs;
        flag_set(fb, FL_EPI, 1);
        if (bid == 0 && lane == 0) __hip_atomic_store((GAS u64*)(ws + WS_EPOCH), tag, RLX_AGENT);
      }
      if (n0 == 0) uprev = 0;
      if ((lane & 3) < 3) {
        lptr ud = lds + L_U + (lane & 3) * UCH + 448 + n0 * 16;
#pragma unroll
        for (int j = 0; j < 8; ++j) {
          st16(ud + j * 16, uc[j]);
          const unsigned pw_ = j ? uc[j - 1][3] : uprev;
          st16(ud + UC1 + j * 16, u32x4{__builtin_amdgcn_alignbit(uc[j][0], pw_, 16), __builtin_amdgcn_alignbit(uc[j][1], uc[j][0], 16),
                                        __builtin_amdgcn_alignbit(uc[j][2], uc[j][1], 16), __builtin_amdgcn_alignbit(uc[j][3], uc[j][2], 16)});
        }
      }
      if (lane < 28) {
#pragma unroll
        for (int c = 0; c < 3; ++c) st16(lds + L_U + c * UCH + lw * UC1 + lane * 16, u32x4{0, 0, 0, 0});
      }
      flag_add(fb, FL_U, lane);
      if (lw) { kexpand(lds + L_KC, lds + L_PC, lane); flag_set(fb, FL_KC, 1); }

    } else {
      f32x4 acc[2][3][4];
      if (!(EXP & 4)) __builtin_amdgcn_s_setprio(2);
      const int tw = wid >> 1, fw = wid & 1;
      lptr pB0 = lds + (tw * 64 + r) * 128 + ((q ^ (r >> 1)) << 4);
      lptr pB1 = lds + (tw * 64 + r) * 128 + (((q ^ (r >> 1)) ^ 4) << 4);
      const int sg = (q & 1) * 32;
      lptr pAb = lds + 16384 + (8 * q + (r >> 2)) * 192 + 8 * (lane & 3);
      lptr pA[3] = {pAb + (fw ? 160 - sg : sg), pAb + (fw ? 128 + sg : 32 - sg), pAb + (fw ? 96 - sg : 64 + sg)};
      u32x4 A0[2][3], B0[4], A1[2][3], B1[4];
      auto rd = [&](u32x4 (&A)[2][3], u32x4 (&B)[4], int off, int kk) {
#pragma unroll
        for (int tb = 0; tb < 4; ++tb) B[tb] = ld16((kk ? pB1 : pB0) + off + tb * 2048);
#pragma unroll
        for (int m = 0; m < 2; ++m)
#pragma unroll
          for (int ii = 0; ii < 3; ++ii) A[m][ii] = tr2(pA[ii] + off + m * 12288 + kk * 6144, 768);
      };
      auto mm = [&](u32x4 (&A)[2][3], u32x4 (&B)[4]) {
#pragma unroll
        for (int e = 0; e < 24; ++e) { const int m = e / 12, ii = (e / 4) % 3, tb = e % 4; acc[m][ii][tb] = MFMA_BF(A[m][ii], B[tb], acc[m][ii][tb]); }
      };
      auto sched = [&]() {
#pragma unroll
        for (int k = 0; k < 16; ++k) { SGB(0x8, 1); SGB(0x100, 1); }
        SGB(0x8, 8);
      };
      BAR_L();
      STAMP(2);
      rd(A0, B0, SL0, 0);
      SBAR();
      rd(A1, B1, SL0, 1);
#pragma unroll
      for (int e = 0; e < 24; ++e) { const int m = e / 12, ii = (e / 4) % 3, tb = e % 4; acc[m][ii][tb] = MFMA_BF(A0[m][ii], B0[tb], (f32x4{0, 0, 0, 0})); }
      sched();
      int s0 = SL1, s1 = SL2, s2 = SL0;
#pragma unroll 1
      for (int s = 0; s < 9; ++s) {
        BAR_L();
        rd(A0, B0, s0, 0); mm(A1, B1); sched();
        SBAR();
        rd(A1, B1, s0, 1); mm(A0, B0); sched();
        const int t = s0; s0 = s1; s1 = s2; s2 = t;
      }
      SBAR();
      flag_add(fb, FL_G1, lane);
      mm(A1, B1);
      SBAR();
      if (EXP & 64) __builtin_amdgcn_s_setprio(0);
      STAMP(3);
      flag_wait(fb, FL_EPI, 1);
      tag = uni64(*(volatile LAS u64*)(lds + L_FLG + FL_TAG * 4));
      STAMP(4);
      {
        float nmb[4], al[4], sd[4];
#pragma unroll
        for (int tb = 0; tb < 4; ++tb) { const f32x2 st = *(LAS f32x2*)(lds + L_STAT + (tw * 64 + tb * 16 + r) * 8); nmb[tb] = -st.x; sd[tb] = st.y; al[tb] = -1.44269504f * __builtin_amdgcn_rcpf(st.y); }
        const unsigned hoff = WS_H + ((rb * 2 + tw) * 16 + cb) * 12288 + lane * 16;
        auto hcalc = [&](const f32x4& g4, const f32x4& u4, const f32x4& cg, const f32x4& cu, int tb) {
          float hv[4];
#pragma unroll
          for (int j = 0; j < 4; ++j) {
            const float g = __builtin_fmaf(nmb[tb], cg[j], g4[j]) * al[tb];
            const float u = __builtin_fmaf(nmb[tb], cu[j], u4[j]);
            const float e = __builtin_amdgcn_exp2f(g);
            hv[j] = u * __builtin_amdgcn_rcpf(__builtin_fmaf(e, sd[tb], sd[tb]));
          }
          return u32x2{pkbf(hv[0], hv[1]), pkbf(hv[2], hv[3])};
        };
        lptr pcs = lds + L_CS + (fw * 48 + 4 * q) * 4;
        const int oa = fw ? 128 : 0, oc = fw ? 0 : 128;
        {
          const f32x4 cga = *(LAS f32x4*)(pcs + oa), cua = *(LAS f32x4*)(pcs + 384 + oa), cgb = *(LAS f32x4*)(pcs + 64), cub = *(LAS f32x4*)(pcs + 384 + 64);
#pragma unroll
          for (int tb = 0; tb < 4; ++tb) {
            const u32x2 ha = hcalc(acc[0][0][tb], acc[1][0][tb], cga, cua, tb);
            const u32x2 hb = hcalc(acc[0][1][tb], acc[1][1][tb], cgb, cub, tb);
            ST_SC1((u32x4{ha[0], ha[1], hb[0], hb[1]}), hoff + fw * 4096 + tb * 1024);
            SBAR();
          }
        }
        {
          const f32x4 cg = *(LAS f32x4*)(pcs + oc), cu = *(LAS f32x4*)(pcs + 384 + oc);
#pragma unroll
          for (int t2 = 0; t2 < 2; ++t2) {
            const u32x2 ha = hcalc(acc[0][2][2 * t2], acc[1][2][2 * t2], cg, cu, 2 * t2);
            SBAR();
            const u32x2 hb = hcalc(acc[0][2][2 * t2 + 1], acc[1][2][2 * t2 + 1], cg, cu, 2 * t2 + 1);
            SBAR();
            ST_SC1((u32x4{ha[0], ha[1], hb[0], hb[1]}), hoff + 8192 + fw * 2048 + t2 * 1024);
            SBAR();
          }
        }
      }
      if (EXP & 128) __builtin_amdgcn_s_setprio(0);
      STAMP(5);
      waitv<2>();
      if (lane < 8) __hip_atomic_store((GAS u64*)(ws + WS_MB + (rb * 16 + tw * 8 + lane) * MBS + cb * 16 + fw * 8), tag, RLX_AGENT);
    {
    const int sw = wid ^ 2, hm = sw & 1;
    lptr am = lds + SA + hm * PCH;
    f32x4 ca[8];
#pragma unroll
    for (int t = 0; t < 8; ++t) ca[t] = f32x4{0, 0, 0, 0};
    lptr pmain = am + O_KL + 1312 + lane * 16;

    flag_wait(fb, FL_KA + hm, 1);
    flag_wait(fb, FL_U, 2);
    STAMP(6);
    conv_units<0xff>(am + O_KREV, lds + L_U + (hm + odd) * UCH, lane, sw < 2 ? 5 : 0, sw < 2 ? 3 : 5, ca, [&](int it) {
      if (it == 0) { waitv<1>(); if (lane < 8) __hip_atomic_store((GAS u64*)(ws + WS_MB + (rb * 16 + tw * 8 + lane) * MBS + 256 + cb * 16 + fw * 8), tag, RLX_AGENT); } });
    if (sw < 2) {
      lptr pc = lds + L_PC;
#pragma unroll
      for (int t = 0; t < 8; ++t) { *(LAS f32x4*)(pmain + t * 1024) = ca[t]; ca[t] = f32x4{0, 0, 0, 0}; }
      flag_set(fb, FL_PA + hm, 1);
      flag_wait(fb, FL_KC, 1);
      lptr uc_ = lds + L_U + (odd ? 0 : 2) * UCH;
      if (odd) conv_units<0x3a>(lds + L_KC, uc_, lane, hm * 4, 4, ca, [](int) {});
      else conv_units<0xc5>(lds + L_KC, uc_, lane, hm * 4, 4, ca, [](int) {});
      STAMP(7);
      f32x4 c4[4];
      c4[0] = odd ? ca[1] : ca[0]; c4[1] = odd ? ca[3] : ca[2]; c4[2] = odd ? ca[4] : ca[6]; c4[3] = odd ? ca[5] : ca[7];
      if (!hm) {
#pragma unroll
        for (int t = 0; t < 4; ++t) *(LAS f32x4*)(pc + t * 1024 + lane * 16) = c4[t];
        flag_set(fb, FL_PC, 1);
      } else {
        flag_wait(fb, FL_PC, 1);
#pragma unroll
        for (int t = 0; t < 4; ++t) c4[t] += *(LAS f32x4*)(pc + t * 1024 + lane * 16);
        waitl();
#pragma unroll
        for (int t = 0; t < 4; ++t) { *(LAS unsigned*)(pc + t * 512 + q * 128 + r * 4) = pkbf(c4[t][0], c4[t][1]); *(LAS unsigned*)(pc + t * 512 + q * 128 + r * 4 + 64) = pkbf(c4[t][2], c4[t][3]); }
#pragma unroll
        for (int i = 0; i < 2; ++i) {
          const int s4 = 2 * i + (lane >> 5);
          const int T = odd ? (s4 == 0 ? 1 : s4 + 2) : (s4 < 2 ? 2 * s4 : s4 + 4);
          ST_SC1(ld16(pc + i * 1024 + lane * 16), WS_Y + ((T * 4 + ((lane >> 3) & 3)) * DM + chC) * 128 + (lane & 7) * 16);
        }
      }
    } else {
      STAMP(7);
      flag_wait(fb, FL_PA + hm, 1);
#pragma unroll
      for (int t = 0; t < 8; ++t) ca[t] += *(LAS f32x4*)(pmain + t * 1024);
#pragma unroll
      for (int t = 0; t < 8; ++t) { *(LAS unsigned*)(am + t * 512 + q * 128 + r * 4) = pkbf(ca[t][0], ca[t][1]); *(LAS unsigned*)(am + t * 512 + q * 128 + r * 4 + 64) = pkbf(ca[t][2], ca[t][3]); }
#pragma unroll
      for (int i = 0; i < 4; ++i) ST_SC1(ld16(am + i * 1024 + lane * 16), WS_Y + ((i * 8 + (lane >> 3)) * DM + chA + hm) * 128 + (lane & 7) * 16);
    }
    waitv<0>();
    if (flag_add(fb, FL_YC, lane) == 3u) {
      if (lane < 32) __hip_atomic_store((GAS u64*)(ws + WS_MB + (lane * 8 + xcd) * MBS + 512 + bi * 8), tag, RLX_AGENT);
      if (YS0) __builtin_amdgcn_s_sleep(YS0);
      _Pragma("nounroll") for (int sp = 0; sp < SPIN_GLB; ++sp) { if (wall(gflag_ok(ws, WS_MB + (tg * 8 + nb) * MBS + 512 + (lane & 31) * 8, tag))) break; __builtin_amdgcn_s_sleep(YS); }
      flag_set(fb, FL_YOK, 1);
    }

    flag_wait(fb, FL_YOK, 1);
    STAMP(8);
    u32x4 yv[3];
#pragma unroll
    for (int j = 0; j < 3; ++j) {
      const int p = umin(sw + 4 * j, 9), c = p * 64 + lane, row = (c * 6554) >> 16;
      glds1((const char*)x + (size_t)tg * 64 * 1280 + nb * 160, row * 1280 + (c - row * 10) * 16, lds0 + L_XT + p * 1024);
    }
#pragma unroll
    for (int j = 0; j < 3; ++j) { const int p = umin(sw + 4 * j, 9); yv[j] = LD_SC1(WS_Y + (tg * DM + nb * 80 + p * 8) * 128 + lane * 16); }
    lptr yb = lds + L_YB + sw * 3072;
#pragma unroll
    for (int j = 0; j < 3; ++j) st16(yb + j * 1024 + lane * 16, yv[j]);
#pragma unroll
    for (int j = 0; j < 3; ++j) {
      const u32x4 a = tr2(yb + j * 1024 + (r >> 2) * 128 + q * 32 + (lane & 3) * 8, 512);
      if (sw + 4 * j < 10) st16(lds + L_BASE + lane * 160 + (sw + 4 * j) * 16, a);
    }
    }
      __builtin_amdgcn_s_setprio(0);
    }
  }

  if (wid >= 4) {
    const int sw = wid - 4;
    const int lane = fresh_lane();
    {
      unsigned bo[4], ao[4];
#pragma unroll
      for (int c = 0; c < 4; ++c) { const int cc = sw * 4 + ((c + nb) & 3); bo[c] = WS_WD + (nb * 48 + cc * 3) * 5120; ao[c] = WS_H + (tg * 16 + cc) * 12288; }
      const unsigned l16 = lane * 16;
#define BFRAG(k, n) __builtin_amdgcn_raw_buffer_load_b128(rs, l16, bo[(k) < 8 ? (k) >> 1 : (k) - 8] + ((k) < 8 ? ((k) & 1) * 5120 : 10240) + (n) * 1024, SC1)
#define AFRAG(k, t) __builtin_amdgcn_raw_buffer_load_b128(rs, l16, ao[(k) < 8 ? (k) >> 1 : (k) - 8] + ((k) < 8 ? ((k) & 1) * 4096 : 8192) + (t) * 1024, (EXP & 8) ? 0 : SC1)
      u32x4 Bw[4][5];
#pragma unroll
      for (int k = 0; k < 4; ++k)
#pragma unroll
        for (int n = 0; n < 5; ++n) Bw[k][n] = BFRAG(k, n);
      STAMP(4);
      wd_dma(lane, sw, 0, 2);
      flag_wait(fb, FL_G1, 4);
      wd_dma(lane, sw, 2, 4);
      lptr park = lds + SL2 + sw * 10240 + (lane >> 2) * 160 + (lane & 3) * 8;
      asm volatile("" : "+v"(park));
      STAMP(7);
      const unsigned fo = WS_MB + (tg * 8 + nb) * MBS + (sw * 8 + (lane & 7)) * 8;
      _Pragma("nounroll") for (int sp = 0; sp < SPIN_GLB; ++sp) { if (wall(gflag_ok(ws, fo, tag))) break; __builtin_amdgcn_s_sleep(HS); }
      asm volatile("" ::: "memory");
      if (EXP & 1024) __builtin_amdgcn_s_sleep(127);
      STAMP(8);
      f32x4 a2[5][4];
#pragma unroll
      for (int n = 0; n < 5; ++n)
#pragma unroll
        for (int tb = 0; tb < 4; ++tb) a2[n][tb] = f32x4{0, 0, 0, 0};
      u32x4 Ah[4][4];
#pragma unroll
      for (int k = 0; k < 3; ++k)
#pragma unroll
        for (int tb = 0; tb < 4; ++tb) Ah[k][tb] = AFRAG(k, tb);
      u64 f2 = 0;
      SBAR();
#pragma unroll
      for (int k = 0; k < 12; ++k) {
        if (k == 3) { f2 = __hip_atomic_load((GAS u64*)(ws + fo + 256), RLX_AGENT); SBAR(); }
        if (k == 5) {
          unsigned tc;
          asm volatile("s_mov_b32 %1, %5\n\t1:\n\tv_cmp_ne_u64 vcc, %2, %0\n\ts_cbranch_vccz 2f\n\ts_sleep 2\n\t"
                       "global_load_dwordx2 %0, %3, %4 offset:256 sc1\n\ts_sub_u32 %1, %1, 1\n\ts_waitcnt vmcnt(0)\n\ts_cbranch_scc0 1b\n\t2:"
                       : "+v"(f2), "=&s"(tc) : "s"(tag), "v"(fo), "s"(ws), "i"(SPIN_GLB) : "memory", "vcc", "scc");
          SBAR();
        }
        u32x4 hf[4];
        if (k >= 8) {
          const u32x4 X0 = Ah[k & 3][0], X1 = Ah[k & 3][1], Y0 = Ah[k & 3][2], Y1 = Ah[k & 3][3];
          hf[0] = u32x4{X0[0], X0[1], Y0[0], Y0[1]}; hf[1] = u32x4{X0[2], X0[3], Y0[2], Y0[3]};
          hf[2] = u32x4{X1[0], X1[1], Y1[0], Y1[1]}; hf[3] = u32x4{X1[2], X1[3], Y1[2], Y1[3]};
        } else {
#pragma unroll
          for (int tb = 0; tb < 4; ++tb) hf[tb] = Ah[k & 3][tb];
        }
#pragma unroll
        for (int e = 0; e < 20; ++e) {
          const int n = e >> 2, tb = e & 3;
          a2[n][tb] = MFMA_BF(Bw[k & 3][n], hf[tb], a2[n][tb]);
          if (e & 1) {
            const int j = e >> 1;
            if (j < 4) { if (k + 3 < 12) Ah[(k + 3) & 3][j] = AFRAG(k + 3, j); }
            else if (j < 9 && k >= 1 && k + 3 < 12) {
              if (k + 3 < 8) Bw[(k + 3) & 3][j - 4] = BFRAG(k + 3, j - 4);
              else Bw[(k + 3) & 3][j - 4] = tr2(park + ((k - 5) >> 1) * (SL0 - SL2) + ((k - 5) & 1) * 5120 + (j - 4) * 32, 2560);
            }
            SBAR();
          }
        }
#pragma unroll
        for (int n = 0; n < 5; ++n) asm volatile("" : "+v"(a2[n][0]), "+v"(a2[n][1]), "+v"(a2[n][2]), "+v"(a2[n][3]));
        if ((EXP & 512) && k < 11) ARR();
      }
      STAMP(9);
      {
        lptr red = lds + SL2 + sw * 10240;
        const int l2 = fresh_lane(), r = l2 & 15, q = l2 >> 4;
#pragma unroll
        for (int n = 0; n < 5; ++n)
#pragma unroll
          for (int tb = 0; tb < 4; ++tb) {
            *(LAS f32x4*)(red + (q >> 1) * (SL0 - SL2) + (2 * n + (q & 1)) * 1024 + ((tb * 16 + r + 3 * n) & 63) * 16) = a2[n][tb];
          }
      }
    }
  }
  STAMP(10);
  BAR_L();
  STAMP(11);
  if (wid < 5) {
    const int tid = wid * 64 + fresh_lane();
    const int tok = (tid * 13108) >> 16, g = tid - tok * 5;
    lptr rp = lds + SL2 + g * 2048 + ((tok + 3 * g) & 63) * 16;
    f32x4 s[4];
#pragma unroll
    for (int m = 0; m < 4; ++m) {
      lptr p = rp + (m >> 1) * (SL0 - SL2) + (m & 1) * 1024;
      s[m] = (*(LAS f32x4*)(p) + *(LAS f32x4*)(p + 10240)) + (*(LAS f32x4*)(p + 20480) + *(LAS f32x4*)(p + 30720));
    }
    u16* op = out + (size_t)(tg * 64 + tok) * DM + nb * 80 + g * 16;
#pragma unroll
    for (int hh = 0; hh < 2; ++hh) {
      const u32x4 bs = ld16(lds + L_BASE + tid * 32 + hh * 16), xs = ld16(lds + L_XT + tid * 32 + hh * 16);
      const f32x4 sa = s[2 * hh], sb = s[2 * hh + 1];
      *(u32x4*)(op + hh * 8) = u32x4{pkbf((bflo(xs[0]) + bflo(bs[0])) + sa[0], (bfhi(xs[0]) + bfhi(bs[0])) + sa[1]), pkbf((bflo(xs[1]) + bflo(bs[1])) + sa[2], (bfhi(xs[1]) + bfhi(bs[1])) + sa[3]),
                                     pkbf((bflo(xs[2]) + bflo(bs[2])) + sb[0], (bfhi(xs[2]) + bfhi(bs[2])) + sb[1]), pkbf((bflo(xs[3]) + bflo(bs[3])) + sb[2], (bfhi(xs[3]) + bfhi(bs[3])) + sb[3])};
    }
  }
}

#if defined(__HIP_DEVICE_COMPILE__)
#pragma clang attribute pop
#endif

extern "C" void kernel_launch(void* const* d_in, const int* in_sizes, int n_in, void* d_out, int out_size, void* d_ws, size_t ws_size, hipStream_t stream) {
  (void)in_sizes; (void)n_in; (void)out_size; (void)ws_size;
  (void)hipFuncSetAttribute((const void*)fused_kernel, hipFuncAttributeMaxDynamicSharedMemorySize, LDS_BYTES);
  fused_kernel<<<256, 512, LDS_BYTES, stream>>>((const u16*)d_in[0], (const u16*)d_in[1], (const u16*)d_in[2], (const u16*)d_in[3], (const u16*)d_in[4],
                                                (const u16*)d_in[5], (const u16*)d_in[6], (const u16*)d_in[8], (const u16*)d_in[9], (const u16*)d_in[10],
                                                (u16*)d_out, (char*)d_ws);
}
```

```cpp
#define CPS 8
#define EXP 1
#define HSPLAN 0,2,3,7,10,14,17,20,24
#include <hip/hip_runtime.h>
#include <stdint.h>

#if defined(__HIP_DEVICE_COMPILE__)
#pragma clang attribute push(__attribute__((target("no-packed-fp32-ops"))), apply_to = function)
#endif
typedef unsigned short u16;
typedef unsigned long long u64;
typedef short bf16x8 __attribute__((ext_vector_type(8)));
typedef _Float16 f16x8 __attribute__((ext_vector_type(8)));
typedef short s16x4 __attribute__((ext_vector_type(4)));
typedef float f32x2 __attribute__((ext_vector_type(2)));
typedef float f32x4 __attribute__((ext_vector_type(4)));
typedef unsigned u32x2 __attribute__((ext_vector_type(2)));
typedef unsigned u32x4 __attribute__((ext_vector_type(4)));
typedef _Float16 h16x2 __attribute__((ext_vector_type(2)));
typedef __bf16 b16x2 __attribute__((ext_vector_type(2)));
#define LAS __attribute__((address_space(3)))
#define GAS __attribute__((address_space(1)))
typedef LAS char* lptr;
#define DI __device__ __forceinline__
#define SBAR() __builtin_amdgcn_sched_barrier(0)
#define SGB(m, n) __builtin_amdgcn_sched_group_barrier(m, n, 0)
#define MFMA_BF(a, b, c) __builtin_amdgcn_mfma_f32_16x16x32_bf16(__builtin_bit_cast(bf16x8, a), __builtin_bit_cast(bf16x8, b), c, 0, 0, 0)
#define MFMA_H(a, b, c) __builtin_amdgcn_mfma_f32_16x16x32_f16(__builtin_bit_cast(f16x8, a), __builtin_bit_cast(f16x8, b), c, 0, 0, 0)
#define SC1 16
#define CPW 6
constexpr unsigned CODE_LINES = 306;
#define YS 8
#define YS0 16
#define HS 2
#define PACE 2

constexpr int DM = 640, NM = 192;
constexpr unsigned WS_EPOCH = 0, WS_F0 = 4096, F0REP = 4352;
constexpr unsigned WS_MB = 262144, MBS = 1280;
constexpr unsigned WS_ST = 65536, WS_CS = 98304, WS_XB = 655360, WS_WD = WS_XB + 2621440 + 4096;
constexpr unsigned WS_H = WS_WD + 1966080, WS_Y = WS_H + 6291456, WS_END = WS_Y + 2621440;
constexpr unsigned WS_STAMP = 16u << 20;
constexpr int STAGE = 40960, SL1 = 0, SA = 40960, SL2 = 81920, SL0 = 122880, POOL_BIAS = SA;
constexpr int PROW = 784, PCH = 23 * PROW;
constexpr int KC1 = 4228, O_KREV = 128, O_KL = 8528;
constexpr int L_FLG = SA + 2 * PCH, L_SC = L_FLG + 128, L_STG = L_FLG + 192, STGW = 2304;
constexpr int L_STAT = L_STG, L_CS = L_STG + 1024;
constexpr int L_RTB = SL2 + 29696;
constexpr int L_LNT = L_STG;
constexpr int L_LNB = SL2 + 18432, LNR = 1344;
constexpr int UCH = 9232, UC1 = 4640;
constexpr int L_U = SL1 + 128, L_KC = L_U + 3 * UCH + 128, L_PC = L_KC + 8400;
constexpr int L_XT = SL1;
constexpr int L_BASE = SA, L_YB = SA + 10240;
constexpr int LDS_BYTES = 163840;
static_assert(O_KL + 1312 + 8192 <= PCH && L_LNB + 8 * LNR <= L_RTB && L_RTB + 10240 + 256 <= SL0 && L_PC + 4096 <= SA && L_STG + 2 * STGW <= SL0 && (L_KC & 15) == 0 && (L_PC & 15) == 0 && (L_STG & 15) == 0, "lds map");
enum { FL_F0 = 0, FL_EPI, FL_U, FL_KA, FL_KB, FL_KC, FL_KP, FL_PA, FL_PB, FL_PC, FL_YC, FL_YOK, FL_H1, FL_H2, FL_G1, FL_S2, FL_TAG = 16 };
constexpr int SPIN_LDS = 1 << 20, SPIN_GLB = 1 << 17;

DI bool wall(bool p) { return __builtin_amdgcn_ballot_w64(!p) == 0; }
DI unsigned umin(unsigned a, unsigned b) { return a < b ? a : b; }
DI unsigned umax(unsigned a, unsigned b) { return a > b ? a : b; }
DI float asf(unsigned v) { return __builtin_bit_cast(float, v); }
DI unsigned asu(float v) { return __builtin_bit_cast(unsigned, v); }
DI float bflo(unsigned v) { return asf(v << 16); }
DI float bfhi(unsigned v) { return asf(v & 0xffff0000u); }
DI float bf1(u16 v) { return asf((unsigned)v << 16); }
DI unsigned pkbf(float lo, float hi) { return __builtin_bit_cast(unsigned, __builtin_convertvector(f32x2{lo, hi}, b16x2)); }
DI unsigned pkh(f32x2 v) { return __builtin_bit_cast(unsigned, __builtin_convertvector(v, h16x2)); }
DI float rbf(float x) { return bflo(pkbf(x, x)); }
template <int CTRL> DI float dppf(float x) { return __builtin_bit_cast(float, __builtin_amdgcn_mov_dpp(__builtin_bit_cast(int, x), CTRL, 0xf, 0xf, true)); }
DI float row16_sum(float v) { v += dppf<0xB1>(v); v += dppf<0x4E>(v); v += dppf<0x124>(v); v += dppf<0x128>(v); return v; }

DI void glds4(const void* sb, unsigned v0, unsigned v1, unsigned v2, unsigned v3, unsigned lds) {
  asm volatile("s_mov_b32 m0, %5\n\ts_nop 0\n\tglobal_load_lds_dwordx4 %0, %4\n\tglobal_load_lds_dwordx4 %1, %4 offset:1024\n\t"
               "global_load_lds_dwordx4 %2, %4 offset:2048\n\tglobal_load_lds_dwordx4 %3, %4 offset:3072"
               :: "v"(v0), "v"(v1), "v"(v2), "v"(v3), "s"(sb), "s"(lds) : "memory");
}
DI void glds1(const void* sb, unsigned v0, unsigned lds) {
  asm volatile("s_mov_b32 m0, %2\n\ts_nop 0\n\tglobal_load_lds_dwordx4 %0, %1" :: "v"(v0), "s"(sb), "s"(lds) : "memory");
}
DI void glds2(const void* sb, unsigned v0, unsigned v1, unsigned lds) {
  asm volatile("s_mov_b32 m0, %3\n\ts_nop 0\n\tglobal_load_lds_dwordx4 %0, %2\n\tglobal_load_lds_dwordx4 %1, %2 offset:1024"
               :: "v"(v0), "v"(v1), "s"(sb), "s"(lds) : "memory");
}
template <int N> DI void waitv() { asm volatile("s_waitcnt vmcnt(%0)" :: "i"(N) : "memory"); }
DI void waitl() { asm volatile("s_waitcnt lgkmcnt(0)" ::: "memory"); }
DI u32x2 vtr(lptr p) { return __builtin_bit_cast(u32x2, __builtin_amdgcn_ds_read_tr16_b64_v4i16((LAS s16x4*)p)); }
DI u32x4 tr2(lptr p, int d) { u32x2 lo = vtr(p), hi = vtr(p + d); return u32x4{lo[0], lo[1], hi[0], hi[1]}; }
DI u32x4 ld16(lptr p) { return *(LAS u32x4*)p; }
DI void st16(lptr p, u32x4 v) { *(LAS u32x4*)p = v; }

DI unsigned flag_ld(unsigned addr) { unsigned r; asm volatile("ds_read_b32 %0, %1\n\ts_waitcnt lgkmcnt(0)" : "=v"(r) : "v"(addr) : "memory"); return __builtin_amdgcn_readfirstlane(r); }
DI void flag_wait(unsigned fb, int idx, unsigned need) { _Pragma("nounroll") for (int sp = 0; sp < SPIN_LDS && flag_ld(fb + idx * 4) < need; ++sp) __builtin_amdgcn_s_sleep(1); }
DI void flag_set(unsigned fb, int idx, unsigned v) {
  asm volatile("s_waitcnt lgkmcnt(0)\n\ts_mov_b64 exec, 1\n\tds_write_b32 %0, %1\n\ts_mov_b64 exec, -1" :: "v"(fb + idx * 4), "v"(v) : "memory"); }
DI unsigned flag_add(unsigned fb, int idx, int lane) {
  unsigned r; (void)lane;
  asm volatile("s_waitcnt lgkmcnt(0)\n\ts_mov_b64 exec, 1\n\tds_add_rtn_u32 %0, %1, %2\n\ts_mov_b64 exec, -1\n\ts_waitcnt lgkmcnt(0)" : "=&v"(r) : "v"(fb + idx * 4), "v"(1u) : "memory");
  return __builtin_amdgcn_readfirstlane(r);
}
#define RLX_AGENT __ATOMIC_RELAXED, __HIP_MEMORY_SCOPE_AGENT
DI bool gflag_ok(const char* ws, unsigned off, u64 tag) { return __hip_atomic_load((GAS u64*)(ws + off), RLX_AGENT) == tag; }

#define STAMP(i) do {} while (0)

DI f32x2 csq(f32x2 x) { const float t = x.x + x.x; return f32x2{__builtin_fmaf(x.x, x.x, -(x.y * x.y)), t * x.y}; }
DI h16x2 toh(f32x2 v) { return __builtin_convertvector(v, h16x2); }
DI h16x2 hmul(h16x2 x, h16x2 y) { return __builtin_elementwise_fma(h16x2{x.y, x.y}, h16x2{-y.y, y.x}, h16x2{x.x, x.x} * y); }
DI f32x2 cexp_p(float a2, float bh, float bl, float p) {
  const float e = __builtin_amdgcn_exp2f(a2 * p);
  const float ph = __builtin_fmaf(bl, p, __builtin_amdgcn_fractf(bh * p));
  return f32x2{e * __builtin_amdgcn_cosf(ph), e * __builtin_amdgcn_sinf(ph)};
}
template <int NS, int ROW0> DI void put4(const unsigned (&tb)[NS], const h16x2 (&a)[NS], const h16x2 (&b)[NS], const h16x2 (&c)[NS], const h16x2 (&d)[NS]) {
#pragma unroll
  for (int e = 0; e < NS; ++e)
    asm volatile("s_mov_b32 m0, %4\n\ts_nop 0\n\tds_write_addtid_b32 %0 offset:%5\n\tds_write_addtid_b32 %1 offset:%5+784\n\t"
                 "ds_write_addtid_b32 %2 offset:%5+1568\n\tds_write_addtid_b32 %3 offset:%5+2352"
                 :: "v"(a[e]), "v"(b[e]), "v"(c[e]), "v"(d[e]), "s"(tb[e]), "i"(ROW0 * PROW + POOL_BIAS) : "memory");
}
#define FORS _Pragma("unroll") for (int e = 0; e < NS; ++e)
template <int NS> DI void kpool(const unsigned (&tb)[NS], const float (&pv)[NS][4], const float (&ldt)[NS], bool join, float (&sc)[NS]) {
  float dt[NS];
  FORS dt[e] = rbf(__builtin_amdgcn_exp2f(ldt[e] * 1.44269504f));
  unsigned eb[NS]; float cs[NS];
  FORS {
    unsigned mi = umax(asu(pv[e][2]) & 0x7fffffffu, asu(pv[e][3]) & 0x7fffffffu);
    mi = umax(mi, asu(dppf<0xB1>(asf(mi)))); mi = umax(mi, asu(dppf<0x4E>(asf(mi)))); mi = umax(mi, asu(dppf<0x124>(asf(mi)))); mi = umax(mi, asu(dppf<0x128>(asf(mi))));
    eb[e] = umax(umax((unsigned)__builtin_amdgcn_readlane(mi, 0), (unsigned)__builtin_amdgcn_readlane(mi, 16)),
                 umax((unsigned)__builtin_amdgcn_readlane(mi, 32), (unsigned)__builtin_amdgcn_readlane(mi, 48))) >> 23;
  }
  if (join) eb[0] = eb[NS - 1] = umax(eb[0], eb[NS - 1]);
  FORS { eb[e] = umin(umax(eb[e], 2u), 252u); cs[e] = asf((254u - eb[e]) << 23); }
  float a2[NS], bh[NS], bl[NS];
  FORS {
    const float ea = rbf(__builtin_amdgcn_exp2f(pv[e][0] * 1.44269504f));
    a2[e] = -(ea * dt[e]) * 1.44269504f;
    const float b = pv[e][1] * dt[e];
    bh[e] = b * 0.15915494f;
    bl[e] = __builtin_fmaf(b, 6.4206382e-9f, __builtin_fmaf(b, 0.15915494f, -bh[e]));
  }
  h16x2 one[NS], w8[NS], w16[NS], w24[NS], w32[NS], w64[NS], w96[NS], w1[NS], w2[NS], w3[NS], w4[NS], C[NS], c4[NS];
  FORS {
    const f32x2 f1 = cexp_p(a2[e], bh[e], bl[e], 1.f), f2 = csq(f1), f4 = csq(f2);
    const f32x2 f8 = csq(f32x2{f4.x, -f4.y}), f16 = csq(f8), f32 = csq(f16), f64 = csq(f32);
    one[e] = h16x2{(_Float16)1.f, (_Float16)0.f};
    w8[e] = toh(f8); w16[e] = toh(f16); w32[e] = toh(f32); w64[e] = toh(f64); w24[e] = hmul(w8[e], w16[e]); w96[e] = hmul(w32[e], w64[e]);
    w1[e] = toh(f1); w2[e] = toh(f2); w4[e] = toh(f4); w3[e] = hmul(w1[e], w2[e]);
    C[e] = toh(f32x2{pv[e][2] * cs[e], pv[e][3] * cs[e]}); c4[e] = hmul(C[e], w4[e]);
  }
  put4<NS, 0>(tb, one, w8, w16, w24);
  put4<NS, 4>(tb, w32, w64, w96, w1);
  put4<NS, 8>(tb, w2, w3, C, c4);
  h16x2 c128[NS], c132[NS], w256[NS], w258[NS], w512[NS], w768[NS], w1024[NS], w1280[NS], w1536[NS], w1792[NS], w1796[NS];
  FORS {
    const f32x2 f128 = cexp_p(a2[e], bh[e], bl[e], 128.f), f256 = csq(f128), f512 = csq(f256);
    const h16x2 w128 = toh(f128);
    w256[e] = toh(f256); w512[e] = toh(f512); c128[e] = hmul(C[e], w128); c132[e] = hmul(c4[e], w128);
    w258[e] = hmul(w256[e], w2[e]); w768[e] = hmul(w512[e], w256[e]);
    w1024[e] = toh(csq(f512)); w1280[e] = hmul(w1024[e], w256[e]); w1536[e] = hmul(w1024[e], w512[e]);
    w1792[e] = hmul(w1024[e], w768[e]); w1796[e] = hmul(w1792[e], w4[e]);
  }
  put4<NS, 12>(tb, c128, c132, w256, w258);
  put4<NS, 16>(tb, w512, w768, w1024, w1280);
  put4<NS, 19>(tb, w1280, w1536, w1792, w1796);
  FORS sc[e] = 2.f * dt[e] * asf(eb[e] << 23);
}
struct KPtr {
  lptr pbx, pby, py, px0, px2;
  DI void init(lptr pool, int lane) {
    const int r = lane & 15, x = r & 3, y = r >> 2;
    lptr p = pool + (lane >> 4) * 16;
    pbx = p + x * PROW; pby = p + (y ? 3 + y : 0) * PROW; py = p + (10 + y) * PROW; px0 = p + (x ? 6 + x : 0) * PROW; px2 = p + (y == 3 ? 22 : 18 + x) * PROW;
  }
  DI void fetch(u32x4 (&v)[6], int o) const {
    v[0] = ld16(pbx + o); v[1] = ld16(pby + o); v[2] = ld16(py + o); v[3] = ld16(px0 + o); v[4] = ld16(pbx + 14 * PROW + o); v[5] = ld16(px2 + o);
  }
  DI void advance() { pbx += 128; pby += 128; py += 128; px0 += 128; px2 += 128; }
};
DI void kprod(const u32x4 (&v)[6], u32x4 (&o)[4]) {
  unsigned t[16];
  if (EXP & 2048) { SBAR(); __builtin_amdgcn_s_setprio(3); SBAR(); }
#pragma unroll
  for (int i = 0; i < 16; ++i) { const unsigned xe = v[(i >> 2) ? 2 + (i >> 2) : 0][i & 3], ye = v[(i >> 2) ? 2 : 1][i & 3];
    asm("v_pk_mul_f16 %0, %1, %2 op_sel_hi:[0,1]" : "=v"(t[i]) : "v"(xe), "v"(ye)); }
  SBAR();
#pragma unroll
  for (int i = 0; i < 16; ++i) { const unsigned xe = v[(i >> 2) ? 2 + (i >> 2) : 0][i & 3], ye = v[(i >> 2) ? 2 : 1][i & 3]; unsigned z;
    asm("v_pk_fma_f16 %0, %1, %2, %3 op_sel:[1,1,0] op_sel_hi:[1,0,1] neg_lo:[0,1,0]" : "=v"(z) : "v"(xe), "v"(ye), "v"(t[i])); o[i >> 2][i & 3] = z; }
  SBAR();
}
template <int H> DI void kprodh(const u32x4 (&v)[6], u32x4 (&o)[4]) {
  unsigned t[8];
#pragma unroll
  for (int i = 8 * H; i < 8 * H + 8; ++i) { const unsigned xe = v[(i >> 2) ? 2 + (i >> 2) : 0][i & 3], ye = v[(i >> 2) ? 2 : 1][i & 3];
    asm("v_pk_mul_f16 %0, %1, %2 op_sel_hi:[0,1]" : "=v"(t[i & 7]) : "v"(xe), "v"(ye)); }
  SBAR();
#pragma unroll
  for (int i = 8 * H; i < 8 * H + 8; ++i) { const unsigned xe = v[(i >> 2) ? 2 + (i >> 2) : 0][i & 3], ye = v[(i >> 2) ? 2 : 1][i & 3]; unsigned z;
    asm("v_pk_fma_f16 %0, %1, %2, %3 op_sel:[1,1,0] op_sel_hi:[1,0,1] neg_lo:[0,1,0]" : "=v"(z) : "v"(xe), "v"(ye), "v"(t[i & 7])); o[i >> 2][i & 3] = z; }
  asm volatile("" : "+v"(o[2 * H]), "+v"(o[2 * H + 1]));
  SBAR();
}
DI void kmma1(const u32x4 (&o0)[4], f32x4 (&acc)[3], bool first) {
  SBAR(); asm volatile("s_setprio 3\n\ts_nop 1"); SBAR();
#pragma unroll
  for (int i = 0; i < 3; ++i) acc[i] = MFMA_H(o0[1 + i], o0[0], (first ? f32x4{0, 0, 0, 0} : acc[i]));
  SBAR(); __builtin_amdgcn_s_setprio(0); SBAR();
}
DI void kcombine(f32x4 (&ko)[3], const f32x4 (&ka)[3][3], float s0, float s1, float s2) {
#pragma unroll
  for (int i = 0; i < 3; ++i)
#pragma unroll
    for (int j = 0; j < 4; ++j) ko[i][j] = __builtin_fmaf(ka[2][i][j], s2, __builtin_fmaf(ka[1][i][j], s1, ka[0][i][j] * s0));
}
DI float ubf(const u16* p, int i) { const unsigned w = ((const __attribute__((address_space(4))) unsigned*)(uintptr_t)p)[i >> 1]; return (i & 1) ? bfhi(w) : bflo(w); }
DI void kmma2(const u32x4 (&o0)[4], const u32x4 (&o1)[4], f32x4 (&acc)[3]) {
  SBAR(); asm volatile("s_setprio 3\n\ts_nop 1"); SBAR();
#pragma unroll
  for (int i = 0; i < 3; ++i) acc[i] = MFMA_H(o0[1 + i], o0[0], acc[i]);
#pragma unroll
  for (int i = 0; i < 3; ++i) acc[i] = MFMA_H(o1[1 + i], o1[0], acc[i]);
  SBAR(); __builtin_amdgcn_s_setprio(0); SBAR();
}
DI void kstore(lptr kl, int lane, const f32x4 (&acc)[3], float dsk) {
  const int c = lane & 15, q = lane >> 4;
  LAS u16* t = (LAS u16*)kl;
  const float k0 = acc[0][0] + (lane == 0 ? dsk : 0.f);
  *(LAS u32x2*)(t + 128 * (q >> 1) + 8 * c + 4 * (q & 1)) = u32x2{pkbf(k0, acc[0][1]), pkbf(acc[0][2], acc[0][3])};
  *(LAS unsigned*)(t + 256 + 64 * (q >> 1) + 2 * (q & 1) + 4 * c) = pkbf(acc[1][0], acc[1][1]);
  const unsigned p1 = pkbf(acc[1][2], acc[1][3]);
  const int i1 = 384 + 32 * (q >> 1) + (q & 1) + 2 * c;
  t[i1] = (u16)p1; t[i1 + 64] = (u16)(p1 >> 16);
  const unsigned r0 = pkbf(acc[2][0], acc[2][1]), r1 = pkbf(acc[2][2], acc[2][3]);
  if (!(q & 1)) { const int i2 = 512 + 8 * q + c; t[i2] = (u16)r0; t[i2 + 32] = (u16)(r0 >> 16); t[i2 + 64] = (u16)r1; t[i2 + 96] = (u16)(r1 >> 16); }
  else if (lane == 63) t[640] = (u16)r0;
}
DI unsigned dup16(unsigned a) { return a | (a << 16); }
DI unsigned bsel(unsigned m, unsigned a, unsigned b) { asm("" : "+v"(m)); return (a & m) | (b & ~m); }
DI unsigned rot16(unsigned a) { return __builtin_amdgcn_alignbit(a, a, 16); }
DI void kexpand(lptr kr, lptr kl, int lane) {
  LAS u16* t = (LAS u16*)kl;
  auto put = [&](int ch, unsigned dp, unsigned d0, unsigned d1, unsigned d2, unsigned d3) {
    st16(kr + 16 * ch, u32x4{d0, d1, d2, d3}); st16(kr + KC1 - 4 + 16 * ch, u32x4{dp, d0, d1, d2}); };
  const int n = 63 - lane, n0 = n & 31;
  const unsigned a0 = t[576 + n], b0 = t[577 + n], a1 = t[512 + n], b1 = t[513 + n];
  const unsigned ab = *(LAS unsigned*)(t + 384 + 2 * n), c2 = t[386 + 2 * n];
  const u32x2 v1 = *(LAS u32x2*)(t + 256 + 4 * n0); const unsigned q4 = t[260 + 4 * n0];
  const u32x4 v0 = ld16((lptr)(t + 8 * n0)); const unsigned w = *(LAS unsigned*)(t + 8 * n0 + 8);
  const unsigned mo = (n & 1) ? ~0u : 0u, lo = lane >= 32 ? ~0u : 0u, m31 = n == 31 ? ~0u : 0u;
  put(lane, dup16(b0), dup16(b0), bsel(mo, dup16(b0), b0 | (a0 << 16)), dup16(a0), dup16(a0));
  put(64 + lane, dup16(b1), dup16(b1), bsel(mo, dup16(b1), b1 | (a1 << 16)), dup16(a1), dup16(a1));
  put(128 + lane, dup16(c2), dup16(c2), dup16(ab >> 16), rot16(ab), dup16(ab & 0xffffu));
  put(192 + lane, bsel(lo, bsel(m31, dup16(w & 0xffffu), rot16(w)), dup16(q4)), bsel(lo, rot16(v0[3]), q4 | (v1[1] & 0xffff0000u)),
      bsel(lo, rot16(v0[2]), dup16(v1[1] & 0xffffu)), bsel(lo, rot16(v0[1]), (v1[1] & 0xffffu) | (v1[0] & 0xffff0000u)), bsel(lo, rot16(v0[0]), dup16(v1[0] & 0xffffu)));
  if (lane >= 60) put(319 - lane, lane == 63 ? rot16(v0[0]) : 0u, 0u, 0u, 0u, 0u);
}

struct ConvPlan { int n; int item[36][3]; };
constexpr ConvPlan conv_plan(int tmask, int maxt) {
  ConvPlan p{};
  int fifo[64] = {}, head = 0, tail = 0, carry[16] = {}, nc = 0, per = (tmask == 0xff) ? 1 : 2;
  for (int pass = 0; pass < 2; ++pass) {
    head = tail = 0;
    for (int i = 0; i < nc; ++i) fifo[tail++] = carry[i] + 32;
    int e = 0;
    for (int m = maxt; m >= 0; --m) {
      for (int j = 0; j + m <= maxt; ++j) {
        const bool on = (tmask >> (m + j)) & 1;
        int k = 0;
        if (m == 0) { if (on && j < maxt - 1) p.item[e][k++] = j; else fifo[tail++] = j; }
        if (j + m == maxt) { fifo[tail++] = 8 + 2 * m; fifo[tail++] = 9 + 2 * m; }
        if (on) { while (k < per && head < tail) p.item[e][k++] = fifo[head++]; while (k < 3) p.item[e][k++] = -1; ++e; }
      }
    }
    p.n = e; nc = 0;
    while (head < tail) carry[nc++] = fifo[head++];
  }
  return p;
}
template <int TMASK, class F>
DI void conv_units(lptr kr, lptr ub, int lane, int rho0, int nrho, f32x4 (&acc)[8], F hook) {
  constexpr int MAXT = (TMASK & 0x80) ? 7 : 5;
  constexpr ConvPlan plan = conv_plan(TMASK, MAXT);
  const int r = lane & 15, q = lane >> 4;
  lptr pk = kr + ((r & 1) ? 0 : KC1) + 4092 - 4 * r + 16 * q - 3584 - 64 * rho0 - 64;
  lptr pu = ub + ((r & 1) ? 0 : UC1) + 64 * (r >> 1) + 16 * q + 64 * (7 - rho0) - 64;
  lptr pk2 = pk + 8;
  u32x2 Kl[8], Kh[8]; u32x4 Uf[8];
  asm volatile("" : "+v"(pk), "+v"(pk2), "+v"(pu));
#pragma unroll
  for (int j = 0; j <= MAXT; ++j) { const int m = MAXT - j; Uf[j] = ld16(pu + 64 + 512 * j); Kl[m] = *(LAS u32x2*)(pk + 64 + 512 * (7 - m)); Kh[m] = *(LAS u32x2*)(pk2 + 64 + 512 * (7 - m)); SBAR(); }
#pragma unroll 1
  for (int it = 0; it < nrho; ++it) {
    int e = 0;
#pragma unroll
    for (int m = MAXT; m >= 0; --m) {
#pragma unroll
      for (int j = 0; j + m <= MAXT; ++j) {
        if ((TMASK >> (m + j)) & 1) {
          acc[m + j] = MFMA_BF(Uf[j], (u32x4{Kl[m][0], Kl[m][1], Kh[m][0], Kh[m][1]}), acc[m + j]);
#pragma unroll
          for (int k = 0; k < 3; ++k) {
            const int c = plan.item[e][k];
            if (c >= 0) {
              const int o = (c & 32) ? 64 : 0, cc = c & 31, mm = (cc - 8) >> 1;
              if (cc < 8) Uf[cc] = ld16(pu + o + 512 * cc);
              else if (cc & 1) Kh[mm] = *(LAS u32x2*)(pk2 + o + 512 * (7 - mm));
              else Kl[mm] = *(LAS u32x2*)(pk + o + 512 * (7 - mm));
            }
          }
          SBAR();
          ++e;
        }
      }
    }
    pk -= 64; pk2 -= 64; pu -= 64;
    asm volatile("" : "+v"(pk), "+v"(pk2), "+v"(pu));
    hook(it);
  }
}

template <int N> struct IC { static constexpr int value = N; };
DI u64 uni64(u64 v) { return (u64)(unsigned)__builtin_amdgcn_readfirstlane((unsigned)v) | ((u64)(unsigned)__builtin_amdgcn_readfirstlane((unsigned)(v >> 32)) << 32); }
DI int fresh_lane() { int l; asm volatile("v_mbcnt_lo_u32_b32 %0, -1, 0\n\tv_mbcnt_hi_u32_b32 %0, -1, %0" : "=v"(l)); return l; }
#define ARR() do {} while (0)
#define BAR() do { SBAR(); ARR(); asm volatile("s_barrier" ::: "memory"); SBAR(); } while (0)
#define BAR_L() do { SBAR(); asm volatile("s_waitcnt lgkmcnt(0)" ::: "memory"); SBAR(); ARR(); asm volatile("s_barrier" ::: "memory"); SBAR(); } while (0)
#define LD_SC1(off) __builtin_amdgcn_raw_buffer_load_b128(rs, (off), 0, SC1)
#define ST_SC1(v, off) __builtin_amdgcn_raw_buffer_store_b128((v), rs, (off), 0, SC1)

__global__ __launch_bounds__(512, 2) void fused_kernel(const u16* __restrict__ x, const u16* __restrict__ p_lar, const u16* __restrict__ p_aim,
                                                       const u16* __restrict__ p_cr, const u16* __restrict__ p_ci, const u16* __restrict__ p_ldt,
                                                       const u16* __restrict__ p_dsk, const u16* __restrict__ wg, const u16* __restrict__ wu,
                                                       const u16* __restrict__ wd, u16* __restrict__ out, char* ws) {
  extern __shared__ __attribute__((aligned(16))) char smem[];
  const int tid = __builtin_amdgcn_workitem_id_x(), bid = __builtin_amdgcn_workgroup_id_x();
  if (__builtin_amdgcn_readfirstlane(tid >> 6) == CPW) {
    u64 pc_; asm volatile("s_getpc_b64 %0" : "=s"(pc_));
    asm volatile("s_mov_b32 m0, %2\n\ts_mov_b32 exec_hi, %3\n\ts_mov_b32 exec_lo, %4\n\tglobal_load_lds_dword %0, %1\n\ts_mov_b64 exec, -1"
                 :: "v"(umin(((bid >> 3) & (CPS - 1)) * (320 / CPS) + (tid & 63), CODE_LINES - 1) * 128), "s"(pc_ & ~127ull), "s"((unsigned)(uintptr_t)smem + L_RTB + 10240),
                    "i"(320 / CPS > 32 ? (1u << ((320 / CPS - 32) & 31)) - 1 : 0u), "i"(320 / CPS >= 32 ? -1 : (int)((1u << ((320 / CPS) & 31)) - 1)) : "memory");
  }
  lptr lds = (lptr)smem;
  const unsigned lds0 = (unsigned)(uintptr_t)smem;
  const unsigned fb = lds0 + L_FLG;
  const int wraw = __builtin_amdgcn_readfirstlane(tid >> 6), lane = tid & 63;
  const int wid = wraw ^ ((wraw >> 2) << 1);
  const int xcd = bid & 7, bi = bid >> 3;
  const int rb = (xcd >> 2) * 8 + (bi >> 2), cb = (xcd & 3) * 4 + (bi & 3);
  const int tg = (xcd >> 1) * 8 + (bi >> 2), nb = (xcd & 1) * 4 + (bi & 3);
  const int cw = xcd * 32 + bi, odd = cw & 1, c0 = 5 * (cw >> 1) + 2 * odd;
  const int chA = c0 + odd, chC = odd ? c0 : c0 + 2;
  const int rm = rb & 7, rot = 2 * (rm >= 5 ? rm - 5 : rm) + (rb >> 3);
  const bool csum = rm < 5;
  const __amdgpu_buffer_rsrc_t rs = __builtin_amdgcn_make_buffer_rsrc(ws, 0, (int)WS_END, 0x00020000);
  const int r = lane & 15, q = lane >> 4;

  u64 tag = 0;
  const int lw = wid - 4, kw = wid - 6;
  unsigned vx[4], vw[12];
  auto issue = [&](int s, int sbase, int h, int P0 = 0, int P1 = 5) {
    int ks = s + rot; if (ks >= 10) ks -= 10;
    const unsigned base = lds0 + sbase;
    const char* sx = (const char*)x + (size_t)(rb * 128 + h * 64) * 1280 - 4096 + ks * 128;
    const char* sw = (const char*)(h ? wu : wg) + cb * 192 - 4096 + (size_t)ks * 64 * 3072;
    if (P0 <= 0 && 0 < P1) glds4(sx, vx[0], vx[1], vx[2], vx[3], base + h * 8192);
    if (P0 <= 1 && 1 < P1) glds4(sw, vw[0], vw[1], vw[2], vw[3], base + 16384 + h * 12288);
    if (P0 <= 2 && 2 < P1) glds4(sx + 32 * 1280, vx[0], vx[1], vx[2], vx[3], base + h * 8192 + 4096);
    if (P0 <= 3 && 3 < P1) glds4(sw, vw[4], vw[5], vw[6], vw[7], base + 16384 + h * 12288 + 4096);
    if (P0 <= 4 && 4 < P1) glds4(sw, vw[8], vw[9], vw[10], vw[11], base + 16384 + h * 12288 + 8192);
  };
  auto prep_dma = [&](int ln) {
#pragma unroll
    for (int j = 0; j < 4; ++j) { const int row = 8 * j + (ln >> 3); vx[j] = row * 1280 + (((ln & 7) ^ ((row >> 1) & 7)) << 4) + 4096 - 1024 * j; }
#pragma unroll
    for (int j = 0; j < 3; ++j) { const int c = j * 64 + ln, row = (c * 43691) >> 19, pos = c - row * 12;
      const unsigned v = row * 3072 + ((pos ^ (((row >> 3) & 1) << 1)) << 4) + 4096;
#pragma unroll
      for (int g = 0; g < 4; ++g) vw[3 * g + j] = v + g * 16 * 3072 - 1024 * ((3 * g + j) & 3); }
  };
  auto wd_dma = [&](int ln, int w, int j0, int j1) {
    unsigned v[5];
#pragma unroll
    for (int i = 0; i < 5; ++i) { const int c = i * 64 + ln, row = (c * 6554) >> 16; v[i] = row * 1280 + (c - row * 10) * 16 + 4096 - (i & 3) * 1024; }
#pragma unroll
    for (int j = j0; j < j1; ++j) {
      const char* sp = (const char*)wd + (size_t)((w * 4 + ((j + nb) & 3)) * 96 + 32) * 1280 + nb * 160 - 4096;
      const unsigned d = lds0 + ((j >> 1) ? SL0 : SL2) + w * 10240 + (j & 1) * 5120;
      glds4(sp, v[0], v[1], v[2], v[3], d); glds1(sp, v[4], d + 4096);
    }
  };
  const int lnb = cw;
  const int ro0 = (0x100222 >> (4 * wid)) & 3, th0 = (0x000210 >> (4 * wid)) & 3, ro1 = (wid >> 1) & 1, th1 = 1 + (wid & 1);
  const int ch0 = c0 + ((0x0210 >> (4 * (ro0 + odd))) & 3), ch1 = chA + ro1;
  const int pt0 = ro0 * PCH + (ro0 >> 1) * (SL2 - SA - 2 * PCH) + th0 * 256, pt1 = ro1 * PCH + th1 * 256;
  unsigned o = (lane < 32 ? ch0 * 384 + th0 * 128 : ch1 * 384 + th1 * 128) + (lane & 31) * 4;
  unsigned xo0 = lane * 16, xo1 = q * 1280 + r * 16 + 1024;
  asm volatile("" : "+v"(o), "+v"(xo0), "+v"(xo1));
  asm volatile("" :: "s"(x), "s"(p_lar), "s"(p_aim), "s"(p_cr), "s"(p_ci), "s"(p_ldt), "s"(p_dsk), "s"(wg), "s"(wu), "s"(wd), "s"(out), "s"(ws));
  const int ptp = wid == 5 ? pt0 : pt1;
  if ((0x2f >> wid) & 1) {
    asm volatile("s_mov_b32 m0, %5\n\ts_nop 0\n\tglobal_load_lds_dword %0, %1\n\tglobal_load_lds_dword %0, %2 offset:784\n\t"
                 "global_load_lds_dword %0, %3 offset:1568\n\tglobal_load_lds_dword %0, %4 offset:2352"
                 :: "v"(o), "s"(p_lar), "s"((const char*)p_aim - 784), "s"((const char*)p_cr - 1568), "s"((const char*)p_ci - 2352), "s"(lds0 + SA + ptp) : "memory");
  }
  if (wid >= 6) {
    const char* xr = (const char*)x + (size_t)(lnb * 8 + kw * 4) * 1280;
#pragma unroll
    for (int j = 0; j < 4; ++j) glds1(xr + j * 1280, xo0, lds0 + L_LNB + (kw * 4 + j) * LNR);
    glds1(xr, xo1, lds0 + L_LNT + kw * 1024);
  } else if (wid >= 4) { if (lane < 16) *(LAS unsigned*)(lds + L_FLG + lane * 4) = 0; prep_dma(lane); }
  SBAR(); asm volatile("s_waitcnt lgkmcnt(0)\n\ts_barrier" ::: "memory"); SBAR();
  const float ldt0 = ubf(p_ldt, ch0), ldt1 = ubf(p_ldt, ch1);
  if (wid == 4 || wid == 5) {
    issue(0, SL0, lw);
  }
  auto lds_params = [&](float (&pv)[4], int m) {
#pragma unroll
    for (int e = 0; e < 4; ++e) pv[e] = bf1(*(LAS u16*)(lds + SA + ptp + e * PROW + m * 2));
  };
  if (wid >= 6) {
    waitv<0>();
    STAMP(0);
    lptr xp = lds + L_LNB + (kw * 4 + q) * LNR + r * 16;
    u32x4 xv[5];
#pragma unroll
    for (int j = 0; j < 4; ++j) xv[j] = ld16(xp + 256 * j);
    xv[4] = ld16(lds + L_LNT + kw * 1024 + lane * 16);
    float v[40]; float s0 = 0.f, s1 = 0.f;
#pragma unroll
    for (int j = 0; j < 20; ++j) { const unsigned w = xv[j >> 2][j & 3]; v[2 * j] = bflo(w); v[2 * j + 1] = bfhi(w); s0 += v[2 * j]; s1 += v[2 * j + 1]; }
    const float mean = row16_sum(s0 + s1) * (1.f / DM);
    float q0 = 0.f, q1 = 0.f;
#pragma unroll
    for (int j = 0; j < 20; ++j) { v[2 * j] -= mean; v[2 * j + 1] -= mean; q0 = __builtin_fmaf(v[2 * j], v[2 * j], q0); q1 = __builtin_fmaf(v[2 * j + 1], v[2 * j + 1], q1); }
    const float var = rbf(row16_sum(q0 + q1) * (1.f / DM));
    const float mb = rbf(mean), sd = rbf(__builtin_amdgcn_sqrtf(rbf(var + 1e-5f)));
    const float rsd = __builtin_amdgcn_rcpf(sd), off = (mean - mb) * rsd;
#pragma unroll
    for (int j = 0; j < 20; ++j) xv[j >> 2][j & 3] = pkbf(__builtin_fmaf(v[2 * j], rsd, off), __builtin_fmaf(v[2 * j + 1], rsd, off));
#pragma unroll
    for (int j = 0; j < 5; ++j) st16(xp + 256 * j, xv[j]);
    if (r == 0) __builtin_amdgcn_raw_buffer_store_b64(u32x2{asu(mb), asu(sd)}, rs, WS_ST + (lnb * 8 + kw * 4 + q) * 8, 0, SC1);
  } else if (wid == 4) {
    const int un = umin(cw, 191) * 2, ukb = un >> 3, unb = un & 7;
    const int c3 = (ukb * 43) >> 7, kt = ukb - 3 * c3;
    const int t0 = kt == 0 ? 0 : kt == 1 ? 80 : 32, t1 = kt == 0 ? 0 : kt == 1 ? 48 : 32;
#pragma unroll
    for (int j = 0; j < 5; ++j) { const int c = j * 64 + lane, row = (c * 6554) >> 16, pos = c - row * 10;
      const unsigned v = (c3 * 96 + row + (row < 16 ? t0 : t1)) * 1280 + unb * 160 + pos * 16;
      glds1(wd, v, lds0 + L_RTB + j * 1024); glds1((const char*)wd + 160, v, lds0 + L_RTB + 5120 + j * 1024); }
  } else {
    if (wid == 5) { __builtin_amdgcn_s_setprio(3); waitv<20>(); } else waitv<0>();
    STAMP(0);
    float pv[1][4], pn[4];
    lds_params(pv[0], lane); lds_params(pn, 64 + lane);
    unsigned tb[1] = {lds0 + pt0}, sa_ = lds0 + L_SC + (ro0 * 3 + th0) * 4, inc = wid < 3;
    float ldt[1] = {ldt0};
#pragma unroll 1
    for (int rnd = wid == 5; rnd < 2; ++rnd) {
      float sc[1];
      kpool<1>(tb, pv, ldt, false, sc);
      asm volatile("s_mov_b64 exec, 1\n\tds_write_b32 %0, %1\n\tds_add_u32 %2, %3\n\ts_mov_b64 exec, -1" :: "v"(sa_), "v"(sc[0]), "v"(fb + FL_KP * 4), "v"(inc) : "memory");
#pragma unroll
      for (int e = 0; e < 4; ++e) pv[0][e] = pn[e];
      tb[0] = lds0 + pt1; sa_ = lds0 + L_SC + (ro1 * 3 + th1) * 4; inc = 0; ldt[0] = ldt1;
    }
  }
  STAMP(1);

  if (wid >= 6) {
    const int lane = fresh_lane(), r = lane & 15, q = lane >> 4;
    const int un = umin(cw * 2 + kw, 383), ukb = un >> 3, unb = un & 7;
    const float dsk = ubf(p_dsk, chA + kw);
    u32x4 xv[5];
    u64 ep = __hip_atomic_load((GAS u64*)(ws + WS_EPOCH), RLX_AGENT);
    u32x4 wt[5], wf[12];
    if (EXP & 1) { flag_add(fb, FL_S2, lane); flag_wait(fb, FL_S2, 3); } else BAR_L();
#pragma unroll
    for (int j = 0; j < 5; ++j) xv[j] = tr2(lds + L_LNB + (r >> 2) * LNR + (kw * 5 + j) * 128 + q * 32 + (lane & 3) * 8, 4 * LNR);
#pragma unroll
    for (int j = 0; j < 5; ++j) wt[j] = tr2(lds + L_RTB + kw * 5120 + (4 * q + (r >> 2)) * 160 + j * 32 + (lane & 3) * 8, 2560);
    if (EXP & 1) BAR_L();
    {
      const int sg = (q & 1) * 32;
      lptr pa = lds + SL0 + 16384 + kw * 12288 + (8 * q + (r >> 2)) * 192 + 8 * (lane & 3);
#pragma unroll
      for (int t = 0; t < 12; ++t) wf[t] = tr2(pa + (t / 6) * 6144 + (t % 6) * 32 + ((t & 1) ? -sg : sg), 768);
    }
    lptr pool = lds + SA + kw * PCH;
    KPtr kp; kp.init(pool, lane);
    u32x4 kv[2][6], kq[4];
    kp.fetch(kv[0], 0); kp.fetch(kv[1], 64);
    BAR_L();
    tag = uni64(ep) + 1;
    *(volatile LAS u64*)(lds + L_FLG + FL_TAG * 4) = tag;
    unsigned pf_sink = 0;
    f32x4 ko[3], ka[3][3];
    u32x4 f0 = {0, 0, 0, 0}, f1 = {0, 0, 0, 0};
    constexpr int HS0[9] = {HSPLAN};
#pragma unroll
    for (int sl = 0; sl < 8; ++sl) {
      if (sl == 6 && kw == 0) { f0 = LD_SC1(WS_F0 + xcd * F0REP + lane * 16); f1 = LD_SC1(WS_F0 + xcd * F0REP + 1024 + lane * 16); SBAR(); }
      if (sl == 3 && kw == 0 && lane < 8) __hip_atomic_store((GAS u64*)(ws + WS_F0 + lane * F0REP + cw * 8), tag, RLX_AGENT);
#pragma unroll
      for (int hs = HS0[sl]; hs < HS0[sl + 1]; ++hs) {
        const int k = hs >> 1;
        if (!(hs & 1)) kprodh<0>(kv[k & 1], kq);
        else { kprodh<1>(kv[k & 1], kq); if (k + 2 < 12) kp.fetch(kv[k & 1], 64 * (k + 2)); kmma1(kq, ka[k >> 2], (k & 3) == 0); }
      }
      if (sl == 0) {
#pragma unroll
        for (int j = 0; j < 5; ++j) ST_SC1(xv[j], WS_XB + lnb * 10240 + (kw * 5 + j) * 1024 + lane * 16);
      }
      if (sl == 1) {
        if (csum) {
          const u32x4 ones = {0x3f803f80u, 0x3f803f80u, 0x3f803f80u, 0x3f803f80u};
          f32x4 cs[6];
          SBAR(); __builtin_amdgcn_s_setprio(3); SBAR();
#pragma unroll
          for (int t = 0; t < 6; ++t) cs[t] = MFMA_BF(ones, wf[t], (f32x4{0, 0, 0, 0}));
#pragma unroll
          for (int t = 0; t < 6; ++t) cs[t] = MFMA_BF(ones, wf[6 + t], cs[t]);
          SBAR(); __builtin_amdgcn_s_setprio(0); SBAR();
          const float cs0 = q == 0 ? cs[0][0] : q == 1 ? cs[1][0] : q == 2 ? cs[2][0] : cs[3][0], cs1 = q == 0 ? cs[4][0] : cs[5][0];
          const unsigned co = WS_CS + (cb * 10 + rot) * 768 + kw * 384 + lane * 4;
          __builtin_amdgcn_raw_buffer_store_b32(asu(cs0), rs, co, 0, SC1);
          if (lane < 32) __builtin_amdgcn_raw_buffer_store_b32(asu(cs1), rs, co + 256, 0, SC1);
        }
        if (cw < 192) {
#pragma unroll
          for (int f = 0; f < 5; ++f) ST_SC1(wt[f], WS_WD + (unb * 48 + ukb) * 5120 + f * 1024 + lane * 16);
        }
      }
      if (sl == 2) waitv<0>();
      if (sl == 6 && kw == 0) {
        const unsigned tl = (unsigned)tag, th = (unsigned)(tag >> 32);
        _Pragma("nounroll") for (int sp = 0; sp < SPIN_GLB; ++sp) {
          if (wall(((f0[0] ^ tl) | (f0[1] ^ th) | (f0[2] ^ tl) | (f0[3] ^ th) | (f1[0] ^ tl) | (f1[1] ^ th) | (f1[2] ^ tl) | (f1[3] ^ th)) == 0u)) break;
          f0 = LD_SC1(WS_F0 + xcd * F0REP + lane * 16); f1 = LD_SC1(WS_F0 + xcd * F0REP + 1024 + lane * 16);
          __builtin_amdgcn_s_sleep(2);
        }
      }
      if (false) {
        const unsigned po = WS_WD + nb * 245760 + (bi >> 2) * 30720 + umin(((sl - 6) * 2 + kw) * 64 + lane, 239u) * 128;
        asm volatile("global_load_dword %0, %1, %2 sc1" : "+v"(pf_sink) : "v"(po), "s"(ws) : "memory");
      }
      BAR();
    }
    STAMP(2);
#pragma unroll
    for (int hs = HS0[8]; hs < 24; ++hs) {
      const int k = hs >> 1;
      if (!(hs & 1)) kprodh<0>(kv[k & 1], kq);
      else { kprodh<1>(kv[k & 1], kq); if (k + 2 < 12) kp.fetch(kv[k & 1], 64 * (k + 2)); kmma1(kq, ka[k >> 2], (k & 3) == 0); }
    }
    kcombine(ko, ka, *(LAS float*)(lds + L_SC + kw * 12), *(LAS float*)(lds + L_SC + kw * 12 + 4), *(LAS float*)(lds + L_SC + kw * 12 + 8));
    kstore(pool + O_KL, lane, ko, dsk);
    kexpand(pool + O_KREV, pool + O_KL, lane);
    flag_set(fb, FL_KA + kw, 1);
    asm volatile("s_waitcnt vmcnt(0)" : "+v"(pf_sink) :: "memory");
    STAMP(3);
  } else {
    if (wid >= 4) {
      const int lane = fresh_lane();
      __builtin_amdgcn_s_setprio(0);
      u32x4 cv[6][6];
      {
        KPtr kp; kp.init(lds + SL2 + lw * 384, lane);
        flag_wait(fb, FL_KP, 3);
#pragma unroll
        for (int i = 0; i < 6; ++i) kp.fetch(cv[i], 64 * i);
      }
      const float sca = *(LAS float*)(lds + L_SC + 24 + lw * 4), scb = *(LAS float*)(lds + L_SC + 24 + lw * 8), scc = *(LAS float*)(lds + L_SC + 28 + lw * 4);
      SBAR();
      prep_dma(fresh_lane());
      if (!lw) { waitv<0>(); if (EXP & 1) flag_add(fb, FL_S2, lane); }
      issue(1, SL1, lw);
      waitv<20>();
      BAR_L();
      if (EXP & 1) issue(2, SL2, lw); else { issue(2, SL2, lw, 0, 1); issue(2, SL2, lw, 2, 3); }
      f32x4 ka[3][3], kc[3];
#pragma unroll
      for (int s = 0; s < 7; ++s) {
        if (s == 0 && !(EXP & 1)) waitv<8>(); else waitv<20>();
        BAR();
        if (s == 0 && !(EXP & 1)) { issue(2, SL2, lw, 1, 2); issue(2, SL2, lw, 3, 5); }
        issue(s + 3, s % 3 == 0 ? SL0 : s % 3 == 1 ? SL1 : SL2, lw);
        if (s >= 1 && !(EXP & 4096)) { u32x4 o[4]; kprod(cv[s - 1], o); kmma1(o, ka[(s - 1) >> 1], s & 1); }
      }
      kcombine(kc, ka, sca, scb, scc);
      if (lw == 0) {
#pragma unroll
        for (int i = 0; i < 3; ++i) *(LAS f32x4*)(lds + L_STG + i * 1024 + lane * 16) = kc[i];
      }
      waitv<20>();
      BAR_L();
      STAMP(2);
      tag = uni64(*(volatile LAS u64*)(lds + L_FLG + FL_TAG * 4));
      u32x4 uc[8]; unsigned uprev;
      const int n0 = lw * 128 + (lane >> 2) * 8;
      const unsigned uo = WS_XB + n0 * 10240 + (c0 + (lane & 3)) * 16;
      u32x4 sv, cp[10];
      if (lw == 0) {
        sv = LD_SC1(WS_ST + rb * 1024 + lane * 16);
#pragma unroll
        for (int j = 0; j < 10; ++j) cp[j] = LD_SC1(WS_CS + (cb * 10 + j) * 768 + (lane < 48 ? lane : 0) * 16);
      }
#pragma unroll
      for (int j = 0; j < 8; ++j) uc[j] = LD_SC1(uo + j * 10240);
      uprev = __builtin_amdgcn_raw_buffer_load_b32(rs, uo - 10240 + 12, 0, SC1);
      SBAR();
      if (lw) {
#pragma unroll
        for (int i = 0; i < 3; ++i) kc[i] += *(LAS f32x4*)(lds + L_STG + i * 1024 + lane * 16);
        kstore(lds + L_PC, lane, kc, ubf(p_dsk, chC));
        waitv<9>();
      } else waitv<20>();
      BAR();
      STAMP(3);
      if (lw == 0) {
        f32x4 cs = {0, 0, 0, 0};
#pragma unroll
        for (int j = 0; j < 10; ++j) cs += __builtin_bit_cast(f32x4, cp[j]);
        st16(lds + L_STAT + lane * 16, sv);
        if (lane < 48) *(LAS f32x4*)(lds + L_CS + lane * 16) = cs;
        flag_set(fb, FL_EPI, 1);
        if (bid == 0 && lane == 0) __hip_atomic_store((GAS u64*)(ws + WS_EPOCH), tag, RLX_AGENT);
      }
      if (n0 == 0) uprev = 0;
      if ((lane & 3) < 3) {
        lptr ud = lds + L_U + (lane & 3) * UCH + 448 + n0 * 16;
#pragma unroll
        for (int j = 0; j < 8; ++j) {
          st16(ud + j * 16, uc[j]);
          const unsigned pw_ = j ? uc[j - 1][3] : uprev;
          st16(ud + UC1 + j * 16, u32x4{__builtin_amdgcn_alignbit(uc[j][0], pw_, 16), __builtin_amdgcn_alignbit(uc[j][1], uc[j][0], 16),
                                        __builtin_amdgcn_alignbit(uc[j][2], uc[j][1], 16), __builtin_amdgcn_alignbit(uc[j][3], uc[j][2], 16)});
        }
      }
      if (lane < 28) {
#pragma unroll
        for (int c = 0; c < 3; ++c) st16(lds + L_U + c * UCH + lw * UC1 + lane * 16, u32x4{0, 0, 0, 0});
      }
      flag_add(fb, FL_U, lane);
      if (lw) { kexpand(lds + L_KC, lds + L_PC, lane); flag_set(fb, FL_KC, 1); }

    } else {
      f32x4 acc[2][3][4];
      if (!(EXP & 4)) __builtin_amdgcn_s_setprio(2);
      const int tw = wid >> 1, fw = wid & 1;
      lptr pB0 = lds + (tw * 64 + r) * 128 + ((q ^ (r >> 1)) << 4);
      lptr pB1 = lds + (tw * 64 + r) * 128 + (((q ^ (r >> 1)) ^ 4) << 4);
      const int sg = (q & 1) * 32;
      lptr pAb = lds + 16384 + (8 * q + (r >> 2)) * 192 + 8 * (lane & 3);
      lptr pA[3] = {pAb + (fw ? 160 - sg : sg), pAb + (fw ? 128 + sg : 32 - sg), pAb + (fw ? 96 - sg : 64 + sg)};
      u32x4 A0[2][3], B0[4], A1[2][3], B1[4];
      auto rd = [&](u32x4 (&A)[2][3], u32x4 (&B)[4], int off, int kk) {
#pragma unroll
        for (int tb = 0; tb < 4; ++tb) B[tb] = ld16((kk ? pB1 : pB0) + off + tb * 2048);
#pragma unroll
        for (int m = 0; m < 2; ++m)
#pragma unroll
          for (int ii = 0; ii < 3; ++ii) A[m][ii] = tr2(pA[ii] + off + m * 12288 + kk * 6144, 768);
      };
      auto mm = [&](u32x4 (&A)[2][3], u32x4 (&B)[4]) {
#pragma unroll
        for (int e = 0; e < 24; ++e) { const int m = e / 12, ii = (e / 4) % 3, tb = e % 4; acc[m][ii][tb] = MFMA_BF(A[m][ii], B[tb], acc[m][ii][tb]); }
      };
      auto sched = [&]() {
#pragma unroll
        for (int k = 0; k < 16; ++k) { SGB(0x8, 1); SGB(0x100, 1); }
        SGB(0x8, 8);
      };
      BAR_L();
      STAMP(2);
      rd(A0, B0, SL0, 0);
      SBAR();
      rd(A1, B1, SL0, 1);
#pragma unroll
      for (int e = 0; e < 24; ++e) { const int m = e / 12, ii = (e / 4) % 3, tb = e % 4; acc[m][ii][tb] = MFMA_BF(A0[m][ii], B0[tb], (f32x4{0, 0, 0, 0})); }
      sched();
      int s0 = SL1, s1 = SL2, s2 = SL0;
#pragma unroll 1
      for (int s = 0; s < 9; ++s) {
        BAR_L();
        rd(A0, B0, s0, 0); mm(A1, B1); sched();
        SBAR();
        rd(A1, B1, s0, 1); mm(A0, B0); sched();
        const int t = s0; s0 = s1; s1 = s2; s2 = t;
      }
      SBAR();
      flag_add(fb, FL_G1, lane);
      mm(A1, B1);
      SBAR();
      if (EXP & 64) __builtin_amdgcn_s_setprio(0);
      STAMP(3);
      flag_wait(fb, FL_EPI, 1);
      tag = uni64(*(volatile LAS u64*)(lds + L_FLG + FL_TAG * 4));
      STAMP(4);
      {
        float nmb[4], al[4], sd[4];
#pragma unroll
        for (int tb = 0; tb < 4; ++tb) { const f32x2 st = *(LAS f32x2*)(lds + L_STAT + (tw * 64 + tb * 16 + r) * 8); nmb[tb] = -st.x; sd[tb] = st.y; al[tb] = -1.44269504f * __builtin_amdgcn_rcpf(st.y); }
        const unsigned hoff = WS_H + ((rb * 2 + tw) * 16 + cb) * 12288 + lane * 16;
        auto hcalc = [&](const f32x4& g4, const f32x4& u4, const f32x4& cg, const f32x4& cu, int tb) {
          float hv[4];
#pragma unroll
          for (int j = 0; j < 4; ++j) {
            const float g = __builtin_fmaf(nmb[tb], cg[j], g4[j]) * al[tb];
            const float u = __builtin_fmaf(nmb[tb], cu[j], u4[j]);
            const float e = __builtin_amdgcn_exp2f(g);
            hv[j] = u * __builtin_amdgcn_rcpf(__builtin_fmaf(e, sd[tb], sd[tb]));
          }
          return u32x2{pkbf(hv[0], hv[1]), pkbf(hv[2], hv[3])};
        };
        lptr pcs = lds + L_CS + (fw * 48 + 4 * q) * 4;
        const int oa = fw ? 128 : 0, oc = fw ? 0 : 128;
        {
          const f32x4 cga = *(LAS f32x4*)(pcs + oa), cua = *(LAS f32x4*)(pcs + 384 + oa), cgb = *(LAS f32x4*)(pcs + 64), cub = *(LAS f32x4*)(pcs + 384 + 64);
#pragma unroll
          for (int tb = 0; tb < 4; ++tb) {
            const u32x2 ha = hcalc(acc[0][0][tb], acc[1][0][tb], cga, cua, tb);
            const u32x2 hb = hcalc(acc[0][1][tb], acc[1][1][tb], cgb, cub, tb);
            ST_SC1((u32x4{ha[0], ha[1], hb[0], hb[1]}), hoff + fw * 4096 + tb * 1024);
            SBAR();
          }
        }
        {
          const f32x4 cg = *(LAS f32x4*)(pcs + oc), cu = *(LAS f32x4*)(pcs + 384 + oc);
#pragma unroll
          for (int t2 = 0; t2 < 2; ++t2) {
            const u32x2 ha = hcalc(acc[0][2][2 * t2], acc[1][2][2 * t2], cg, cu, 2 * t2);
            SBAR();
            const u32x2 hb = hcalc(acc[0][2][2 * t2 + 1], acc[1][2][2 * t2 + 1], cg, cu, 2 * t2 + 1);
            SBAR();
            ST_SC1((u32x4{ha[0], ha[1], hb[0], hb[1]}), hoff + 8192 + fw * 2048 + t2 * 1024);
            SBAR();
          }
        }
      }
      if (EXP & 128) __builtin_amdgcn_s_setprio(0);
      STAMP(5);
      waitv<2>();
      if (lane < 8) __hip_atomic_store((GAS u64*)(ws + WS_MB + (rb * 16 + tw * 8 + lane) * MBS + cb * 16 + fw * 8), tag, RLX_AGENT);
    {
    const int sw = wid ^ 2, hm = sw & 1;
    lptr am = lds + SA + hm * PCH;
    f32x4 ca[8];
#pragma unroll
    for (int t = 0; t < 8; ++t) ca[t] = f32x4{0, 0, 0, 0};
    lptr pmain = am + O_KL + 1312 + lane * 16;

    flag_wait(fb, FL_KA + hm, 1);
    flag_wait(fb, FL_U, 2);
    STAMP(6);
    conv_units<0xff>(am + O_KREV, lds + L_U + (hm + odd) * UCH, lane, sw < 2 ? 5 : 0, sw < 2 ? 3 : 5, ca, [&](int it) {
      if (it == 0) { waitv<1>(); if (lane < 8) __hip_atomic_store((GAS u64*)(ws + WS_MB + (rb * 16 + tw * 8 + lane) * MBS + 256 + cb * 16 + fw * 8), tag, RLX_AGENT); } });
    if (sw < 2) {
      lptr pc = lds + L_PC;
#pragma unroll
      for (int t = 0; t < 8; ++t) { *(LAS f32x4*)(pmain + t * 1024) = ca[t]; ca[t] = f32x4{0, 0, 0, 0}; }
      flag_set(fb, FL_PA + hm, 1);
      flag_wait(fb, FL_KC, 1);
      lptr uc_ = lds + L_U + (odd ? 0 : 2) * UCH;
      if (odd) conv_units<0x3a>(lds + L_KC, uc_, lane, hm * 4, 4, ca, [](int) {});
      else conv_units<0xc5>(lds + L_KC, uc_, lane, hm * 4, 4, ca, [](int) {});
      STAMP(7);
      f32x4 c4[4];
      c4[0] = odd ? ca[1] : ca[0]; c4[1] = odd ? ca[3] : ca[2]; c4[2] = odd ? ca[4] : ca[6]; c4[3] = odd ? ca[5] : ca[7];
      if (!hm) {
#pragma unroll
        for (int t = 0; t < 4; ++t) *(LAS f32x4*)(pc + t * 1024 + lane * 16) = c4[t];
        flag_set(fb, FL_PC, 1);
      } else {
        flag_wait(fb, FL_PC, 1);
#pragma unroll
        for (int t = 0; t < 4; ++t) c4[t] += *(LAS f32x4*)(pc + t * 1024 + lane * 16);
        waitl();
#pragma unroll
        for (int t = 0; t < 4; ++t) { *(LAS unsigned*)(pc + t * 512 + q * 128 + r * 4) = pkbf(c4[t][0], c4[t][1]); *(LAS unsigned*)(pc + t * 512 + q * 128 + r * 4 + 64) = pkbf(c4[t][2], c4[t][3]); }
#pragma unroll
        for (int i = 0; i < 2; ++i) {
          const int s4 = 2 * i + (lane >> 5);
          const int T = odd ? (s4 == 0 ? 1 : s4 + 2) : (s4 < 2 ? 2 * s4 : s4 + 4);
          ST_SC1(ld16(pc + i * 1024 + lane * 16), WS_Y + ((T * 4 + ((lane >> 3) & 3)) * DM + chC) * 128 + (lane & 7) * 16);
        }
      }
    } else {
      STAMP(7);
      flag_wait(fb, FL_PA + hm, 1);
#pragma unroll
      for (int t = 0; t < 8; ++t) ca[t] += *(LAS f32x4*)(pmain + t * 1024);
#pragma unroll
      for (int t = 0; t < 8; ++t) { *(LAS unsigned*)(am + t * 512 + q * 128 + r * 4) = pkbf(ca[t][0], ca[t][1]); *(LAS unsigned*)(am + t * 512 + q * 128 + r * 4 + 64) = pkbf(ca[t][2], ca[t][3]); }
#pragma unroll
      for (int i = 0; i < 4; ++i) ST_SC1(ld16(am + i * 1024 + lane * 16), WS_Y + ((i * 8 + (lane >> 3)) * DM + chA + hm) * 128 + (lane & 7) * 16);
    }
    waitv<0>();
    if (flag_add(fb, FL_YC, lane) == 3u) {
      if (lane < 32) __hip_atomic_store((GAS u64*)(ws + WS_MB + (lane * 8 + xcd) * MBS + 512 + bi * 8), tag, RLX_AGENT);
      if (YS0) __builtin_amdgcn_s_sleep(YS0);
      _Pragma("nounroll") for (int sp = 0; sp < SPIN_GLB; ++sp) { if (wall(gflag_ok(ws, WS_MB + (tg * 8 + nb) * MBS + 512 + (lane & 31) * 8, tag))) break; __builtin_amdgcn_s_sleep(YS); }
      flag_set(fb, FL_YOK, 1);
    }

    flag_wait(fb, FL_YOK, 1);
    STAMP(8);
    u32x4 yv[3];
#pragma unroll
    for (int j = 0; j < 3; ++j) {
      const int p = umin(sw + 4 * j, 9), c = p * 64 + lane, row = (c * 6554) >> 16;
      glds1((const char*)x + (size_t)tg * 64 * 1280 + nb * 160, row * 1280 + (c - row * 10) * 16, lds0 + L_XT + p * 1024);
    }
#pragma unroll
    for (int j = 0; j < 3; ++j) { const int p = umin(sw + 4 * j, 9); yv[j] = LD_SC1(WS_Y + (tg * DM + nb * 80 + p * 8) * 128 + lane * 16); }
    lptr yb = lds + L_YB + sw * 3072;
#pragma unroll
    for (int j = 0; j < 3; ++j) st16(yb + j * 1024 + lane * 16, yv[j]);
#pragma unroll
    for (int j = 0; j < 3; ++j) {
      const u32x4 a = tr2(yb + j * 1024 + (r >> 2) * 128 + q * 32 + (lane & 3) * 8, 512);
      if (sw + 4 * j < 10) st16(lds + L_BASE + lane * 160 + (sw + 4 * j) * 16, a);
    }
    }
      __builtin_amdgcn_s_setprio(0);
    }
  }

  if (wid >= 4) {
    const int sw = wid - 4;
    const int lane = fresh_lane();
    {
      unsigned bo[4], ao[4];
#pragma unroll
      for (int c = 0; c < 4; ++c) { const int cc = sw * 4 + ((c + nb) & 3); bo[c] = WS_WD + (nb * 48 + cc * 3) * 5120; ao[c] = WS_H + (tg * 16 + cc) * 12288; }
      const unsigned l16 = lane * 16;
#define BFRAG(k, n) __builtin_amdgcn_raw_buffer_load_b128(rs, l16, bo[(k) < 8 ? (k) >> 1 : (k) - 8] + ((k) < 8 ? ((k) & 1) * 5120 : 10240) + (n) * 1024, SC1)
#define AFRAG(k, t) __builtin_amdgcn_raw_buffer_load_b128(rs, l16, ao[(k) < 8 ? (k) >> 1 : (k) - 8] + ((k) < 8 ? ((k) & 1) * 4096 : 8192) + (t) * 1024, (EXP & 8) ? 0 : SC1)
      u32x4 Bw[4][5];
#pragma unroll
      for (int k = 0; k < 4; ++k)
#pragma unroll
        for (int n = 0; n < 5; ++n) Bw[k][n] = BFRAG(k, n);
      STAMP(4);
      wd_dma(lane, sw, 0, 2);
      flag_wait(fb, FL_G1, 4);
      wd_dma(lane, sw, 2, 4);
      lptr park = lds + SL2 + sw * 10240 + (lane >> 2) * 160 + (lane & 3) * 8;
      asm volatile("" : "+v"(park));
      STAMP(7);
      const unsigned fo = WS_MB + (tg * 8 + nb) * MBS + (sw * 8 + (lane & 7)) * 8;
      _Pragma("nounroll") for (int sp = 0; sp < SPIN_GLB; ++sp) { if (wall(gflag_ok(ws, fo, tag))) break; __builtin_amdgcn_s_sleep(HS); }
      asm volatile("" ::: "memory");
      if (EXP & 1024) __builtin_amdgcn_s_sleep(127);
      STAMP(8);
      f32x4 a2[5][4];
#pragma unroll
      for (int n = 0; n < 5; ++n)
#pragma unroll
        for (int tb = 0; tb < 4; ++tb) a2[n][tb] = f32x4{0, 0, 0, 0};
      u32x4 Ah[4][4];
#pragma unroll
      for (int k = 0; k < 3; ++k)
#pragma unroll
        for (int tb = 0; tb < 4; ++tb) Ah[k][tb] = AFRAG(k, tb);
      u64 f2 = 0;
      SBAR();
#pragma unroll
      for (int k = 0; k < 12; ++k) {
        if (k == 3) { f2 = __hip_atomic_load((GAS u64*)(ws + fo + 256), RLX_AGENT); SBAR(); }
        if (k == 5) {
          unsigned tc;
          asm volatile("s_mov_b32 %1, %5\n\t1:\n\tv_cmp_ne_u64 vcc, %2, %0\n\ts_cbranch_vccz 2f\n\ts_sleep 2\n\t"
                       "global_load_dwordx2 %0, %3, %4 offset:256 sc1\n\ts_sub_u32 %1, %1, 1\n\ts_waitcnt vmcnt(0)\n\ts_cbranch_scc0 1b\n\t2:"
                       : "+v"(f2), "=&s"(tc) : "s"(tag), "v"(fo), "s"(ws), "i"(SPIN_GLB) : "memory", "vcc", "scc");
          SBAR();
        }
        u32x4 hf[4];
        if (k >= 8) {
          const u32x4 X0 = Ah[k & 3][0], X1 = Ah[k & 3][1], Y0 = Ah[k & 3][2], Y1 = Ah[k & 3][3];
          hf[0] = u32x4{X0[0], X0[1], Y0[0], Y0[1]}; hf[1] = u32x4{X0[2], X0[3], Y0[2], Y0[3]};
          hf[2] = u32x4{X1[0], X1[1], Y1[0], Y1[1]}; hf[3] = u32x4{X1[2], X1[3], Y1[2], Y1[3]};
        } else {
#pragma unroll
          for (int tb = 0; tb < 4; ++tb) hf[tb] = Ah[k & 3][tb];
        }
#pragma unroll
        for (int e = 0; e < 20; ++e) {
          const int n = e >> 2, tb = e & 3;
          a2[n][tb] = MFMA_BF(Bw[k & 3][n], hf[tb], a2[n][tb]);
          if (e & 1) {
            const int j = e >> 1;
            if (j < 4) { if (k + 3 < 12) Ah[(k + 3) & 3][j] = AFRAG(k + 3, j); }
            else if (j < 9 && k >= 1 && k + 3 < 12) {
              if (k + 3 < 8) Bw[(k + 3) & 3][j - 4] = BFRAG(k + 3, j - 4);
              else Bw[(k + 3) & 3][j - 4] = tr2(park + ((k - 5) >> 1) * (SL0 - SL2) + ((k - 5) & 1) * 5120 + (j - 4) * 32, 2560);
            }
            SBAR();
          }
        }
#pragma unroll
        for (int n = 0; n < 5; ++n) asm volatile("" : "+v"(a2[n][0]), "+v"(a2[n][1]), "+v"(a2[n][2]), "+v"(a2[n][3]));
        if ((EXP & 512) && k < 11) ARR();
      }
      STAMP(9);
      {
        lptr red = lds + SL2 + sw * 10240;
        const int l2 = fresh_lane(), r = l2 & 15, q = l2 >> 4;
#pragma unroll
        for (int n = 0; n < 5; ++n)
#pragma unroll
          for (int tb = 0; tb < 4; ++tb) {
            *(LAS f32x4*)(red + (q >> 1) * (SL0 - SL2) + (2 * n + (q & 1)) * 1024 + ((tb * 16 + r + 3 * n) & 63) * 16) = a2[n][tb];
          }
      }
    }
  }
  STAMP(10);
  BAR_L();
  STAMP(11);
  if (wid < 5) {
    const int tid = wid * 64 + fresh_lane();
    const int tok = (tid * 13108) >> 16, g = tid - tok * 5;
    lptr rp = lds + SL2 + g * 2048 + ((tok + 3 * g) & 63) * 16;
    f32x4 s[4];
#pragma unroll
    for (int m = 0; m < 4; ++m) {
      lptr p = rp + (m >> 1) * (SL0 - SL2) + (m & 1) * 1024;
      s[m] = (*(LAS f32x4*)(p) + *(LAS f32x4*)(p + 10240)) + (*(LAS f32x4*)(p + 20480) + *(LAS f32x4*)(p + 30720));
    }
    u16* op = out + (size_t)(tg * 64 + tok) * DM + nb * 80 + g * 16;
#pragma unroll
    for (int hh = 0; hh < 2; ++hh) {
      const u32x4 bs = ld16(lds + L_BASE + tid * 32 + hh * 16), xs = ld16(lds + L_XT + tid * 32 + hh * 16);
      const f32x4 sa = s[2 * hh], sb = s[2 * hh + 1];
      *(u32x4*)(op + hh * 8) = u32x4{pkbf((bflo(xs[0]) + bflo(bs[0])) + sa[0], (bfhi(xs[0]) + bfhi(bs[0])) + sa[1]), pkbf((bflo(xs[1]) + bflo(bs[1])) + sa[2], (bfhi(xs[1]) + bfhi(bs[1])) + sa[3]),
                                     pkbf((bflo(xs[2]) + bflo(bs[2])) + sb[0], (bfhi(xs[2]) + bfhi(bs[2])) + sb[1]), pkbf((bflo(xs[3]) + bflo(bs[3])) + sb[2], (bfhi(xs[3]) + bfhi(bs[3])) + sb[3])};
    }
  }
}

#if defined(__HIP_DEVICE_COMPILE__)
#pragma clang attribute pop
#endif

extern "C" void kernel_launch(void* const* d_in, const int* in_sizes, int n_in, void* d_out, int out_size, void* d_ws, size_t ws_size, hipStream_t stream) {
  (void)in_sizes; (void)n_in; (void)out_size; (void)ws_size;
  (void)hipFuncSetAttribute((const void*)fused_kernel, hipFuncAttributeMaxDynamicSharedMemorySize, LDS_BYTES);
  fused_kernel<<<256, 512, LDS_BYTES, stream>>>((const u16*)d_in[0], (const u16*)d_in[1], (const u16*)d_in[2], (const u16*)d_in[3], (const u16*)d_in[4],
                                                (const u16*)d_in[5], (const u16*)d_in[6], (const u16*)d_in[8], (const u16*)d_in[9], (const u16*)d_in[10],
                                                (u16*)d_out, (char*)d_ws);
}
```

```cpp
#include <hip/hip_runtime.h>
#include <stdint.h>

#if defined(__HIP_DEVICE_COMPILE__)
#pragma clang attribute push(__attribute__((target("no-packed-fp32-ops"))), apply_to = function)
#endif
typedef unsigned short u16;
typedef unsigned long long u64;
typedef short bf16x8 __attribute__((ext_vector_type(8)));
typedef _Float16 f16x8 __attribute__((ext_vector_type(8)));
typedef short s16x4 __attribute__((ext_vector_type(4)));
typedef float f32x2 __attribute__((ext_vector_type(2)));
typedef float f32x4 __attribute__((ext_vector_type(4)));
typedef unsigned u32x2 __attribute__((ext_vector_type(2)));
typedef unsigned u32x4 __attribute__((ext_vector_type(4)));
typedef _Float16 h16x2 __attribute__((ext_vector_type(2)));
typedef __bf16 b16x2 __attribute__((ext_vector_type(2)));
#define LAS __attribute__((address_space(3)))
#define GAS __attribute__((address_space(1)))
typedef LAS char* lptr;
#define DI __device__ __forceinline__
#define SBAR() __builtin_amdgcn_sched_barrier(0)
#define SGB(m, n) __builtin_amdgcn_sched_group_barrier(m, n, 0)
#define MFMA_BF(a, b, c) __builtin_amdgcn_mfma_f32_16x16x32_bf16(__builtin_bit_cast(bf16x8, a), __builtin_bit_cast(bf16x8, b), c, 0, 0, 0)
#define MFMA_H(a, b, c) __builtin_amdgcn_mfma_f32_16x16x32_f16(__builtin_bit_cast(f16x8, a), __builtin_bit_cast(f16x8, b), c, 0, 0, 0)
#define SC1 16

constexpr int DM = 640, NM = 192;
constexpr unsigned WS_EPOCH = 0, WS_F0 = 4096, F0REP = 4352;
constexpr unsigned WS_MB = 262144, MBS = 1280;
constexpr unsigned WS_ST = 65536, WS_CS = 98304, WS_XB = 655360, WS_WD = WS_XB + 2621440 + 4096;
constexpr unsigned WS_H = WS_WD + 1966080, WS_Y = WS_H + 6291456, WS_END = WS_Y + 2621440;
constexpr int STAGE = 40960, SL1 = 0, SA = 40960, SL2 = 81920, SL0 = 122880, POOL_BIAS = SA;
constexpr int PROW = 784, PCH = 23 * PROW;
constexpr int KC1 = 4228, O_KREV = 128, O_KL = 8528;
constexpr int L_FLG = SA + 2 * PCH, L_SC = L_FLG + 128, L_STG = L_FLG + 192, STGW = 2304;
constexpr int L_STAT = L_STG, L_CS = L_STG + 1024;
constexpr int L_RTB = SL2 + 29696;
constexpr int L_LNT = L_STG;
constexpr int L_LNB = SL2 + 18432, LNR = 1344;
constexpr int UCH = 9232, UC1 = 4640;
constexpr int L_U = SL1 + 128, L_KC = L_U + 3 * UCH + 128, L_PC = L_KC + 8400;
constexpr int L_XT = SL1;
constexpr int L_BASE = SA, L_YB = SA + 10240;
constexpr int LDS_BYTES = 163840;
static_assert(O_KL + 1312 + 8192 <= PCH && L_LNB + 8 * LNR <= L_RTB && L_RTB + 10240 + 256 <= SL0 && L_PC + 4096 <= SA && L_STG + 2 * STGW <= SL0 && (L_KC & 15) == 0 && (L_PC & 15) == 0 && (L_STG & 15) == 0, "lds map");
enum { FL_EPI = 0, FL_U, FL_KA, FL_KB, FL_KC, FL_KP, FL_PA, FL_PB, FL_PC, FL_YC, FL_YOK, FL_G1, FL_S2, FL_TAG = 16 };
constexpr int SPIN_LDS = 1 << 20, SPIN_GLB = 1 << 17;

DI bool wall(bool p) { return __builtin_amdgcn_ballot_w64(!p) == 0; }
DI unsigned umin(unsigned a, unsigned b) { return a < b ? a : b; }
DI unsigned umax(unsigned a, unsigned b) { return a > b ? a : b; }
DI float asf(unsigned v) { return __builtin_bit_cast(float, v); }
DI unsigned asu(float v) { return __builtin_bit_cast(unsigned, v); }
DI float bflo(unsigned v) { return asf(v << 16); }
DI float bfhi(unsigned v) { return asf(v & 0xffff0000u); }
DI float bf1(u16 v) { return asf((unsigned)v << 16); }
DI unsigned pkbf(float lo, float hi) { return __builtin_bit_cast(unsigned, __builtin_convertvector(f32x2{lo, hi}, b16x2)); }
DI float rbf(float x) { return bflo(pkbf(x, x)); }
template <int CTRL> DI float dppf(float x) { return __builtin_bit_cast(float, __builtin_amdgcn_mov_dpp(__builtin_bit_cast(int, x), CTRL, 0xf, 0xf, true)); }
DI float row16_sum(float v) { v += dppf<0xB1>(v); v += dppf<0x4E>(v); v += dppf<0x124>(v); v += dppf<0x128>(v); return v; }

DI void glds4(const void* sb, unsigned v0, unsigned v1, unsigned v2, unsigned v3, unsigned lds) {
  asm volatile("s_mov_b32 m0, %5\n\ts_nop 0\n\tglobal_load_lds_dwordx4 %0, %4\n\tglobal_load_lds_dwordx4 %1, %4 offset:1024\n\t"
               "global_load_lds_dwordx4 %2, %4 offset:2048\n\tglobal_load_lds_dwordx4 %3, %4 offset:3072"
               :: "v"(v0), "v"(v1), "v"(v2), "v"(v3), "s"(sb), "s"(lds) : "memory");
}
DI void glds1(const void* sb, unsigned v0, unsigned lds) {
  asm volatile("s_mov_b32 m0, %2\n\ts_nop 0\n\tglobal_load_lds_dwordx4 %0, %1" :: "v"(v0), "s"(sb), "s"(lds) : "memory");
}
template <int N> DI void waitv() { asm volatile("s_waitcnt vmcnt(%0)" :: "i"(N) : "memory"); }
DI void waitl() { asm volatile("s_waitcnt lgkmcnt(0)" ::: "memory"); }
DI u32x2 vtr(lptr p) { return __builtin_bit_cast(u32x2, __builtin_amdgcn_ds_read_tr16_b64_v4i16((LAS s16x4*)p)); }
DI u32x4 tr2(lptr p, int d) { u32x2 lo = vtr(p), hi = vtr(p + d); return u32x4{lo[0], lo[1], hi[0], hi[1]}; }
DI u32x4 ld16(lptr p) { return *(LAS u32x4*)p; }
DI void st16(lptr p, u32x4 v) { *(LAS u32x4*)p = v; }

DI unsigned flag_ld(unsigned addr) { unsigned r; asm volatile("ds_read_b32 %0, %1\n\ts_waitcnt lgkmcnt(0)" : "=v"(r) : "v"(addr) : "memory"); return __builtin_amdgcn_readfirstlane(r); }
DI void flag_wait(unsigned fb, int idx, unsigned need) { _Pragma("nounroll") for (int sp = 0; sp < SPIN_LDS && flag_ld(fb + idx * 4) < need; ++sp) __builtin_amdgcn_s_sleep(1); }
DI void flag_set(unsigned fb, int idx, unsigned v) {
  asm volatile("s_waitcnt lgkmcnt(0)\n\ts_mov_b64 exec, 1\n\tds_write_b32 %0, %1\n\ts_mov_b64 exec, -1" :: "v"(fb + idx * 4), "v"(v) : "memory"); }
DI unsigned flag_add(unsigned fb, int idx, int lane) {
  unsigned r; (void)lane;
  asm volatile("s_waitcnt lgkmcnt(0)\n\ts_mov_b64 exec, 1\n\tds_add_rtn_u32 %0, %1, %2\n\ts_mov_b64 exec, -1\n\ts_waitcnt lgkmcnt(0)" : "=&v"(r) : "v"(fb + idx * 4), "v"(1u) : "memory");
  return __builtin_amdgcn_readfirstlane(r);
}
#define RLX_AGENT __ATOMIC_RELAXED, __HIP_MEMORY_SCOPE_AGENT
DI bool gflag_ok(const char* ws, unsigned off, u64 tag) { return __hip_atomic_load((GAS u64*)(ws + off), RLX_AGENT) == tag; }

DI f32x2 csq(f32x2 x) { const float t = x.x + x.x; return f32x2{__builtin_fmaf(x.x, x.x, -(x.y * x.y)), t * x.y}; }
DI h16x2 toh(f32x2 v) { return __builtin_convertvector(v, h16x2); }
DI h16x2 hmul(h16x2 x, h16x2 y) { return __builtin_elementwise_fma(h16x2{x.y, x.y}, h16x2{-y.y, y.x}, h16x2{x.x, x.x} * y); }
DI f32x2 cexp_p(float a2, float bh, float bl, float p) {
  const float e = __builtin_amdgcn_exp2f(a2 * p);
  const float ph = __builtin_fmaf(bl, p, __builtin_amdgcn_fractf(bh * p));
  return f32x2{e * __builtin_amdgcn_cosf(ph), e * __builtin_amdgcn_sinf(ph)};
}
template <int NS, int ROW0> DI void put4(const unsigned (&tb)[NS], const h16x2 (&a)[NS], const h16x2 (&b)[NS], const h16x2 (&c)[NS], const h16x2 (&d)[NS]) {
#pragma unroll
  for (int e = 0; e < NS; ++e)
    asm volatile("s_mov_b32 m0, %4\n\ts_nop 0\n\tds_write_addtid_b32 %0 offset:%5\n\tds_write_addtid_b32 %1 offset:%5+784\n\t"
                 "ds_write_addtid_b32 %2 offset:%5+1568\n\tds_write_addtid_b32 %3 offset:%5+2352"
                 :: "v"(a[e]), "v"(b[e]), "v"(c[e]), "v"(d[e]), "s"(tb[e]), "i"(ROW0 * PROW + POOL_BIAS) : "memory");
}
#define FORS _Pragma("unroll") for (int e = 0; e < NS; ++e)
template <int NS> DI void kpool(const unsigned (&tb)[NS], const float (&pv)[NS][4], const float (&ldt)[NS], bool join, float (&sc)[NS]) {
  float dt[NS];
  FORS dt[e] = rbf(__builtin_amdgcn_exp2f(ldt[e] * 1.44269504f));
  unsigned eb[NS]; float cs[NS];
  FORS {
    unsigned mi = umax(asu(pv[e][2]) & 0x7fffffffu, asu(pv[e][3]) & 0x7fffffffu);
    mi = umax(mi, asu(dppf<0xB1>(asf(mi)))); mi = umax(mi, asu(dppf<0x4E>(asf(mi)))); mi = umax(mi, asu(dppf<0x124>(asf(mi)))); mi = umax(mi, asu(dppf<0x128>(asf(mi))));
    eb[e] = umax(umax((unsigned)__builtin_amdgcn_readlane(mi, 0), (unsigned)__builtin_amdgcn_readlane(mi, 16)),
                 umax((unsigned)__builtin_amdgcn_readlane(mi, 32), (unsigned)__builtin_amdgcn_readlane(mi, 48))) >> 23;
  }
  if (join) eb[0] = eb[NS - 1] = umax(eb[0], eb[NS - 1]);
  FORS { eb[e] = umin(umax(eb[e], 2u), 252u); cs[e] = asf((254u - eb[e]) << 23); }
  float a2[NS], bh[NS], bl[NS];
  FORS {
    const float ea = rbf(__builtin_amdgcn_exp2f(pv[e][0] * 1.44269504f));
    a2[e] = -(ea * dt[e]) * 1.44269504f;
    const float b = pv[e][1] * dt[e];
    bh[e] = b * 0.15915494f;
    bl[e] = __builtin_fmaf(b, 6.4206382e-9f, __builtin_fmaf(b, 0.15915494f, -bh[e]));
  }
  h16x2 one[NS], w8[NS], w16[NS], w24[NS], w32[NS], w64[NS], w96[NS], w1[NS], w2[NS], w3[NS], w4[NS], C[NS], c4[NS];
  FORS {
    const f32x2 f1 = cexp_p(a2[e], bh[e], bl[e], 1.f), f2 = csq(f1), f4 = csq(f2);
    const f32x2 f8 = csq(f32x2{f4.x, -f4.y}), f16 = csq(f8), f32 = csq(f16), f64 = csq(f32);
    one[e] = h16x2{(_Float16)1.f, (_Float16)0.f};
    w8[e] = toh(f8); w16[e] = toh(f16); w32[e] = toh(f32); w64[e] = toh(f64); w24[e] = hmul(w8[e], w16[e]); w96[e] = hmul(w32[e], w64[e]);
    w1[e] = toh(f1); w2[e] = toh(f2); w4[e] = toh(f4); w3[e] = hmul(w1[e], w2[e]);
    C[e] = toh(f32x2{pv[e][2] * cs[e], pv[e][3] * cs[e]}); c4[e] = hmul(C[e], w4[e]);
  }
  put4<NS, 0>(tb, one, w8, w16, w24);
  put4<NS, 4>(tb, w32, w64, w96, w1);
  put4<NS, 8>(tb, w2, w3, C, c4);
  h16x2 c128[NS], c132[NS], w256[NS], w258[NS], w512[NS], w768[NS], w1024[NS], w1280[NS], w1536[NS], w1792[NS], w1796[NS];
  FORS {
    const f32x2 f128 = cexp_p(a2[e], bh[e], bl[e], 128.f), f256 = csq(f128), f512 = csq(f256);
    const h16x2 w128 = toh(f128);
    w256[e] = toh(f256); w512[e] = toh(f512); c128[e] = hmul(C[e], w128); c132[e] = hmul(c4[e], w128);
    w258[e] = hmul(w256[e], w2[e]); w768[e] = hmul(w512[e], w256[e]);
    w1024[e] = toh(csq(f512)); w1280[e] = hmul(w1024[e], w256[e]); w1536[e] = hmul(w1024[e], w512[e]);
    w1792[e] = hmul(w1024[e], w768[e]); w1796[e] = hmul(w1792[e], w4[e]);
  }
  put4<NS, 12>(tb, c128, c132, w256, w258);
  put4<NS, 16>(tb, w512, w768, w1024, w1280);
  put4<NS, 19>(tb, w1280, w1536, w1792, w1796);
  FORS sc[e] = 2.f * dt[e] * asf(eb[e] << 23);
}
struct KPtr {
  lptr pbx, pby, py, px0, px2;
  DI void init(lptr pool, int lane) {
    const int r = lane & 15, x = r & 3, y = r >> 2;
    lptr p = pool + (lane >> 4) * 16;
    pbx = p + x * PROW; pby = p + (y ? 3 + y : 0) * PROW; py = p + (10 + y) * PROW; px0 = p + (x ? 6 + x : 0) * PROW; px2 = p + (y == 3 ? 22 : 18 + x) * PROW;
  }
  DI void fetch(u32x4 (&v)[6], int o) const {
    v[0] = ld16(pbx + o); v[1] = ld16(pby + o); v[2] = ld16(py + o); v[3] = ld16(px0 + o); v[4] = ld16(pbx + 14 * PROW + o); v[5] = ld16(px2 + o);
  }
};
DI void kprod(const u32x4 (&v)[6], u32x4 (&o)[4]) {
  unsigned t[16];
#pragma unroll
  for (int i = 0; i < 16; ++i) { const unsigned xe = v[(i >> 2) ? 2 + (i >> 2) : 0][i & 3], ye = v[(i >> 2) ? 2 : 1][i & 3];
    asm("v_pk_mul_f16 %0, %1, %2 op_sel_hi:[0,1]" : "=v"(t[i]) : "v"(xe), "v"(ye)); }
  SBAR();
#pragma unroll
  for (int i = 0; i < 16; ++i) { const unsigned xe = v[(i >> 2) ? 2 + (i >> 2) : 0][i & 3], ye = v[(i >> 2) ? 2 : 1][i & 3]; unsigned z;
    asm("v_pk_fma_f16 %0, %1, %2, %3 op_sel:[1,1,0] op_sel_hi:[1,0,1] neg_lo:[0,1,0]" : "=v"(z) : "v"(xe), "v"(ye), "v"(t[i])); o[i >> 2][i & 3] = z; }
  SBAR();
}
template <int H> DI void kprodh(const u32x4 (&v)[6], u32x4 (&o)[4]) {
  unsigned t[8];
#pragma unroll
  for (int i = 8 * H; i < 8 * H + 8; ++i) { const unsigned xe = v[(i >> 2) ? 2 + (i >> 2) : 0][i & 3], ye = v[(i >> 2) ? 2 : 1][i & 3];
    asm("v_pk_mul_f16 %0, %1, %2 op_sel_hi:[0,1]" : "=v"(t[i & 7]) : "v"(xe), "v"(ye)); }
  SBAR();
#pragma unroll
  for (int i = 8 * H; i < 8 * H + 8; ++i) { const unsigned xe = v[(i >> 2) ? 2 + (i >> 2) : 0][i & 3], ye = v[(i >> 2) ? 2 : 1][i & 3]; unsigned z;
    asm("v_pk_fma_f16 %0, %1, %2, %3 op_sel:[1,1,0] op_sel_hi:[1,0,1] neg_lo:[0,1,0]" : "=v"(z) : "v"(xe), "v"(ye), "v"(t[i & 7])); o[i >> 2][i & 3] = z; }
  asm volatile("" : "+v"(o[2 * H]), "+v"(o[2 * H + 1]));
  SBAR();
}
DI void kmma1(const u32x4 (&o0)[4], f32x4 (&acc)[3], bool first) {
  SBAR(); asm volatile("s_setprio 3\n\ts_nop 1"); SBAR();
#pragma unroll
  for (int i = 0; i < 3; ++i) acc[i] = MFMA_H(o0[1 + i], o0[0], (first ? f32x4{0, 0, 0, 0} : acc[i]));
  SBAR(); __builtin_amdgcn_s_setprio(0); SBAR();
}
DI void kcombine(f32x4 (&ko)[3], const f32x4 (&ka)[3][3], float s0, float s1, float s2) {
#pragma unroll
  for (int i = 0; i < 3; ++i)
#pragma unroll
    for (int j = 0; j < 4; ++j) ko[i][j] = __builtin_fmaf(ka[2][i][j], s2, __builtin_fmaf(ka[1][i][j], s1, ka[0][i][j] * s0));
}
DI float ubf(const u16* p, int i) { const unsigned w = ((const __attribute__((address_space(4))) unsigned*)(uintptr_t)p)[i >> 1]; return (i & 1) ? bfhi(w) : bflo(w); }
DI void kstore(lptr kl, int lane, const f32x4 (&acc)[3], float dsk) {
  const int c = lane & 15, q = lane >> 4;
  LAS u16* t = (LAS u16*)kl;
  const float k0 = acc[0][0] + (lane == 0 ? dsk : 0.f);
  *(LAS u32x2*)(t + 128 * (q >> 1) + 8 * c + 4 * (q & 1)) = u32x2{pkbf(k0, acc[0][1]), pkbf(acc[0][2], acc[0][3])};
  *(LAS unsigned*)(t + 256 + 64 * (q >> 1) + 2 * (q & 1) + 4 * c) = pkbf(acc[1][0], acc[1][1]);
  const unsigned p1 = pkbf(acc[1][2], acc[1][3]);
  const int i1 = 384 + 32 * (q >> 1) + (q & 1) + 2 * c;
  t[i1] = (u16)p1; t[i1 + 64] = (u16)(p1 >> 16);
  const unsigned r0 = pkbf(acc[2][0], acc[2][1]), r1 = pkbf(acc[2][2], acc[2][3]);
  if (!(q & 1)) { const int i2 = 512 + 8 * q + c; t[i2] = (u16)r0; t[i2 + 32] = (u16)(r0 >> 16); t[i2 + 64] = (u16)r1; t[i2 + 96] = (u16)(r1 >> 16); }
  else if (lane == 63) t[640] = (u16)r0;
}
DI unsigned dup16(unsigned a) { return a | (a << 16); }
DI unsigned bsel(unsigned m, unsigned a, unsigned b) { asm("" : "+v"(m)); return (a & m) | (b & ~m); }
DI unsigned rot16(unsigned a) { return __builtin_amdgcn_alignbit(a, a, 16); }
DI void kexpand(lptr kr, lptr kl, int lane) {
  LAS u16* t = (LAS u16*)kl;
  auto put = [&](int ch, unsigned dp, unsigned d0, unsigned d1, unsigned d2, unsigned d3) {
    st16(kr + 16 * ch, u32x4{d0, d1, d2, d3}); st16(kr + KC1 - 4 + 16 * ch, u32x4{dp, d0, d1, d2}); };
  const int n = 63 - lane, n0 = n & 31;
  const unsigned a0 = t[576 + n], b0 = t[577 + n], a1 = t[512 + n], b1 = t[513 + n];
  const unsigned ab = *(LAS unsigned*)(t + 384 + 2 * n), c2 = t[386 + 2 * n];
  const u32x2 v1 = *(LAS u32x2*)(t + 256 + 4 * n0); const unsigned q4 = t[260 + 4 * n0];
  const u32x4 v0 = ld16((lptr)(t + 8 * n0)); const unsigned w = *(LAS unsigned*)(t + 8 * n0 + 8);
  const unsigned mo = (n & 1) ? ~0u : 0u, lo = lane >= 32 ? ~0u : 0u, m31 = n == 31 ? ~0u : 0u;
  put(lane, dup16(b0), dup16(b0), bsel(mo, dup16(b0), b0 | (a0 << 16)), dup16(a0), dup16(a0));
  put(64 + lane, dup16(b1), dup16(b1), bsel(mo, dup16(b1), b1 | (a1 << 16)), dup16(a1), dup16(a1));
  put(128 + lane, dup16(c2), dup16(c2), dup16(ab >> 16), rot16(ab), dup16(ab & 0xffffu));
  put(192 + lane, bsel(lo, bsel(m31, dup16(w & 0xffffu), rot16(w)), dup16(q4)), bsel(lo, rot16(v0[3]), q4 | (v1[1] & 0xffff0000u)),
      bsel(lo, rot16(v0[2]), dup16(v1[1] & 0xffffu)), bsel(lo, rot16(v0[1]), (v1[1] & 0xffffu) | (v1[0] & 0xffff0000u)), bsel(lo, rot16(v0[0]), dup16(v1[0] & 0xffffu)));
  if (lane >= 60) put(319 - lane, lane == 63 ? rot16(v0[0]) : 0u, 0u, 0u, 0u, 0u);
}

struct ConvPlan { int n; int item[36][3]; };
constexpr ConvPlan conv_plan(int tmask, int maxt) {
  ConvPlan p{};
  int fifo[64] = {}, head = 0, tail = 0, carry[16] = {}, nc = 0, per = (tmask == 0xff) ? 1 : 2;
  for (int pass = 0; pass < 2; ++pass) {
    head = tail = 0;
    for (int i = 0; i < nc; ++i) fifo[tail++] = carry[i] + 32;
    int e = 0;
    for (int m = maxt; m >= 0; --m) {
      for (int j = 0; j + m <= maxt; ++j) {
        const bool on = (tmask >> (m + j)) & 1;
        int k = 0;
        if (m == 0) { if (on && j < maxt - 1) p.item[e][k++] = j; else fifo[tail++] = j; }
        if (j + m == maxt) { fifo[tail++] = 8 + 2 * m; fifo[tail++] = 9 + 2 * m; }
        if (on) { while (k < per && head < tail) p.item[e][k++] = fifo[head++]; while (k < 3) p.item[e][k++] = -1; ++e; }
      }
    }
    p.n = e; nc = 0;
    while (head < tail) carry[nc++] = fifo[head++];
  }
  return p;
}
template <int TMASK, class F>
DI void conv_units(lptr kr, lptr ub, int lane, int rho0, int nrho, f32x4 (&acc)[8], F hook) {
  constexpr int MAXT = (TMASK & 0x80) ? 7 : 5;
  constexpr ConvPlan plan = conv_plan(TMASK, MAXT);
  const int r = lane & 15, q = lane >> 4;
  lptr pk = kr + ((r & 1) ? 0 : KC1) + 4092 - 4 * r + 16 * q - 3584 - 64 * rho0 - 64;
  lptr pu = ub + ((r & 1) ? 0 : UC1) + 64 * (r >> 1) + 16 * q + 64 * (7 - rho0) - 64;
  lptr pk2 = pk + 8;
  u32x2 Kl[8], Kh[8]; u32x4 Uf[8];
  asm volatile("" : "+v"(pk), "+v"(pk2), "+v"(pu));
#pragma unroll
  for (int j = 0; j <= MAXT; ++j) { const int m = MAXT - j; Uf[j] = ld16(pu + 64 + 512 * j); Kl[m] = *(LAS u32x2*)(pk + 64 + 512 * (7 - m)); Kh[m] = *(LAS u32x2*)(pk2 + 64 + 512 * (7 - m)); SBAR(); }
#pragma unroll 1
  for (int it = 0; it < nrho; ++it) {
    int e = 0;
#pragma unroll
    for (int m = MAXT; m >= 0; --m) {
#pragma unroll
      for (int j = 0; j + m <= MAXT; ++j) {
        if ((TMASK >> (m + j)) & 1) {
          acc[m + j] = MFMA_BF(Uf[j], (u32x4{Kl[m][0], Kl[m][1], Kh[m][0], Kh[m][1]}), acc[m + j]);
#pragma unroll
          for (int k = 0; k < 3; ++k) {
            const int c = plan.item[e][k];
            if (c >= 0) {
              const int o = (c & 32) ? 64 : 0, cc = c & 31, mm = (cc - 8) >> 1;
              if (cc < 8) Uf[cc] = ld16(pu + o + 512 * cc);
              else if (cc & 1) Kh[mm] = *(LAS u32x2*)(pk2 + o + 512 * (7 - mm));
              else Kl[mm] = *(LAS u32x2*)(pk + o + 512 * (7 - mm));
            }
          }
          SBAR();
          ++e;
        }
      }
    }
    pk -= 64; pk2 -= 64; pu -= 64;
    asm volatile("" : "+v"(pk), "+v"(pk2), "+v"(pu));
    hook(it);
  }
}

DI u64 uni64(u64 v) { return (u64)(unsigned)__builtin_amdgcn_readfirstlane((unsigned)v) | ((u64)(unsigned)__builtin_amdgcn_readfirstlane((unsigned)(v >> 32)) << 32); }
DI int fresh_lane() { int l; asm volatile("v_mbcnt_lo_u32_b32 %0, -1, 0\n\tv_mbcnt_hi_u32_b32 %0, -1, %0" : "=v"(l)); return l; }
#define BAR() do { SBAR(); asm volatile("s_barrier" ::: "memory"); SBAR(); } while (0)
#define BAR_L() do { SBAR(); asm volatile("s_waitcnt lgkmcnt(0)\n\ts_barrier" ::: "memory"); SBAR(); } while (0)
#define LD_SC1(off) __builtin_amdgcn_raw_buffer_load_b128(rs, (off), 0, SC1)
#define ST_SC1(v, off) __builtin_amdgcn_raw_buffer_store_b128((v), rs, (off), 0, SC1)

__global__ __launch_bounds__(512, 2) void fused_kernel(const u16* __restrict__ x, const u16* __restrict__ p_lar, const u16* __restrict__ p_aim,
                                                       const u16* __restrict__ p_cr, const u16* __restrict__ p_ci, const u16* __restrict__ p_ldt,
                                                       const u16* __restrict__ p_dsk, const u16* __restrict__ wg, const u16* __restrict__ wu,
                                                       const u16* __restrict__ wd, u16* __restrict__ out, char* ws) {
  extern __shared__ __attribute__((aligned(16))) char smem[];
  const int tid = __builtin_amdgcn_workitem_id_x(), bid = __builtin_amdgcn_workgroup_id_x();
  if (__builtin_amdgcn_readfirstlane(tid >> 6) == 6) {
    u64 pc_; unsigned len_;
    asm volatile("s_getpc_b64 %0\n.Lfk_head:\n\ts_mov_b32 %1, .Lfk_tail-.Lfk_head" : "=s"(pc_), "=s"(len_));
    asm volatile("s_mov_b32 m0, %2\n\ts_mov_b32 exec_hi, 0xff\n\tglobal_load_lds_dword %0, %1\n\ts_mov_b64 exec, -1"
                 :: "v"(umin((((bid >> 3) & 7) * 40 + (tid & 63)) * 128, len_)), "s"(pc_), "s"((unsigned)(uintptr_t)smem + L_RTB + 10240) : "memory");
  }
  lptr lds = (lptr)smem;
  const unsigned lds0 = (unsigned)(uintptr_t)smem;
  const unsigned fb = lds0 + L_FLG;
  const int wraw = __builtin_amdgcn_readfirstlane(tid >> 6), lane = tid & 63;
  const int wid = wraw ^ ((wraw >> 2) << 1);
  const int xcd = bid & 7, bi = bid >> 3;
  const int rb = (xcd >> 2) * 8 + (bi >> 2), cb = (xcd & 3) * 4 + (bi & 3);
  const int tg = (xcd >> 1) * 8 + (bi >> 2), nb = (xcd & 1) * 4 + (bi & 3);
  const int cw = xcd * 32 + bi, odd = cw & 1, c0 = 5 * (cw >> 1) + 2 * odd;
  const int chA = c0 + odd, chC = odd ? c0 : c0 + 2;
  const int rm = rb & 7, rot = 2 * (rm >= 5 ? rm - 5 : rm) + (rb >> 3);
  const bool csum = rm < 5;
  const __amdgpu_buffer_rsrc_t rs = __builtin_amdgcn_make_buffer_rsrc(ws, 0, (int)WS_END, 0x00020000);
  const int r = lane & 15, q = lane >> 4;

  u64 tag = 0;
  const int lw = wid - 4, kw = wid - 6;
  unsigned vx[4], vw[12];
  auto issue = [&](int s, int sbase, int h) {
    int ks = s + rot; if (ks >= 10) ks -= 10;
    const unsigned base = lds0 + sbase;
    const char* sx = (const char*)x + (size_t)(rb * 128 + h * 64) * 1280 - 4096 + ks * 128;
    const char* sw = (const char*)(h ? wu : wg) + cb * 192 - 4096 + (size_t)ks * 64 * 3072;
    glds4(sx, vx[0], vx[1], vx[2], vx[3], base + h * 8192);
    glds4(sw, vw[0], vw[1], vw[2], vw[3], base + 16384 + h * 12288);
    glds4(sx + 32 * 1280, vx[0], vx[1], vx[2], vx[3], base + h * 8192 + 4096);
    glds4(sw, vw[4], vw[5], vw[6], vw[7], base + 16384 + h * 12288 + 4096);
    glds4(sw, vw[8], vw[9], vw[10], vw[11], base + 16384 + h * 12288 + 8192);
  };
  auto prep_dma = [&](int ln) {
#pragma unroll
    for (int j = 0; j < 4; ++j) { const int row = 8 * j + (ln >> 3); vx[j] = row * 1280 + (((ln & 7) ^ ((row >> 1) & 7)) << 4) + 4096 - 1024 * j; }
#pragma unroll
    for (int j = 0; j < 3; ++j) { const int c = j * 64 + ln, row = (c * 43691) >> 19, pos = c - row * 12;
      const unsigned v = row * 3072 + ((pos ^ (((row >> 3) & 1) << 1)) << 4) + 4096;
#pragma unroll
      for (int g = 0; g < 4; ++g) vw[3 * g + j] = v + g * 16 * 3072 - 1024 * ((3 * g + j) & 3); }
  };
  auto wd_dma = [&](int ln, int w, int j0, int j1) {
    unsigned v[5];
#pragma unroll
    for (int i = 0; i < 5; ++i) { const int c = i * 64 + ln, row = (c * 6554) >> 16; v[i] = row * 1280 + (c - row * 10) * 16 + 4096 - (i & 3) * 1024; }
#pragma unroll
    for (int j = j0; j < j1; ++j) {
      const char* sp = (const char*)wd + (size_t)((w * 4 + ((j + nb) & 3)) * 96 + 32) * 1280 + nb * 160 - 4096;
      const unsigned d = lds0 + ((j >> 1) ? SL0 : SL2) + w * 10240 + (j & 1) * 5120;
      glds4(sp, v[0], v[1], v[2], v[3], d); glds1(sp, v[4], d + 4096);
    }
  };
  const int lnb = cw;
  const int ro0 = (0x100222 >> (4 * wid)) & 3, th0 = (0x000210 >> (4 * wid)) & 3, ro1 = (wid >> 1) & 1, th1 = 1 + (wid & 1);
  const int ch0 = c0 + ((0x0210 >> (4 * (ro0 + odd))) & 3), ch1 = chA + ro1;
  const int pt0 = ro0 * PCH + (ro0 >> 1) * (SL2 - SA - 2 * PCH) + th0 * 256, pt1 = ro1 * PCH + th1 * 256;
  unsigned o = (lane < 32 ? ch0 * 384 + th0 * 128 : ch1 * 384 + th1 * 128) + (lane & 31) * 4;
  unsigned xo0 = lane * 16, xo1 = q * 1280 + r * 16 + 1024;
  asm volatile("" : "+v"(o), "+v"(xo0), "+v"(xo1));
  asm volatile("" :: "s"(x), "s"(p_lar), "s"(p_aim), "s"(p_cr), "s"(p_ci), "s"(p_ldt), "s"(p_dsk), "s"(wg), "s"(wu), "s"(wd), "s"(out), "s"(ws));
  const int ptp = wid == 5 ? pt0 : pt1;
  if ((0x2f >> wid) & 1) {
    asm volatile("s_mov_b32 m0, %5\n\ts_nop 0\n\tglobal_load_lds_dword %0, %1\n\tglobal_load_lds_dword %0, %2 offset:784\n\t"
                 "global_load_lds_dword %0, %3 offset:1568\n\tglobal_load_lds_dword %0, %4 offset:2352"
                 :: "v"(o), "s"(p_lar), "s"((const char*)p_aim - 784), "s"((const char*)p_cr - 1568), "s"((const char*)p_ci - 2352), "s"(lds0 + SA + ptp) : "memory");
  }
  if (wid >= 6) {
    const char* xr = (const char*)x + (size_t)(lnb * 8 + kw * 4) * 1280;
#pragma unroll
    for (int j = 0; j < 4; ++j) glds1(xr + j * 1280, xo0, lds0 + L_LNB + (kw * 4 + j) * LNR);
    glds1(xr, xo1, lds0 + L_LNT + kw * 1024);
  } else if (wid >= 4) { if (lane < 16) *(LAS unsigned*)(lds + L_FLG + lane * 4) = 0; prep_dma(lane); }
  SBAR(); asm volatile("s_waitcnt lgkmcnt(0)\n\ts_barrier" ::: "memory"); SBAR();
  const float ldt0 = ubf(p_ldt, ch0), ldt1 = ubf(p_ldt, ch1);
  if (wid == 4 || wid == 5) {
    issue(0, SL0, lw);
  }
  auto lds_params = [&](float (&pv)[4], int m) {
#pragma unroll
    for (int e = 0; e < 4; ++e) pv[e] = bf1(*(LAS u16*)(lds + SA + ptp + e * PROW + m * 2));
  };
  if (wid >= 6) {
    waitv<0>();
    lptr xp = lds + L_LNB + (kw * 4 + q) * LNR + r * 16;
    u32x4 xv[5];
#pragma unroll
    for (int j = 0; j < 4; ++j) xv[j] = ld16(xp + 256 * j);
    xv[4] = ld16(lds + L_LNT + kw * 1024 + lane * 16);
    float v[40]; float s0 = 0.f, s1 = 0.f;
#pragma unroll
    for (int j = 0; j < 20; ++j) { const unsigned w = xv[j >> 2][j & 3]; v[2 * j] = bflo(w); v[2 * j + 1] = bfhi(w); s0 += v[2 * j]; s1 += v[2 * j + 1]; }
    const float mean = row16_sum(s0 + s1) * (1.f / DM);
    float q0 = 0.f, q1 = 0.f;
#pragma unroll
    for (int j = 0; j < 20; ++j) { v[2 * j] -= mean; v[2 * j + 1] -= mean; q0 = __builtin_fmaf(v[2 * j], v[2 * j], q0); q1 = __builtin_fmaf(v[2 * j + 1], v[2 * j + 1], q1); }
    const float var = rbf(row16_sum(q0 + q1) * (1.f / DM));
    const float mb = rbf(mean), sd = rbf(__builtin_amdgcn_sqrtf(rbf(var + 1e-5f)));
    const float rsd = __builtin_amdgcn_rcpf(sd), off = (mean - mb) * rsd;
#pragma unroll
    for (int j = 0; j < 20; ++j) xv[j >> 2][j & 3] = pkbf(__builtin_fmaf(v[2 * j], rsd, off), __builtin_fmaf(v[2 * j + 1], rsd, off));
#pragma unroll
    for (int j = 0; j < 5; ++j) st16(xp + 256 * j, xv[j]);
    if (r == 0) __builtin_amdgcn_raw_buffer_store_b64(u32x2{asu(mb), asu(sd)}, rs, WS_ST + (lnb * 8 + kw * 4 + q) * 8, 0, SC1);
  } else if (wid == 4) {
    const int c3 = cw >> 4, kt = (cw >> 3) & 1, unb = cw & 7;
#pragma unroll
    for (int j = 0; j < 5; ++j) { const int c = j * 64 + lane, row = (c * 6554) >> 16, pos = c - row * 10;
      glds1(wd, (c3 * 96 + row + kt * (row < 16 ? 80 : 48)) * 1280 + unb * 160 + pos * 16, lds0 + L_RTB + j * 1024); }
    glds1(wd, ((cw >> 4) * 96 + 32 + (cw & 15) * 2) * 1280 + lane * 16, lds0 + L_RTB + 5120); glds1(wd, ((cw >> 4) * 96 + 32 + (cw & 15) * 2) * 1280 + 1024 + lane * 16, lds0 + L_RTB + 6144);
    glds1(wd, ((cw >> 4) * 96 + 32 + (cw & 15) * 2) * 1280 + 2048 + (lane & 31) * 16, lds0 + L_RTB + 7168);
  } else {
    if (wid == 5) { __builtin_amdgcn_s_setprio(3); waitv<20>(); } else waitv<0>();
    float pv[1][4], pn[4];
    lds_params(pv[0], lane); lds_params(pn, 64 + lane);
    unsigned tb[1] = {lds0 + pt0}, sa_ = lds0 + L_SC + (ro0 * 3 + th0) * 4, inc = wid < 3;
    float ldt[1] = {ldt0};
#pragma unroll 1
    for (int rnd = wid == 5; rnd < 2; ++rnd) {
      float sc[1];
      kpool<1>(tb, pv, ldt, false, sc);
      asm volatile("s_mov_b64 exec, 1\n\tds_write_b32 %0, %1\n\tds_add_u32 %2, %3\n\ts_mov_b64 exec, -1" :: "v"(sa_), "v"(sc[0]), "v"(fb + FL_KP * 4), "v"(inc) : "memory");
#pragma unroll
      for (int e = 0; e < 4; ++e) pv[0][e] = pn[e];
      tb[0] = lds0 + pt1; sa_ = lds0 + L_SC + (ro1 * 3 + th1) * 4; inc = 0; ldt[0] = ldt1;
    }
  }

  if (wid >= 6) {
    const int lane = fresh_lane(), r = lane & 15, q = lane >> 4;
    const float dsk = ubf(p_dsk, chA + kw);
    u32x4 xv[5];
    u64 ep = __hip_atomic_load((GAS u64*)(ws + WS_EPOCH), RLX_AGENT);
    u32x4 wt[3], wf[12];
    flag_add(fb, FL_S2, lane);
    flag_wait(fb, FL_S2, 3);
#pragma unroll
    for (int j = 0; j < 5; ++j) xv[j] = tr2(lds + L_LNB + (r >> 2) * LNR + (kw * 5 + j) * 128 + q * 32 + (lane & 3) * 8, 4 * LNR);
#pragma unroll
    for (int j = 0; j < 3; ++j) wt[j] = tr2(lds + L_RTB + (4 * q + (r >> 2)) * 160 + (kw * 2 + j) * 32 + (lane & 3) * 8, 2560);
    BAR_L();
    {
      const int sg = (q & 1) * 32;
      lptr pa = lds + SL0 + 16384 + kw * 12288 + (8 * q + (r >> 2)) * 192 + 8 * (lane & 3);
#pragma unroll
      for (int t = 0; t < 12; ++t) wf[t] = tr2(pa + (t / 6) * 6144 + (t % 6) * 32 + ((t & 1) ? -sg : sg), 768);
    }
    lptr pool = lds + SA + kw * PCH;
    KPtr kp; kp.init(pool, lane);
    u32x4 kv[2][6], kq[4];
    kp.fetch(kv[0], 0); kp.fetch(kv[1], 64);
    BAR_L();
    tag = uni64(ep) + 1;
    *(volatile LAS u64*)(lds + L_FLG + FL_TAG * 4) = tag;
    f32x4 ko[3], ka[3][3];
    u32x4 f0 = {0, 0, 0, 0}, f1 = {0, 0, 0, 0};
    constexpr int HS0[9] = {0, 2, 3, 7, 10, 14, 17, 20, 24};
#pragma unroll
    for (int sl = 0; sl < 8; ++sl) {
      if (sl == 6 && kw == 0) { f0 = LD_SC1(WS_F0 + xcd * F0REP + lane * 16); f1 = LD_SC1(WS_F0 + xcd * F0REP + 1024 + lane * 16); SBAR(); }
      if (sl == 3 && kw == 0 && lane < 8) __hip_atomic_store((GAS u64*)(ws + WS_F0 + lane * F0REP + cw * 8), tag, RLX_AGENT);
#pragma unroll
      for (int hs = HS0[sl]; hs < HS0[sl + 1]; ++hs) {
        const int k = hs >> 1;
        if (!(hs & 1)) kprodh<0>(kv[k & 1], kq);
        else { kprodh<1>(kv[k & 1], kq); if (k + 2 < 12) kp.fetch(kv[k & 1], 64 * (k + 2)); kmma1(kq, ka[k >> 2], (k & 3) == 0); }
      }
      if (sl == 0) {
#pragma unroll
        for (int j = 0; j < 5; ++j) ST_SC1(xv[j], WS_XB + lnb * 10240 + (kw * 5 + j) * 1024 + lane * 16);
      }
      if (sl == 1) {
        if (csum) {
          const u32x4 ones = {0x3f803f80u, 0x3f803f80u, 0x3f803f80u, 0x3f803f80u};
          f32x4 cs[6];
          SBAR(); __builtin_amdgcn_s_setprio(3); SBAR();
#pragma unroll
          for (int t = 0; t < 6; ++t) cs[t] = MFMA_BF(ones, wf[t], (f32x4{0, 0, 0, 0}));
#pragma unroll
          for (int t = 0; t < 6; ++t) cs[t] = MFMA_BF(ones, wf[6 + t], cs[t]);
          SBAR(); __builtin_amdgcn_s_setprio(0); SBAR();
          const float cs0 = q == 0 ? cs[0][0] : q == 1 ? cs[1][0] : q == 2 ? cs[2][0] : cs[3][0], cs1 = q == 0 ? cs[4][0] : cs[5][0];
          const unsigned co = WS_CS + (cb * 10 + rot) * 768 + kw * 384 + lane * 4;
          __builtin_amdgcn_raw_buffer_store_b32(asu(cs0), rs, co, 0, SC1);
          if (lane < 32) __builtin_amdgcn_raw_buffer_store_b32(asu(cs1), rs, co + 256, 0, SC1);
        }
#pragma unroll
        for (int f = 0; f < 3; ++f) if (f || !kw) ST_SC1(wt[f], WS_WD + ((cw & 7) * 48 + (cw >> 4) * 3 + ((cw >> 3) & 1)) * 5120 + (kw * 2 + f) * 1024 + lane * 16);
      }
      if (sl == 2) waitv<0>();
      if (sl == 6 && kw == 0) {
        const unsigned tl = (unsigned)tag, th = (unsigned)(tag >> 32);
        _Pragma("nounroll") for (int sp = 0; sp < SPIN_GLB; ++sp) {
          if (wall(((f0[0] ^ tl) | (f0[1] ^ th) | (f0[2] ^ tl) | (f0[3] ^ th) | (f1[0] ^ tl) | (f1[1] ^ th) | (f1[2] ^ tl) | (f1[3] ^ th)) == 0u)) break;
          asm volatile("" ::: "memory");
          f0 = LD_SC1(WS_F0 + xcd * F0REP + lane * 16); f1 = LD_SC1(WS_F0 + xcd * F0REP + 1024 + lane * 16);
          __builtin_amdgcn_s_sleep(2);
        }
      }
      BAR();
    }
    kcombine(ko, ka, *(LAS float*)(lds + L_SC + kw * 12), *(LAS float*)(lds + L_SC + kw * 12 + 4), *(LAS float*)(lds + L_SC + kw * 12 + 8));
    kstore(pool + O_KL, lane, ko, dsk);
    kexpand(pool + O_KREV, pool + O_KL, lane);
    flag_set(fb, FL_KA + kw, 1);
  } else {
    if (wid >= 4) {
      const int lane = fresh_lane();
      __builtin_amdgcn_s_setprio(0);
      u32x4 cv[6][6];
      {
        KPtr kp; kp.init(lds + SL2 + lw * 384, lane);
        flag_wait(fb, FL_KP, 3);
#pragma unroll
        for (int i = 0; i < 6; ++i) kp.fetch(cv[i], 64 * i);
      }
      const float sca = *(LAS float*)(lds + L_SC + 24 + lw * 4), scb = *(LAS float*)(lds + L_SC + 24 + lw * 8), scc = *(LAS float*)(lds + L_SC + 28 + lw * 4);
      SBAR();
      prep_dma(fresh_lane());
      if (!lw) { waitv<0>(); flag_add(fb, FL_S2, lane); }
      issue(1, SL1, lw);
      waitv<20>();
      BAR_L();
      issue(2, SL2, lw);
      f32x4 ka[3][3], kc[3];
#pragma unroll
      for (int s = 0; s < 7; ++s) {
        waitv<20>();
        BAR();
        issue(s + 3, s % 3 == 0 ? SL0 : s % 3 == 1 ? SL1 : SL2, lw);
        if (s >= 1) { u32x4 o[4]; kprod(cv[s - 1], o); kmma1(o, ka[(s - 1) >> 1], s & 1); }
      }
      kcombine(kc, ka, sca, scb, scc);
      if (lw == 0) {
#pragma unroll
        for (int i = 0; i < 3; ++i) *(LAS f32x4*)(lds + L_STG + i * 1024 + lane * 16) = kc[i];
      }
      waitv<20>();
      BAR_L();
      tag = uni64(*(volatile LAS u64*)(lds + L_FLG + FL_TAG * 4));
      u32x4 uc[8]; unsigned uprev;
      const int n0 = lw * 128 + (lane >> 2) * 8;
      const unsigned uo = WS_XB + n0 * 10240 + (c0 + (lane & 3)) * 16;
      u32x4 sv, cp[10];
      if (lw == 0) {
        sv = LD_SC1(WS_ST + rb * 1024 + lane * 16);
#pragma unroll
        for (int j = 0; j < 10; ++j) cp[j] = LD_SC1(WS_CS + (cb * 10 + j) * 768 + (lane < 48 ? lane : 0) * 16);
      }
#pragma unroll
      for (int j = 0; j < 8; ++j) uc[j] = LD_SC1(uo + j * 10240);
      uprev = __builtin_amdgcn_raw_buffer_load_b32(rs, uo - 10240 + 12, 0, SC1);
      SBAR();
      if (lw) {
#pragma unroll
        for (int i = 0; i < 3; ++i) kc[i] += *(LAS f32x4*)(lds + L_STG + i * 1024 + lane * 16);
        kstore(lds + L_PC, lane, kc, ubf(p_dsk, chC));
        waitv<9>();
      } else waitv<20>();
      BAR();
      if (lw == 0) {
        f32x4 cs = {0, 0, 0, 0};
#pragma unroll
        for (int j = 0; j < 10; ++j) cs += __builtin_bit_cast(f32x4, cp[j]);
        st16(lds + L_STAT + lane * 16, sv);
        if (lane < 48) *(LAS f32x4*)(lds + L_CS + lane * 16) = cs;
        flag_set(fb, FL_EPI, 1);
        if (bid == 0 && lane == 0) __hip_atomic_store((GAS u64*)(ws + WS_EPOCH), tag, RLX_AGENT);
      }
      if (n0 == 0) uprev = 0;
      if ((lane & 3) < 3) {
        lptr ud = lds + L_U + (lane & 3) * UCH + 448 + n0 * 16;
#pragma unroll
        for (int j = 0; j < 8; ++j) {
          st16(ud + j * 16, uc[j]);
          const unsigned pw_ = j ? uc[j - 1][3] : uprev;
          st16(ud + UC1 + j * 16, u32x4{__builtin_amdgcn_alignbit(uc[j][0], pw_, 16), __builtin_amdgcn_alignbit(uc[j][1], uc[j][0], 16),
                                        __builtin_amdgcn_alignbit(uc[j][2], uc[j][1], 16), __builtin_amdgcn_alignbit(uc[j][3], uc[j][2], 16)});
        }
      }
      if (lane < 28) {
#pragma unroll
        for (int c = 0; c < 3; ++c) st16(lds + L_U + c * UCH + lw * UC1 + lane * 16, u32x4{0, 0, 0, 0});
      }
      flag_add(fb, FL_U, lane);
      if (lw) { kexpand(lds + L_KC, lds + L_PC, lane); flag_set(fb, FL_KC, 1); }

    } else {
      f32x4 acc[2][3][4];
      __builtin_amdgcn_s_setprio(2);
      const int tw = wid >> 1, fw = wid & 1;
      lptr pB0 = lds + (tw * 64 + r) * 128 + ((q ^ (r >> 1)) << 4);
      lptr pB1 = lds + (tw * 64 + r) * 128 + (((q ^ (r >> 1)) ^ 4) << 4);
      const int sg = (q & 1) * 32;
      lptr pAb = lds + 16384 + (8 * q + (r >> 2)) * 192 + 8 * (lane & 3);
      lptr pA[3] = {pAb + (fw ? 160 - sg : sg), pAb + (fw ? 128 + sg : 32 - sg), pAb + (fw ? 96 - sg : 64 + sg)};
      u32x4 A0[2][3], B0[4], A1[2][3], B1[4];
      auto rd = [&](u32x4 (&A)[2][3], u32x4 (&B)[4], int off, int kk) {
#pragma unroll
        for (int tb = 0; tb < 4; ++tb) B[tb] = ld16((kk ? pB1 : pB0) + off + tb * 2048);
#pragma unroll
        for (int m = 0; m < 2; ++m)
#pragma unroll
          for (int ii = 0; ii < 3; ++ii) A[m][ii] = tr2(pA[ii] + off + m * 12288 + kk * 6144, 768);
      };
      auto mm = [&](u32x4 (&A)[2][3], u32x4 (&B)[4]) {
#pragma unroll
        for (int e = 0; e < 24; ++e) { const int m = e / 12, ii = (e / 4) % 3, tb = e % 4; acc[m][ii][tb] = MFMA_BF(A[m][ii], B[tb], acc[m][ii][tb]); }
      };
      auto sched = [&]() {
#pragma unroll
        for (int k = 0; k < 16; ++k) { SGB(0x8, 1); SGB(0x100, 1); }
        SGB(0x8, 8);
      };
      BAR_L();
      rd(A0, B0, SL0, 0);
      SBAR();
      rd(A1, B1, SL0, 1);
#pragma unroll
      for (int e = 0; e < 24; ++e) { const int m = e / 12, ii = (e / 4) % 3, tb = e % 4; acc[m][ii][tb] = MFMA_BF(A0[m][ii], B0[tb], (f32x4{0, 0, 0, 0})); }
      sched();
      int s0 = SL1, s1 = SL2, s2 = SL0;
#pragma unroll 1
      for (int s = 0; s < 9; ++s) {
        BAR_L();
        rd(A0, B0, s0, 0); mm(A1, B1); sched();
        SBAR();
        rd(A1, B1, s0, 1); mm(A0, B0); sched();
        const int t = s0; s0 = s1; s1 = s2; s2 = t;
      }
      SBAR();
      asm volatile("s_waitcnt lgkmcnt(0)\n\ts_mov_b64 exec, 1\n\tds_add_u32 %0, %1\n\ts_mov_b64 exec, -1" :: "v"(fb + FL_G1 * 4), "v"(1u) : "memory");
      auto mlast = [&](int ii, int tb) { acc[0][ii][tb] = MFMA_BF(A1[0][ii], B1[tb], acc[0][ii][tb]); acc[1][ii][tb] = MFMA_BF(A1[1][ii], B1[tb], acc[1][ii][tb]); };
      mlast(0, 0); mlast(1, 0); mlast(0, 1); mlast(1, 1);
      SBAR();
      flag_wait(fb, FL_EPI, 1);
      {
        f32x2 st[4];
#pragma unroll
        for (int tb = 0; tb < 4; ++tb) st[tb] = *(LAS f32x2*)(lds + L_STAT + (tw * 64 + tb * 16 + r) * 8);
        lptr pcs = lds + L_CS + (fw * 48 + 4 * q) * 4;
        const int oa = fw ? 128 : 0, oc = fw ? 0 : 128;
        const f32x4 cga = *(LAS f32x4*)(pcs + oa), cua = *(LAS f32x4*)(pcs + 384 + oa), cgb = *(LAS f32x4*)(pcs + 64), cub = *(LAS f32x4*)(pcs + 384 + 64);
        const u64 tv = *(volatile LAS u64*)(lds + L_FLG + FL_TAG * 4);
        SBAR();
        mlast(0, 2); mlast(1, 2); mlast(0, 3); mlast(1, 3);
        SBAR();
        tag = uni64(tv);
        float nmb[4], al[4], sd[4];
#pragma unroll
        for (int tb = 0; tb < 4; ++tb) { nmb[tb] = -st[tb].x; sd[tb] = st[tb].y; al[tb] = -1.44269504f * __builtin_amdgcn_rcpf(st[tb].y); }
        const unsigned hoff = WS_H + ((rb * 2 + tw) * 16 + cb) * 12288 + lane * 16;
        auto hcalc = [&](const f32x4& g4, const f32x4& u4, const f32x4& cg, const f32x4& cu, int tb) {
          float hv[4];
#pragma unroll
          for (int j = 0; j < 4; ++j) {
            const float g = __builtin_fmaf(nmb[tb], cg[j], g4[j]) * al[tb];
            const float u = __builtin_fmaf(nmb[tb], cu[j], u4[j]);
            const float e = __builtin_amdgcn_exp2f(g);
            hv[j] = u * __builtin_amdgcn_rcpf(__builtin_fmaf(e, sd[tb], sd[tb]));
          }
          return u32x2{pkbf(hv[0], hv[1]), pkbf(hv[2], hv[3])};
        };
#pragma unroll
        for (int tb = 0; tb < 4; ++tb) {
          const u32x2 ha = hcalc(acc[0][0][tb], acc[1][0][tb], cga, cua, tb);
          const u32x2 hb = hcalc(acc[0][1][tb], acc[1][1][tb], cgb, cub, tb);
          ST_SC1((u32x4{ha[0], ha[1], hb[0], hb[1]}), hoff + fw * 4096 + tb * 1024);
          SBAR();
        }
        mlast(2, 0); mlast(2, 1); mlast(2, 2); mlast(2, 3);
        SBAR();
        {
          const f32x4 cg = *(LAS f32x4*)(pcs + oc), cu = *(LAS f32x4*)(pcs + 384 + oc);
          u32x2 hs[4];
#pragma unroll
          for (int tb = 0; tb < 4; ++tb) { hs[tb] = hcalc(acc[0][2][tb], acc[1][2][tb], cg, cu, tb); asm volatile("" : "+v"(hs[tb])); SBAR(); }
          waitv<0>();
          if (lane < 8) __hip_atomic_store((GAS u64*)(ws + WS_MB + (rb * 16 + tw * 8 + lane) * MBS + cb * 16 + fw * 8), tag, RLX_AGENT);
#pragma unroll
          for (int t2 = 0; t2 < 2; ++t2) ST_SC1((u32x4{hs[2 * t2][0], hs[2 * t2][1], hs[2 * t2 + 1][0], hs[2 * t2 + 1][1]}), hoff + 8192 + fw * 2048 + t2 * 1024);
        }
      }
    {
    const int sw = wid ^ 2, hm = sw & 1;
    lptr am = lds + SA + hm * PCH;
    f32x4 ca[8];
#pragma unroll
    for (int t = 0; t < 8; ++t) ca[t] = f32x4{0, 0, 0, 0};
    lptr pmain = am + O_KL + 1312 + lane * 16;
    flag_wait(fb, FL_KA + hm, 1);
    flag_wait(fb, FL_U, 2);
    conv_units<0xff>(am + O_KREV, lds + L_U + (hm + odd) * UCH, lane, sw < 2 ? 5 : 0, sw < 2 ? 3 : 5, ca, [&](int it) {
      if (it == 0) { waitv<0>(); if (lane < 8) __hip_atomic_store((GAS u64*)(ws + WS_MB + (rb * 16 + tw * 8 + lane) * MBS + 256 + cb * 16 + fw * 8), tag, RLX_AGENT); } });
    if (sw < 2) {
      lptr pc = lds + L_PC;
#pragma unroll
      for (int t = 0; t < 8; ++t) { *(LAS f32x4*)(pmain + t * 1024) = ca[t]; ca[t] = f32x4{0, 0, 0, 0}; }
      flag_set(fb, FL_PA + hm, 1);
      flag_wait(fb, FL_KC, 1);
      lptr uc_ = lds + L_U + (odd ? 0 : 2) * UCH;
      if (odd) conv_units<0x3a>(lds + L_KC, uc_, lane, hm * 4, 4, ca, [](int) {});
      else conv_units<0xc5>(lds + L_KC, uc_, lane, hm * 4, 4, ca, [](int) {});
      f32x4 c4[4];
      c4[0] = odd ? ca[1] : ca[0]; c4[1] = odd ? ca[3] : ca[2]; c4[2] = odd ? ca[4] : ca[6]; c4[3] = odd ? ca[5] : ca[7];
      {
        lptr po = pc + hm * 2048, pi = pc + (hm ^ 1) * 2048;
        *(LAS f32x4*)(po + lane * 16) = hm ? c4[0] : c4[2]; *(LAS f32x4*)(po + 1024 + lane * 16) = hm ? c4[1] : c4[3];
        flag_add(fb, FL_PC, lane);
        flag_wait(fb, FL_PC, 2);
        const f32x4 d0 = (hm ? c4[2] : c4[0]) + *(LAS f32x4*)(pi + lane * 16), d1 = (hm ? c4[3] : c4[1]) + *(LAS f32x4*)(pi + 1024 + lane * 16);
        *(LAS unsigned*)(pi + q * 128 + r * 4) = pkbf(d0[0], d0[1]); *(LAS unsigned*)(pi + q * 128 + r * 4 + 64) = pkbf(d0[2], d0[3]);
        *(LAS unsigned*)(pi + 512 + q * 128 + r * 4) = pkbf(d1[0], d1[1]); *(LAS unsigned*)(pi + 512 + q * 128 + r * 4 + 64) = pkbf(d1[2], d1[3]);
        const int T = ((odd ? 0x5431 : 0x7620) >> (4 * (2 * hm + (lane >> 5)))) & 7;
        ST_SC1(ld16(pi + lane * 16), WS_Y + ((T * 4 + ((lane >> 3) & 3)) * DM + chC) * 128 + (lane & 7) * 16);
      }
    } else {
      flag_wait(fb, FL_PA + hm, 1);
#pragma unroll
      for (int t = 0; t < 8; ++t) ca[t] += *(LAS f32x4*)(pmain + t * 1024);
#pragma unroll
      for (int t = 0; t < 8; ++t) { *(LAS unsigned*)(am + t * 512 + q * 128 + r * 4) = pkbf(ca[t][0], ca[t][1]); *(LAS unsigned*)(am + t * 512 + q * 128 + r * 4 + 64) = pkbf(ca[t][2], ca[t][3]); }
#pragma unroll
      for (int i = 0; i < 4; ++i) ST_SC1(ld16(am + i * 1024 + lane * 16), WS_Y + ((i * 8 + (lane >> 3)) * DM + chA + hm) * 128 + (lane & 7) * 16);
    }
    waitv<0>();
    if (flag_add(fb, FL_YC, lane) == 3u) {
      if (lane < 32) __hip_atomic_store((GAS u64*)(ws + WS_MB + (lane * 8 + xcd) * MBS + 512 + bi * 8), tag, RLX_AGENT);
      __builtin_amdgcn_s_sleep(16);
      _Pragma("nounroll") for (int sp = 0; sp < SPIN_GLB; ++sp) { if (wall(gflag_ok(ws, WS_MB + (tg * 8 + nb) * MBS + 512 + (lane & 31) * 8, tag))) break; __builtin_amdgcn_s_sleep(8); }
      flag_set(fb, FL_YOK, 1);
    }
    flag_wait(fb, FL_YOK, 1);
    u32x4 yv[3];
#pragma unroll
    for (int j = 0; j < 3; ++j) {
      const int p = umin(sw + 4 * j, 9), c = p * 64 + lane, row = (c * 6554) >> 16;
      glds1((const char*)x + (size_t)tg * 64 * 1280 + nb * 160, row * 1280 + (c - row * 10) * 16, lds0 + L_XT + p * 1024);
    }
#pragma unroll
    for (int j = 0; j < 3; ++j) { const int p = umin(sw + 4 * j, 9); yv[j] = LD_SC1(WS_Y + (tg * DM + nb * 80 + p * 8) * 128 + lane * 16); }
    lptr yb = lds + L_YB + sw * 3072;
#pragma unroll
    for (int j = 0; j < 3; ++j) st16(yb + j * 1024 + lane * 16, yv[j]);
#pragma unroll
    for (int j = 0; j < 3; ++j) {
      const u32x4 a = tr2(yb + j * 1024 + (r >> 2) * 128 + q * 32 + (lane & 3) * 8, 512);
      if (sw + 4 * j < 10) st16(lds + L_BASE + lane * 160 + (sw + 4 * j) * 16, a);
    }
    }
      __builtin_amdgcn_s_setprio(0);
    }
  }

  if (wid >= 4) {
    const int sw = wid - 4;
    const int lane = fresh_lane();
    {
      unsigned bo[4], ao[4];
#pragma unroll
      for (int c = 0; c < 4; ++c) { const int cc = sw * 4 + ((c + nb) & 3); bo[c] = WS_WD + (nb * 48 + cc * 3) * 5120; ao[c] = WS_H + (tg * 16 + cc) * 12288; }
      const unsigned l16 = lane * 16;
#define BFRAG(k, n) __builtin_amdgcn_raw_buffer_load_b128(rs, l16, bo[(k) < 8 ? (k) >> 1 : (k) - 8] + ((k) < 8 ? ((k) & 1) * 5120 : 10240) + (n) * 1024, SC1)
#define AFRAG(k, t) __builtin_amdgcn_raw_buffer_load_b128(rs, l16, ao[(k) < 8 ? (k) >> 1 : (k) - 8] + ((k) < 8 ? ((k) & 1) * 4096 : 8192) + (t) * 1024, SC1)
      u32x4 Bw[4][5];
#pragma unroll
      for (int k = 0; k < 4; ++k)
#pragma unroll
        for (int n = 0; n < 5; ++n) Bw[k][n] = BFRAG(k, n);
      wd_dma(lane, sw, 0, 2);
      flag_wait(fb, FL_G1, 4);
      wd_dma(lane, sw, 2, 4);
      lptr park = lds + SL2 + sw * 10240 + (lane >> 2) * 160 + (lane & 3) * 8;
      asm volatile("" : "+v"(park));
      const unsigned fo = WS_MB + (tg * 8 + nb) * MBS + (sw * 8 + (lane & 7)) * 8;
      _Pragma("nounroll") for (int sp = 0; sp < SPIN_GLB; ++sp) { if (wall(gflag_ok(ws, fo, tag))) break; __builtin_amdgcn_s_sleep(2); }
      asm volatile("" ::: "memory");
      f32x4 a2[5][4];
#pragma unroll
      for (int n = 0; n < 5; ++n)
#pragma unroll
        for (int tb = 0; tb < 4; ++tb) a2[n][tb] = f32x4{0, 0, 0, 0};
      u32x4 Ah[4][4];
#pragma unroll
      for (int k = 0; k < 3; ++k)
#pragma unroll
        for (int tb = 0; tb < 4; ++tb) Ah[k][tb] = AFRAG(k, tb);
      u64 f2 = 0;
      SBAR();
#pragma unroll
      for (int k = 0; k < 12; ++k) {
        if (k == 3) { f2 = __hip_atomic_load((GAS u64*)(ws + fo + 256), RLX_AGENT); SBAR(); }
        if (k == 5) {
          unsigned tc;
          asm volatile("s_mov_b32 %1, %5\n\t1:\n\tv_cmp_ne_u64 vcc, %2, %0\n\ts_cbranch_vccz 2f\n\ts_sleep 2\n\t"
                       "global_load_dwordx2 %0, %3, %4 offset:256 sc1\n\ts_sub_u32 %1, %1, 1\n\ts_waitcnt vmcnt(0)\n\ts_cbranch_scc0 1b\n\t2:"
                       : "+v"(f2), "=&s"(tc) : "s"(tag), "v"(fo), "s"(ws), "i"(SPIN_GLB) : "memory", "vcc", "scc");
          SBAR();
        }
        u32x4 hf[4];
        if (k >= 8) {
          const u32x4 X0 = Ah[k & 3][0], X1 = Ah[k & 3][1], Y0 = Ah[k & 3][2], Y1 = Ah[k & 3][3];
          hf[0] = u32x4{X0[0], X0[1], Y0[0], Y0[1]}; hf[1] = u32x4{X0[2], X0[3], Y0[2], Y0[3]};
          hf[2] = u32x4{X1[0], X1[1], Y1[0], Y1[1]}; hf[3] = u32x4{X1[2], X1[3], Y1[2], Y1[3]};
        } else {
#pragma unroll
          for (int tb = 0; tb < 4; ++tb) hf[tb] = Ah[k & 3][tb];
        }
#pragma unroll
        for (int e = 0; e < 20; ++e) {
          const int n = e >> 2, tb = e & 3;
          a2[n][tb] = MFMA_BF(Bw[k & 3][n], hf[tb], a2[n][tb]);
          if (e & 1) {
            const int j = e >> 1;
            if (j < 4) { if (k + 3 < 12) Ah[(k + 3) & 3][j] = AFRAG(k + 3, j); }
            else if (j < 9 && k >= 1 && k + 3 < 12) {
              if (k + 3 < 8) Bw[(k + 3) & 3][j - 4] = BFRAG(k + 3, j - 4);
              else Bw[(k + 3) & 3][j - 4] = tr2(park + ((k - 5) >> 1) * (SL0 - SL2) + ((k - 5) & 1) * 5120 + (j - 4) * 32, 2560);
            }
            SBAR();
          }
        }
#pragma unroll
        for (int n = 0; n < 5; ++n) asm volatile("" : "+v"(a2[n][0]), "+v"(a2[n][1]), "+v"(a2[n][2]), "+v"(a2[n][3]));
      }
      {
        lptr red = lds + SL2 + sw * 10240;
        const int l2 = fresh_lane(), r = l2 & 15, q = l2 >> 4;
#pragma unroll
        for (int n = 0; n < 5; ++n)
#pragma unroll
          for (int tb = 0; tb < 4; ++tb) {
            *(LAS f32x4*)(red + (q >> 1) * (SL0 - SL2) + (2 * n + (q & 1)) * 1024 + ((tb * 16 + r + 3 * n) & 63) * 16) = a2[n][tb];
          }
      }
    }
  }
  BAR_L();
  if (wid < 5) {
    const int tid = wid * 64 + fresh_lane();
    const int tok = (tid * 13108) >> 16, g = tid - tok * 5;
    lptr rp = lds + SL2 + g * 2048 + ((tok + 3 * g) & 63) * 16;
    f32x4 s[4];
#pragma unroll
    for (int m = 0; m < 4; ++m) {
      lptr p = rp + (m >> 1) * (SL0 - SL2) + (m & 1) * 1024;
      s[m] = (*(LAS f32x4*)(p) + *(LAS f32x4*)(p + 10240)) + (*(LAS f32x4*)(p + 20480) + *(LAS f32x4*)(p + 30720));
    }
    u16* op = out + (size_t)(tg * 64 + tok) * DM + nb * 80 + g * 16;
#pragma unroll
    for (int hh = 0; hh < 2; ++hh) {
      const u32x4 bs = ld16(lds + L_BASE + tid * 32 + hh * 16), xs = ld16(lds + L_XT + tid * 32 + hh * 16);
      const f32x4 sa = s[2 * hh], sb = s[2 * hh + 1];
      *(u32x4*)(op + hh * 8) = u32x4{pkbf((bflo(xs[0]) + bflo(bs[0])) + sa[0], (bfhi(xs[0]) + bfhi(bs[0])) + sa[1]), pkbf((bflo(xs[1]) + bflo(bs[1])) + sa[2], (bfhi(xs[1]) + bfhi(bs[1])) + sa[3]),
                                     pkbf((bflo(xs[2]) + bflo(bs[2])) + sb[0], (bfhi(xs[2]) + bfhi(bs[2])) + sb[1]), pkbf((bflo(xs[3]) + bflo(bs[3])) + sb[2], (bfhi(xs[3]) + bfhi(bs[3])) + sb[3])};
    }
  }
  asm volatile(".Lfk_tail:");
}

#if defined(__HIP_DEVICE_COMPILE__)
#pragma clang attribute pop
#endif

extern "C" void kernel_launch(void* const* d_in, const int* in_sizes, int n_in, void* d_out, int out_size, void* d_ws, size_t ws_size, hipStream_t stream) {
  (void)in_sizes; (void)n_in; (void)out_size; (void)ws_size;
  (void)hipFuncSetAttribute((const void*)fused_kernel, hipFuncAttributeMaxDynamicSharedMemorySize, LDS_BYTES);
  fused_kernel<<<256, 512, LDS_BYTES, stream>>>((const u16*)d_in[0], (const u16*)d_in[1], (const u16*)d_in[2], (const u16*)d_in[3], (const u16*)d_in[4],
                                                (const u16*)d_in[5], (const u16*)d_in[6], (const u16*)d_in[8], (const u16*)d_in[9], (const u16*)d_in[10],
                                                (u16*)d_out, (char*)d_ws);
}
```

```cpp
#include <hip/hip_runtime.h>
#include <stdint.h>

#if defined(__HIP_DEVICE_COMPILE__)
#pragma clang attribute push(__attribute__((target("no-packed-fp32-ops"))), apply_to = function)
#endif
typedef unsigned short u16;
typedef unsigned long long u64;
typedef short bf16x8 __attribute__((ext_vector_type(8)));
typedef _Float16 f16x8 __attribute__((ext_vector_type(8)));
typedef short s16x4 __attribute__((ext_vector_type(4)));
typedef float f32x2 __attribute__((ext_vector_type(2)));
typedef float f32x4 __attribute__((ext_vector_type(4)));
typedef unsigned u32x2 __attribute__((ext_vector_type(2)));
typedef unsigned u32x4 __attribute__((ext_vector_type(4)));
typedef _Float16 h16x2 __attribute__((ext_vector_type(2)));
typedef __bf16 b16x2 __attribute__((ext_vector_type(2)));
#define LAS __attribute__((address_space(3)))
#define GAS __attribute__((address_space(1)))
typedef LAS char* lptr;
#define DI __device__ __forceinline__
#define SBAR() __builtin_amdgcn_sched_barrier(0)
#define SGB(m, n) __builtin_amdgcn_sched_group_barrier(m, n, 0)
#define MFMA_BF(a, b, c) __builtin_amdgcn_mfma_f32_16x16x32_bf16(__builtin_bit_cast(bf16x8, a), __builtin_bit_cast(bf16x8, b), c, 0, 0, 0)
#define MFMA_H(a, b, c) __builtin_amdgcn_mfma_f32_16x16x32_f16(__builtin_bit_cast(f16x8, a), __builtin_bit_cast(f16x8, b), c, 0, 0, 0)
#define SC1 16

constexpr int DM = 640, NM = 192;
constexpr unsigned FBS = 64, WS_EPOCH = 0, WS_F0 = 262144, WS_FY = WS_F0 + 256 * FBS, WS_H1 = WS_FY + 256 * FBS, WS_H2 = WS_H1 + 1024 * FBS;
constexpr unsigned WS_ST = 65536, WS_CS = 98304, WS_XB = 655360, WS_WD = WS_XB + 2621440 + 4096;
constexpr unsigned WS_H = WS_WD + 1966080, WS_Y = WS_H + 6291456, WS_END = WS_Y + 2621440;
constexpr int STAGE = 40960, SL1 = 0, SA = 40960, SL2 = 81920, SL0 = 122880, POOL_BIAS = SA;
constexpr int PROW = 784, PCH = 23 * PROW;
constexpr int KC1 = 4228, O_KREV = 128, O_KL = 8528;
constexpr int L_FLG = SA + 2 * PCH, L_SC = L_FLG + 128, L_STG = L_FLG + 192, STGW = 2304;
constexpr int L_STAT = L_STG, L_CS = L_STG + 1024;
constexpr int L_RTB = SL2 + 32768;
constexpr int L_LNT = L_STG;
constexpr int L_LNB = SL2 + 18432, LNR = 1344;
constexpr int UCH = 9232, UC1 = 4640;
constexpr int L_U = SL1 + 128, L_KC = L_U + 3 * UCH + 128, L_PC = L_KC + 8400;
constexpr int L_XT = SL1;
constexpr int L_BASE = SA, L_YB = SA + 10240;
constexpr int LDS_BYTES = 163840;
static_assert(O_KL + 1312 + 8192 <= PCH && L_LNB + 8 * LNR <= L_RTB && L_RTB + 8192 <= SL0 && L_PC + 4096 <= SA && L_STG + 2 * STGW <= SL0 && (L_KC & 15) == 0 && (L_PC & 15) == 0 && (L_STG & 15) == 0, "lds map");
enum { FL_EPI = 0, FL_U, FL_KA, FL_KB, FL_KC, FL_KP, FL_PA, FL_PB, FL_PC, FL_YC, FL_YOK, FL_G1, FL_S2, FL_RT, FL_TAG = 16 };
constexpr int SPIN_LDS = 1 << 20, SPIN_GLB = 1 << 17;

DI bool wall(bool p) { return __builtin_amdgcn_ballot_w64(!p) == 0; }
DI unsigned umin(unsigned a, unsigned b) { return a < b ? a : b; }
DI unsigned umax(unsigned a, unsigned b) { return a > b ? a : b; }
DI float asf(unsigned v) { return __builtin_bit_cast(float, v); }
DI unsigned asu(float v) { return __builtin_bit_cast(unsigned, v); }
DI float bflo(unsigned v) { return asf(v << 16); }
DI float bfhi(unsigned v) { return asf(v & 0xffff0000u); }
DI float bf1(u16 v) { return asf((unsigned)v << 16); }
DI unsigned pkbf(float lo, float hi) { return __builtin_bit_cast(unsigned, __builtin_convertvector(f32x2{lo, hi}, b16x2)); }
DI float rbf(float x) { return bflo(pkbf(x, x)); }
template <int CTRL> DI float dppf(float x) { return __builtin_bit_cast(float, __builtin_amdgcn_mov_dpp(__builtin_bit_cast(int, x), CTRL, 0xf, 0xf, true)); }
DI float row16_sum(float v) { v += dppf<0xB1>(v); v += dppf<0x4E>(v); v += dppf<0x124>(v); v += dppf<0x128>(v); return v; }

DI void glds4(const void* sb, unsigned v0, unsigned v1, unsigned v2, unsigned v3, unsigned lds) {
  asm volatile("s_mov_b32 m0, %5\n\ts_nop 0\n\tglobal_load_lds_dwordx4 %0, %4\n\tglobal_load_lds_dwordx4 %1, %4 offset:1024\n\t"
               "global_load_lds_dwordx4 %2, %4 offset:2048\n\tglobal_load_lds_dwordx4 %3, %4 offset:3072"
               :: "v"(v0), "v"(v1), "v"(v2), "v"(v3), "s"(sb), "s"(lds) : "memory");
}
DI void glds1(const void* sb, unsigned v0, unsigned lds) {
  asm volatile("s_mov_b32 m0, %2\n\ts_nop 0\n\tglobal_load_lds_dwordx4 %0, %1" :: "v"(v0), "s"(sb), "s"(lds) : "memory");
}
template <int N> DI void waitv() { asm volatile("s_waitcnt vmcnt(%0)" :: "i"(N) : "memory"); }
DI void waitl() { asm volatile("s_waitcnt lgkmcnt(0)" ::: "memory"); }
DI u32x2 vtr(lptr p) { return __builtin_bit_cast(u32x2, __builtin_amdgcn_ds_read_tr16_b64_v4i16((LAS s16x4*)p)); }
DI u32x4 tr2(lptr p, int d) { u32x2 lo = vtr(p), hi = vtr(p + d); return u32x4{lo[0], lo[1], hi[0], hi[1]}; }
DI u32x4 ld16(lptr p) { return *(LAS u32x4*)p; }
DI void st16(lptr p, u32x4 v) { *(LAS u32x4*)p = v; }

DI unsigned flag_ld(unsigned addr) { unsigned r; asm volatile("ds_read_b32 %0, %1\n\ts_waitcnt lgkmcnt(0)" : "=v"(r) : "v"(addr) : "memory"); return __builtin_amdgcn_readfirstlane(r); }
DI void flag_wait(unsigned fb, int idx, unsigned need) { _Pragma("nounroll") for (int sp = 0; sp < SPIN_LDS && flag_ld(fb + idx * 4) < need; ++sp) __builtin_amdgcn_s_sleep(1); }
DI void flag_set(unsigned fb, int idx, unsigned v) {
  asm volatile("s_waitcnt lgkmcnt(0)\n\ts_mov_b64 exec, 1\n\tds_write_b32 %0, %1\n\ts_mov_b64 exec, -1" :: "v"(fb + idx * 4), "v"(v) : "memory"); }
DI unsigned flag_add(unsigned fb, int idx, int lane) {
  unsigned r; (void)lane;
  asm volatile("s_waitcnt lgkmcnt(0)\n\ts_mov_b64 exec, 1\n\tds_add_rtn_u32 %0, %1, %2\n\ts_mov_b64 exec, -1\n\ts_waitcnt lgkmcnt(0)" : "=&v"(r) : "v"(fb + idx * 4), "v"(1u) : "memory");
  return __builtin_amdgcn_readfirstlane(r);
}
#define RLX_AGENT __ATOMIC_RELAXED, __HIP_MEMORY_SCOPE_AGENT
DI bool gflag_ok(const char* ws, unsigned off, u64 tag) { return __hip_atomic_load((GAS u64*)(ws + off), RLX_AGENT) == tag; }

DI f32x2 csq(f32x2 x) { const float t = x.x + x.x; return f32x2{__builtin_fmaf(x.x, x.x, -(x.y * x.y)), t * x.y}; }
DI h16x2 toh(f32x2 v) { return __builtin_convertvector(v, h16x2); }
DI h16x2 hmul(h16x2 x, h16x2 y) { return __builtin_elementwise_fma(h16x2{x.y, x.y}, h16x2{-y.y, y.x}, h16x2{x.x, x.x} * y); }
DI f32x2 cexp_p(float a2, float bh, float bl, float p) {
  const float e = __builtin_amdgcn_exp2f(a2 * p);
  const float ph = __builtin_fmaf(bl, p, __builtin_amdgcn_fractf(bh * p));
  return f32x2{e * __builtin_amdgcn_cosf(ph), e * __builtin_amdgcn_sinf(ph)};
}
template <int NS, int ROW0> DI void put4(const unsigned (&tb)[NS], const h16x2 (&a)[NS], const h16x2 (&b)[NS], const h16x2 (&c)[NS], const h16x2 (&d)[NS]) {
#pragma unroll
  for (int e = 0; e < NS; ++e)
    asm volatile("s_mov_b32 m0, %4\n\ts_nop 0\n\tds_write_addtid_b32 %0 offset:%5\n\tds_write_addtid_b32 %1 offset:%5+784\n\t"
                 "ds_write_addtid_b32 %2 offset:%5+1568\n\tds_write_addtid_b32 %3 offset:%5+2352"
                 :: "v"(a[e]), "v"(b[e]), "v"(c[e]), "v"(d[e]), "s"(tb[e]), "i"(ROW0 * PROW + POOL_BIAS) : "memory");
}
#define FORS _Pragma("unroll") for (int e = 0; e < NS; ++e)
template <int NS> DI void kpool(const unsigned (&tb)[NS], const float (&pv)[NS][4], const float (&ldt)[NS], bool join, float (&sc)[NS]) {
  float dt[NS];
  FORS dt[e] = rbf(__builtin_amdgcn_exp2f(ldt[e] * 1.44269504f));
  unsigned eb[NS]; float cs[NS];
  FORS {
    unsigned mi = umax(asu(pv[e][2]) & 0x7fffffffu, asu(pv[e][3]) & 0x7fffffffu);
    mi = umax(mi, asu(dppf<0xB1>(asf(mi)))); mi = umax(mi, asu(dppf<0x4E>(asf(mi)))); mi = umax(mi, asu(dppf<0x124>(asf(mi)))); mi = umax(mi, asu(dppf<0x128>(asf(mi))));
    eb[e] = umax(umax((unsigned)__builtin_amdgcn_readlane(mi, 0), (unsigned)__builtin_amdgcn_readlane(mi, 16)),
                 umax((unsigned)__builtin_amdgcn_readlane(mi, 32), (unsigned)__builtin_amdgcn_readlane(mi, 48))) >> 23;
  }
  if (join) eb[0] = eb[NS - 1] = umax(eb[0], eb[NS - 1]);
  FORS { eb[e] = umin(umax(eb[e], 2u), 252u); cs[e] = asf((254u - eb[e]) << 23); }
  float a2[NS], bh[NS], bl[NS];
  FORS {
    const float ea = rbf(__builtin_amdgcn_exp2f(pv[e][0] * 1.44269504f));
    a2[e] = -(ea * dt[e]) * 1.44269504f;
    const float b = pv[e][1] * dt[e];
    bh[e] = b * 0.15915494f;
    bl[e] = __builtin_fmaf(b, 6.4206382e-9f, __builtin_fmaf(b, 0.15915494f, -bh[e]));
  }
  h16x2 one[NS], w8[NS], w16[NS], w24[NS], w32[NS], w64[NS], w96[NS], w1[NS], w2[NS], w3[NS], w4[NS], C[NS], c4[NS];
  FORS {
    const f32x2 f1 = cexp_p(a2[e], bh[e], bl[e], 1.f), f2 = csq(f1), f4 = csq(f2);
    const f32x2 f8 = csq(f32x2{f4.x, -f4.y}), f16 = csq(f8), f32 = csq(f16), f64 = csq(f32);
    one[e] = h16x2{(_Float16)1.f, (_Float16)0.f};
    w8[e] = toh(f8); w16[e] = toh(f16); w32[e] = toh(f32); w64[e] = toh(f64); w24[e] = hmul(w8[e], w16[e]); w96[e] = hmul(w32[e], w64[e]);
    w1[e] = toh(f1); w2[e] = toh(f2); w4[e] = toh(f4); w3[e] = hmul(w1[e], w2[e]);
    C[e] = toh(f32x2{pv[e][2] * cs[e], pv[e][3] * cs[e]}); c4[e] = hmul(C[e], w4[e]);
  }
  put4<NS, 0>(tb, one, w8, w16, w24);
  put4<NS, 4>(tb, w32, w64, w96, w1);
  put4<NS, 8>(tb, w2, w3, C, c4);
  h16x2 c128[NS], c132[NS], w256[NS], w258[NS], w512[NS], w768[NS], w1024[NS], w1280[NS], w1536[NS], w1792[NS], w1796[NS];
  FORS {
    const f32x2 f128 = cexp_p(a2[e], bh[e], bl[e], 128.f), f256 = csq(f128), f512 = csq(f256);
    const h16x2 w128 = toh(f128);
    w256[e] = toh(f256); w512[e] = toh(f512); c128[e] = hmul(C[e], w128); c132[e] = hmul(c4[e], w128);
    w258[e] = hmul(w256[e], w2[e]); w768[e] = hmul(w512[e], w256[e]);
    w1024[e] = toh(csq(f512)); w1280[e] = hmul(w1024[e], w256[e]); w1536[e] = hmul(w1024[e], w512[e]);
    w1792[e] = hmul(w1024[e], w768[e]); w1796[e] = hmul(w1792[e], w4[e]);
  }
  put4<NS, 12>(tb, c128, c132, w256, w258);
  put4<NS, 16>(tb, w512, w768, w1024, w1280);
  put4<NS, 19>(tb, w1280, w1536, w1792, w1796);
  FORS sc[e] = 2.f * dt[e] * asf(eb[e] << 23);
}
struct KPtr {
  lptr pbx, pby, py, px0, px2;
  DI void init(lptr pool, int lane) {
    const int r = lane & 15, x = r & 3, y = r >> 2;
    lptr p = pool + (lane >> 4) * 16;
    pbx = p + x * PROW; pby = p + (y ? 3 + y : 0) * PROW; py = p + (10 + y) * PROW; px0 = p + (x ? 6 + x : 0) * PROW; px2 = p + (y == 3 ? 22 : 18 + x) * PROW;
  }
  DI void fetch(u32x4 (&v)[6], int o) const {
    v[0] = ld16(pbx + o); v[1] = ld16(pby + o); v[2] = ld16(py + o); v[3] = ld16(px0 + o); v[4] = ld16(pbx + 14 * PROW + o); v[5] = ld16(px2 + o);
  }
};
DI void kprod(const u32x4 (&v)[6], u32x4 (&o)[4]) {
  unsigned t[16];
#pragma unroll
  for (int i = 0; i < 16; ++i) { const unsigned xe = v[(i >> 2) ? 2 + (i >> 2) : 0][i & 3], ye = v[(i >> 2) ? 2 : 1][i & 3];
    asm("v_pk_mul_f16 %0, %1, %2 op_sel_hi:[0,1]" : "=v"(t[i]) : "v"(xe), "v"(ye)); }
  SBAR();
#pragma unroll
  for (int i = 0; i < 16; ++i) { const unsigned xe = v[(i >> 2) ? 2 + (i >> 2) : 0][i & 3], ye = v[(i >> 2) ? 2 : 1][i & 3]; unsigned z;
    asm("v_pk_fma_f16 %0, %1, %2, %3 op_sel:[1,1,0] op_sel_hi:[1,0,1] neg_lo:[0,1,0]" : "=v"(z) : "v"(xe), "v"(ye), "v"(t[i])); o[i >> 2][i & 3] = z; }
  SBAR();
}
template <int H> DI void kprodh(const u32x4 (&v)[6], u32x4 (&o)[4]) {
  unsigned t[8];
#pragma unroll
  for (int i = 8 * H; i < 8 * H + 8; ++i) { const unsigned xe = v[(i >> 2) ? 2 + (i >> 2) : 0][i & 3], ye = v[(i >> 2) ? 2 : 1][i & 3];
    asm("v_pk_mul_f16 %0, %1, %2 op_sel_hi:[0,1]" : "=v"(t[i & 7]) : "v"(xe), "v"(ye)); }
  SBAR();
#pragma unroll
  for (int i = 8 * H; i < 8 * H + 8; ++i) { const unsigned xe = v[(i >> 2) ? 2 + (i >> 2) : 0][i & 3], ye = v[(i >> 2) ? 2 : 1][i & 3]; unsigned z;
    asm("v_pk_fma_f16 %0, %1, %2, %3 op_sel:[1,1,0] op_sel_hi:[1,0,1] neg_lo:[0,1,0]" : "=v"(z) : "v"(xe), "v"(ye), "v"(t[i & 7])); o[i >> 2][i & 3] = z; }
  asm volatile("" : "+v"(o[2 * H]), "+v"(o[2 * H + 1]));
  SBAR();
}
DI void kmma1(const u32x4 (&o0)[4], f32x4 (&acc)[3], bool first) {
  SBAR(); asm volatile("s_setprio 3\n\ts_nop 1"); SBAR();
#pragma unroll
  for (int i = 0; i < 3; ++i) acc[i] = MFMA_H(o0[1 + i], o0[0], (first ? f32x4{0, 0, 0, 0} : acc[i]));
  SBAR(); __builtin_amdgcn_s_setprio(0); SBAR();
}
DI void kcombine(f32x4 (&ko)[3], const f32x4 (&ka)[3][3], float s0, float s1, float s2) {
#pragma unroll
  for (int i = 0; i < 3; ++i)
#pragma unroll
    for (int j = 0; j < 4; ++j) ko[i][j] = __builtin_fmaf(ka[2][i][j], s2, __builtin_fmaf(ka[1][i][j], s1, ka[0][i][j] * s0));
}
DI float ubf(const u16* p, int i) { const unsigned w = ((const __attribute__((address_space(4))) unsigned*)(uintptr_t)p)[i >> 1]; return (i & 1) ? bfhi(w) : bflo(w); }
DI void kstore(lptr kl, int lane, const f32x4 (&acc)[3], float dsk) {
  const int c = lane & 15, q = lane >> 4;
  LAS u16* t = (LAS u16*)kl;
  const float k0 = acc[0][0] + (lane == 0 ? dsk : 0.f);
  *(LAS u32x2*)(t + 128 * (q >> 1) + 8 * c + 4 * (q & 1)) = u32x2{pkbf(k0, acc[0][1]), pkbf(acc[0][2], acc[0][3])};
  *(LAS unsigned*)(t + 256 + 64 * (q >> 1) + 2 * (q & 1) + 4 * c) = pkbf(acc[1][0], acc[1][1]);
  const unsigned p1 = pkbf(acc[1][2], acc[1][3]);
  const int i1 = 384 + 32 * (q >> 1) + (q & 1) + 2 * c;
  t[i1] = (u16)p1; t[i1 + 64] = (u16)(p1 >> 16);
  const unsigned r0 = pkbf(acc[2][0], acc[2][1]), r1 = pkbf(acc[2][2], acc[2][3]);
  if (!(q & 1)) { const int i2 = 512 + 8 * q + c; t[i2] = (u16)r0; t[i2 + 32] = (u16)(r0 >> 16); t[i2 + 64] = (u16)r1; t[i2 + 96] = (u16)(r1 >> 16); }
  else if (lane == 63) t[640] = (u16)r0;
}
DI unsigned dup16(unsigned a) { return a | (a << 16); }
DI unsigned bsel(unsigned m, unsigned a, unsigned b) { asm("" : "+v"(m)); return (a & m) | (b & ~m); }
DI unsigned rot16(unsigned a) { return __builtin_amdgcn_alignbit(a, a, 16); }
DI void kexpand(lptr kr, lptr kl, int lane) {
  LAS u16* t = (LAS u16*)kl;
  auto put = [&](int ch, unsigned dp, unsigned d0, unsigned d1, unsigned d2, unsigned d3) {
    st16(kr + 16 * ch, u32x4{d0, d1, d2, d3}); st16(kr + KC1 - 4 + 16 * ch, u32x4{dp, d0, d1, d2}); };
  const int n = 63 - lane, n0 = n & 31;
  const unsigned a0 = t[576 + n], b0 = t[577 + n], a1 = t[512 + n], b1 = t[513 + n];
  const unsigned ab = *(LAS unsigned*)(t + 384 + 2 * n), c2 = t[386 + 2 * n];
  const u32x2 v1 = *(LAS u32x2*)(t + 256 + 4 * n0); const unsigned q4 = t[260 + 4 * n0];
  const u32x4 v0 = ld16((lptr)(t + 8 * n0)); const unsigned w = *(LAS unsigned*)(t + 8 * n0 + 8);
  const unsigned mo = (n & 1) ? ~0u : 0u, lo = lane >= 32 ? ~0u : 0u, m31 = n == 31 ? ~0u : 0u;
  put(lane, dup16(b0), dup16(b0), bsel(mo, dup16(b0), b0 | (a0 << 16)), dup16(a0), dup16(a0));
  put(64 + lane, dup16(b1), dup16(b1), bsel(mo, dup16(b1), b1 | (a1 << 16)), dup16(a1), dup16(a1));
  put(128 + lane, dup16(c2), dup16(c2), dup16(ab >> 16), rot16(ab), dup16(ab & 0xffffu));
  put(192 + lane, bsel(lo, bsel(m31, dup16(w & 0xffffu), rot16(w)), dup16(q4)), bsel(lo, rot16(v0[3]), q4 | (v1[1] & 0xffff0000u)),
      bsel(lo, rot16(v0[2]), dup16(v1[1] & 0xffffu)), bsel(lo, rot16(v0[1]), (v1[1] & 0xffffu) | (v1[0] & 0xffff0000u)), bsel(lo, rot16(v0[0]), dup16(v1[0] & 0xffffu)));
  if (lane >= 60) put(319 - lane, lane == 63 ? rot16(v0[0]) : 0u, 0u, 0u, 0u, 0u);
}

struct ConvPlan { int n; int item[36][3]; };
constexpr ConvPlan conv_plan(int tmask, int maxt) {
  ConvPlan p{};
  int fifo[64] = {}, head = 0, tail = 0, carry[16] = {}, nc = 0, per = (tmask == 0xff) ? 1 : 2;
  for (int pass = 0; pass < 2; ++pass) {
    head = tail = 0;
    for (int i = 0; i < nc; ++i) fifo[tail++] = carry[i] + 32;
    int e = 0;
    for (int m = maxt; m >= 0; --m) {
      for (int j = 0; j + m <= maxt; ++j) {
        const bool on = (tmask >> (m + j)) & 1;
        int k = 0;
        const bool lastu = on && ((tmask >> j) & ((1 << m) - 1)) == 0;
        if (lastu) { if (j < maxt - 1) p.item[e][k++] = j; else fifo[tail++] = j; }
        if (j + m == maxt) { fifo[tail++] = 8 + 2 * m; fifo[tail++] = 9 + 2 * m; }
        if (on) { while (k < per && head < tail) p.item[e][k++] = fifo[head++]; while (k < 3) p.item[e][k++] = -1; ++e; }
      }
    }
    p.n = e; nc = 0;
    while (head < tail) carry[nc++] = fifo[head++];
  }
  return p;
}
template <int TMASK, class F>
DI void conv_units(lptr kr, lptr ub, int lane, int rho0, int nrho, f32x4 (&acc)[8], F hook) {
  constexpr int MAXT = (TMASK & 0x80) ? 7 : 5;
  constexpr ConvPlan plan = conv_plan(TMASK, MAXT);
  const int r = lane & 15, q = lane >> 4;
  lptr pk = kr + ((r & 1) ? 0 : KC1) + 4092 - 4 * r + 16 * q - 3584 - 64 * rho0 - 64;
  lptr pu = ub + ((r & 1) ? 0 : UC1) + 64 * (r >> 1) + 16 * q + 64 * (7 - rho0) - 64;
  lptr pk2 = pk + 8;
  u32x2 Kl[8], Kh[8]; u32x4 Uf[8];
  asm volatile("" : "+v"(pk), "+v"(pk2), "+v"(pu));
#pragma unroll
  for (int j = 0; j <= MAXT; ++j) { const int m = MAXT - j; Uf[j] = ld16(pu + 64 + 512 * j); Kl[m] = *(LAS u32x2*)(pk + 64 + 512 * (7 - m)); Kh[m] = *(LAS u32x2*)(pk2 + 64 + 512 * (7 - m)); SBAR(); }
#pragma unroll 1
  for (int it = 0; it < nrho; ++it) {
    int e = 0;
#pragma unroll
    for (int m = MAXT; m >= 0; --m) {
#pragma unroll
      for (int j = 0; j + m <= MAXT; ++j) {
        if ((TMASK >> (m + j)) & 1) {
          acc[m + j] = MFMA_BF(Uf[j], (u32x4{Kl[m][0], Kl[m][1], Kh[m][0], Kh[m][1]}), acc[m + j]);
#pragma unroll
          for (int k = 0; k < 3; ++k) {
            const int c = plan.item[e][k];
            if (c >= 0) {
              const int o = (c & 32) ? 64 : 0, cc = c & 31, mm = (cc - 8) >> 1;
              if (cc < 8) Uf[cc] = ld16(pu + o + 512 * cc);
              else if (cc & 1) Kh[mm] = *(LAS u32x2*)(pk2 + o + 512 * (7 - mm));
              else Kl[mm] = *(LAS u32x2*)(pk + o + 512 * (7 - mm));
            }
          }
          SBAR();
          ++e;
        }
      }
    }
    pk -= 64; pk2 -= 64; pu -= 64;
    asm volatile("" : "+v"(pk), "+v"(pk2), "+v"(pu));
    hook(it);
  }
}

DI u64 uni64(u64 v) { return (u64)(unsigned)__builtin_amdgcn_readfirstlane((unsigned)v) | ((u64)(unsigned)__builtin_amdgcn_readfirstlane((unsigned)(v >> 32)) << 32); }
DI int fresh_lane() { int l; asm volatile("v_mbcnt_lo_u32_b32 %0, -1, 0\n\tv_mbcnt_hi_u32_b32 %0, -1, %0" : "=v"(l)); return l; }
#define BAR() do { SBAR(); asm volatile("s_barrier" ::: "memory"); SBAR(); } while (0)
#define BAR_L() do { SBAR(); asm volatile("s_waitcnt lgkmcnt(0)\n\ts_barrier" ::: "memory"); SBAR(); } while (0)
#define PUBLISH(off) do { if (lane < FBS / 16) ST_SC1((u32x4{(unsigned)tag, (unsigned)(tag >> 32), (unsigned)tag, (unsigned)(tag >> 32)}), (off) + lane * 16); } while (0)
#define LD_SC1(off) __builtin_amdgcn_raw_buffer_load_b128(rs, (off), 0, SC1)
#define ST_SC1(v, off) __builtin_amdgcn_raw_buffer_store_b128((v), rs, (off), 0, SC1)

__global__ __launch_bounds__(512, 2) void fused_kernel(const u16* __restrict__ x, const u16* __restrict__ p_lar, const u16* __restrict__ p_aim,
                                                       const u16* __restrict__ p_cr, const u16* __restrict__ p_ci, const u16* __restrict__ p_ldt,
                                                       const u16* __restrict__ p_dsk, const u16* __restrict__ wg, const u16* __restrict__ wu,
                                                       const u16* __restrict__ wd, u16* __restrict__ out, char* ws) {
  extern __shared__ __attribute__((aligned(16))) char smem[];
  const int tid = __builtin_amdgcn_workitem_id_x(), bid = __builtin_amdgcn_workgroup_id_x();
  if (__builtin_amdgcn_readfirstlane(tid >> 6) == 6) {
    u64 pc_; unsigned len_;
    asm volatile("s_getpc_b64 %0\n.Lfk_head:\n\ts_mov_b32 %1, .Lfk_tail-.Lfk_head" : "=s"(pc_), "=s"(len_));
    asm volatile("s_mov_b32 m0, %2\n\ts_mov_b32 exec_hi, 0xff\n\tglobal_load_lds_dword %0, %1\n\ts_mov_b64 exec, -1"
                 :: "v"(umin((((bid >> 3) & 7) * 40 + (tid & 63)) * 128, len_)), "s"(pc_), "s"((unsigned)(uintptr_t)smem + L_RTB + 7936) : "memory");
  }
  lptr lds = (lptr)smem;
  const unsigned lds0 = (unsigned)(uintptr_t)smem;
  const unsigned fb = lds0 + L_FLG;
  const int wraw = __builtin_amdgcn_readfirstlane(tid >> 6), lane = tid & 63;
  const int wid = wraw ^ ((wraw >> 2) << 1);
  const int xcd = bid & 7, bi = bid >> 3;
  const int rb = (xcd >> 2) * 8 + (bi >> 2), cb = (xcd & 3) * 4 + (bi & 3);
  const int tg = (xcd >> 1) * 8 + (bi >> 2), nb = (xcd & 1) * 4 + (bi & 3);
  const int cw = xcd * 32 + bi, odd = cw & 1, c0 = 5 * (cw >> 1) + 2 * odd;
  const int chA = c0 + odd, chC = odd ? c0 : c0 + 2;
  const int rm = rb & 7, rot = 2 * (rm >= 5 ? rm - 5 : rm) + (rb >> 3);
  const bool csum = rm < 5;
  const __amdgpu_buffer_rsrc_t rs = __builtin_amdgcn_make_buffer_rsrc(ws, 0, (int)WS_END, 0x00020000);
  const int r = lane & 15, q = lane >> 4;

  u64 tag = 0;
  const int lw = wid - 4, kw = wid - 6;
  unsigned vx[4], vw[12];
  auto issue = [&](int s, int sbase, int h, int part = 3) {
    int ks = s + rot; if (ks >= 10) ks -= 10;
    const unsigned base = lds0 + sbase;
    const char* sx = (const char*)x + (size_t)(rb * 128 + h * 64) * 1280 - 4096 + ks * 128;
    const char* sw = (const char*)(h ? wu : wg) + cb * 192 - 4096 + (size_t)ks * 64 * 3072;
    if (part & 1) {
      glds4(sx, vx[0], vx[1], vx[2], vx[3], base + h * 8192);
      glds4(sw, vw[0], vw[1], vw[2], vw[3], base + 16384 + h * 12288);
      glds4(sx + 32 * 1280, vx[0], vx[1], vx[2], vx[3], base + h * 8192 + 4096);
    }
    if (part & 2) {
      glds4(sw, vw[4], vw[5], vw[6], vw[7], base + 16384 + h * 12288 + 4096);
      glds4(sw, vw[8], vw[9], vw[10], vw[11], base + 16384 + h * 12288 + 8192);
    }
  };
  auto prep_dma = [&](int ln) {
#pragma unroll
    for (int j = 0; j < 4; ++j) { const int row = 8 * j + (ln >> 3); vx[j] = row * 1280 + (((ln & 7) ^ ((row >> 1) & 7)) << 4) + 4096 - 1024 * j; }
#pragma unroll
    for (int j = 0; j < 3; ++j) { const int c = j * 64 + ln, row = (c * 43691) >> 19, pos = c - row * 12;
      const unsigned v = row * 3072 + ((pos ^ (((row >> 3) & 1) << 1)) << 4) + 4096;
#pragma unroll
      for (int g = 0; g < 4; ++g) vw[3 * g + j] = v + g * 16 * 3072 - 1024 * ((3 * g + j) & 3); }
  };
  auto wd_dma = [&](int ln, int w, int j0, int j1) {
    unsigned v[5];
#pragma unroll
    for (int i = 0; i < 5; ++i) { const int c = i * 64 + ln, row = (c * 6554) >> 16; v[i] = row * 1280 + (c - row * 10) * 16 + 4096 - (i & 3) * 1024; }
#pragma unroll
    for (int j = j0; j < j1; ++j) {
      const char* sp = (const char*)wd + (size_t)((w * 4 + ((j + nb) & 3)) * 96 + 32) * 1280 + nb * 160 - 4096;
      const unsigned d = lds0 + ((j >> 1) ? SL0 : SL2) + w * 10240 + (j & 1) * 5120;
      glds4(sp, v[0], v[1], v[2], v[3], d); glds1(sp, v[4], d + 4096);
    }
  };
  const int lnb = cw;
  const int ro0 = (0x100222 >> (4 * wid)) & 3, th0 = (0x000210 >> (4 * wid)) & 3, ro1 = (wid >> 1) & 1, th1 = 1 + (wid & 1);
  const int ch0 = c0 + ((0x0210 >> (4 * (ro0 + odd))) & 3), ch1 = chA + ro1;
  const int pt0 = ro0 * PCH + (ro0 >> 1) * (SL2 - SA - 2 * PCH) + th0 * 256, pt1 = ro1 * PCH + th1 * 256;
  unsigned o = (lane < 32 ? ch0 * 384 + th0 * 128 : ch1 * 384 + th1 * 128) + (lane & 31) * 4;
  unsigned xo0 = lane * 16, xo1 = q * 1280 + r * 16 + 1024;
  asm volatile("" : "+v"(o), "+v"(xo0), "+v"(xo1));
  asm volatile("" :: "s"(x), "s"(p_lar), "s"(p_aim), "s"(p_cr), "s"(p_ci), "s"(p_ldt), "s"(p_dsk), "s"(wg), "s"(wu), "s"(wd), "s"(out), "s"(ws));
  const int ptp = wid == 5 ? pt0 : pt1;
  if ((0x2f >> wid) & 1) {
    asm volatile("s_mov_b32 m0, %5\n\ts_nop 0\n\tglobal_load_lds_dword %0, %1\n\tglobal_load_lds_dword %0, %2 offset:784\n\t"
                 "global_load_lds_dword %0, %3 offset:1568\n\tglobal_load_lds_dword %0, %4 offset:2352"
                 :: "v"(o), "s"(p_lar), "s"((const char*)p_aim - 784), "s"((const char*)p_cr - 1568), "s"((const char*)p_ci - 2352), "s"(lds0 + SA + ptp) : "memory");
  }
  if (wid >= 6) {
    const char* xr = (const char*)x + (size_t)(lnb * 8 + kw * 4) * 1280;
#pragma unroll
    for (int j = 0; j < 4; ++j) glds1(xr + j * 1280, xo0, lds0 + L_LNB + (kw * 4 + j) * LNR);
    glds1(xr, xo1, lds0 + L_LNT + kw * 1024);
  } else if (wid >= 4) { if (lane < 16) *(LAS unsigned*)(lds + L_FLG + lane * 4) = 0; prep_dma(lane); }
  SBAR(); asm volatile("s_waitcnt lgkmcnt(0)\n\ts_barrier" ::: "memory"); SBAR();
  const float ldt0 = ubf(p_ldt, ch0), ldt1 = ubf(p_ldt, ch1);
  if (wid == 4 || wid == 5) {
    issue(0, SL0, lw);
  }
  auto lds_params = [&](float (&pv)[4], int m) {
#pragma unroll
    for (int e = 0; e < 4; ++e) pv[e] = bf1(*(LAS u16*)(lds + SA + ptp + e * PROW + m * 2));
  };
  if (wid >= 6) {
    waitv<0>();
    lptr xp = lds + L_LNB + (kw * 4 + q) * LNR + r * 16;
    u32x4 xv[5];
#pragma unroll
    for (int j = 0; j < 4; ++j) xv[j] = ld16(xp + 256 * j);
    xv[4] = ld16(lds + L_LNT + kw * 1024 + lane * 16);
    float v[40]; float s0 = 0.f, s1 = 0.f;
#pragma unroll
    for (int j = 0; j < 20; ++j) { const unsigned w = xv[j >> 2][j & 3]; v[2 * j] = bflo(w); v[2 * j + 1] = bfhi(w); s0 += v[2 * j]; s1 += v[2 * j + 1]; }
    const float mean = row16_sum(s0 + s1) * (1.f / DM);
    float q0 = 0.f, q1 = 0.f;
#pragma unroll
    for (int j = 0; j < 20; ++j) { v[2 * j] -= mean; v[2 * j + 1] -= mean; q0 = __builtin_fmaf(v[2 * j], v[2 * j], q0); q1 = __builtin_fmaf(v[2 * j + 1], v[2 * j + 1], q1); }
    const float var = rbf(row16_sum(q0 + q1) * (1.f / DM));
    const float mb = rbf(mean), sd = rbf(__builtin_amdgcn_sqrtf(rbf(var + 1e-5f)));
    const float rsd = __builtin_amdgcn_rcpf(sd), off = (mean - mb) * rsd;
#pragma unroll
    for (int j = 0; j < 20; ++j) xv[j >> 2][j & 3] = pkbf(__builtin_fmaf(v[2 * j], rsd, off), __builtin_fmaf(v[2 * j + 1], rsd, off));
#pragma unroll
    for (int j = 0; j < 5; ++j) st16(xp + 256 * j, xv[j]);
    if (r == 0) __builtin_amdgcn_raw_buffer_store_b64(u32x2{asu(mb), asu(sd)}, rs, WS_ST + (lnb * 8 + kw * 4 + q) * 8, 0, SC1);
  } else if (wid == 4) {
    issue(1, SL1, 0);
    const int c3 = cw >> 4, kt = (cw >> 3) & 1, unb = cw & 7;
#pragma unroll
    for (int j = 0; j < 5; ++j) { const int c = j * 64 + lane, row = (c * 6554) >> 16, pos = c - row * 10;
      glds1(wd, (c3 * 96 + row + kt * (row < 16 ? 80 : 48)) * 1280 + unb * 160 + pos * 16, lds0 + L_RTB + j * 1024); }
    glds1(wd, ((cw >> 4) * 96 + 32 + (cw & 15) * 2) * 1280 + lane * 16, lds0 + L_RTB + 5120); glds1(wd, ((cw >> 4) * 96 + 32 + (cw & 15) * 2) * 1280 + 1024 + lane * 16, lds0 + L_RTB + 6144);
    glds1(wd, ((cw >> 4) * 96 + 32 + (cw & 15) * 2) * 1280 + 2048 + (lane & 31) * 16, lds0 + L_RTB + 6144);
  } else {
    if (wid == 5) { __builtin_amdgcn_s_setprio(3); waitv<20>(); } else waitv<0>();
    float pv[1][4], pn[4];
    lds_params(pv[0], lane); lds_params(pn, 64 + lane);
    unsigned tb[1] = {lds0 + pt0}, sa_ = lds0 + L_SC + (ro0 * 3 + th0) * 4, inc = wid < 3;
    float ldt[1] = {ldt0};
#pragma unroll 1
    for (int rnd = wid == 5; rnd < 2; ++rnd) {
      float sc[1];
      kpool<1>(tb, pv, ldt, false, sc);
      asm volatile("s_mov_b64 exec, 1\n\tds_write_b32 %0, %1\n\tds_add_u32 %2, %3\n\ts_mov_b64 exec, -1" :: "v"(sa_), "v"(sc[0]), "v"(fb + FL_KP * 4), "v"(inc) : "memory");
#pragma unroll
      for (int e = 0; e < 4; ++e) pv[0][e] = pn[e];
      tb[0] = lds0 + pt1; sa_ = lds0 + L_SC + (ro1 * 3 + th1) * 4; inc = 0; ldt[0] = ldt1;
    }
  }

  if (wid >= 6) {
    const int lane = fresh_lane(), r = lane & 15, q = lane >> 4;
    const float dsk = ubf(p_dsk, chA + kw);
    u32x4 xv[5];
    u64 ep = __hip_atomic_load((GAS u64*)(ws + WS_EPOCH), RLX_AGENT);
    u32x4 wt[3], wf[12];
    flag_add(fb, FL_S2, lane);
    flag_wait(fb, FL_S2, 2);
#pragma unroll
    for (int j = 0; j < 5; ++j) xv[j] = tr2(lds + L_LNB + (r >> 2) * LNR + (kw * 5 + j) * 128 + q * 32 + (lane & 3) * 8, 4 * LNR);
    BAR_L();
    {
      const int sg = (q & 1) * 32;
      lptr pa = lds + SL0 + 16384 + kw * 12288 + (8 * q + (r >> 2)) * 192 + 8 * (lane & 3);
#pragma unroll
      for (int t = 0; t < 12; ++t) wf[t] = tr2(pa + (t / 6) * 6144 + (t % 6) * 32 + ((t & 1) ? -sg : sg), 768);
    }
    lptr pool = lds + SA + kw * PCH;
    KPtr kp; kp.init(pool, lane);
    u32x4 kv[2][6], kq[4];
    kp.fetch(kv[0], 0); kp.fetch(kv[1], 64);
    flag_wait(fb, FL_RT, 1);
#pragma unroll
    for (int j = 0; j < 3; ++j) wt[j] = tr2(lds + L_RTB + (4 * q + (r >> 2)) * 160 + (kw * 2 + j) * 32 + (lane & 3) * 8, 2560);
    BAR_L();
    tag = uni64(ep) + 1;
    *(volatile LAS u64*)(lds + L_FLG + FL_TAG * 4) = tag;
    f32x4 ko[3], ka[3][3];
    u32x4 f0 = {0, 0, 0, 0}, f1 = {0, 0, 0, 0};
    constexpr int HS0[9] = {0, 2, 3, 7, 10, 14, 17, 20, 24};
#pragma unroll
    for (int sl = 0; sl < 8; ++sl) {
      if (sl == 6) { f0 = LD_SC1(WS_F0 + (kw * 128 + lane) * FBS); f1 = LD_SC1(WS_F0 + (kw * 128 + 64 + lane) * FBS); SBAR(); }
      if (sl == 3 && kw == 0) PUBLISH(WS_F0 + cw * FBS);
#pragma unroll
      for (int hs = HS0[sl]; hs < HS0[sl + 1]; ++hs) {
        const int k = hs >> 1;
        if (!(hs & 1)) kprodh<0>(kv[k & 1], kq);
        else { kprodh<1>(kv[k & 1], kq); if (k + 2 < 12) kp.fetch(kv[k & 1], 64 * (k + 2)); kmma1(kq, ka[k >> 2], (k & 3) == 0); }
      }
      if (sl == 0) {
#pragma unroll
        for (int j = 0; j < 5; ++j) ST_SC1(xv[j], WS_XB + lnb * 10240 + (kw * 5 + j) * 1024 + lane * 16);
      }
      if (sl == 1) {
        if (csum) {
          const u32x4 ones = {0x3f803f80u, 0x3f803f80u, 0x3f803f80u, 0x3f803f80u};
          f32x4 cs[6];
          SBAR(); __builtin_amdgcn_s_setprio(3); SBAR();
#pragma unroll
          for (int t = 0; t < 6; ++t) cs[t] = MFMA_BF(ones, wf[t], (f32x4{0, 0, 0, 0}));
#pragma unroll
          for (int t = 0; t < 6; ++t) cs[t] = MFMA_BF(ones, wf[6 + t], cs[t]);
          SBAR(); __builtin_amdgcn_s_setprio(0); SBAR();
          const float cs0 = q == 0 ? cs[0][0] : q == 1 ? cs[1][0] : q == 2 ? cs[2][0] : cs[3][0], cs1 = q == 0 ? cs[4][0] : cs[5][0];
          const unsigned co = WS_CS + (cb * 10 + rot) * 768 + kw * 384 + lane * 4;
          __builtin_amdgcn_raw_buffer_store_b32(asu(cs0), rs, co, 0, SC1);
          if (lane < 32) __builtin_amdgcn_raw_buffer_store_b32(asu(cs1), rs, co + 256, 0, SC1);
        }
#pragma unroll
        for (int f = 0; f < 3; ++f) if (f || !kw) ST_SC1(wt[f], WS_WD + ((cw & 7) * 48 + (cw >> 4) * 3 + ((cw >> 3) & 1)) * 5120 + (kw * 2 + f) * 1024 + lane * 16);
      }
      if (sl == 2) waitv<0>();
      if (sl == 6) {
        const unsigned tl = (unsigned)tag, th = (unsigned)(tag >> 32);
        _Pragma("nounroll") for (int sp = 0; sp < SPIN_GLB; ++sp) {
          if (wall(((f0[0] ^ tl) | (f0[1] ^ th) | (f0[2] ^ tl) | (f0[3] ^ th) | (f1[0] ^ tl) | (f1[1] ^ th) | (f1[2] ^ tl) | (f1[3] ^ th)) == 0u)) break;
          asm volatile("" ::: "memory");
          f0 = LD_SC1(WS_F0 + (kw * 128 + lane) * FBS); f1 = LD_SC1(WS_F0 + (kw * 128 + 64 + lane) * FBS);
          __builtin_amdgcn_s_sleep(2);
        }
      }
      BAR();
    }
    kcombine(ko, ka, *(LAS float*)(lds + L_SC + kw * 12), *(LAS float*)(lds + L_SC + kw * 12 + 4), *(LAS float*)(lds + L_SC + kw * 12 + 8));
    kstore(pool + O_KL, lane, ko, dsk);
    kexpand(pool + O_KREV, pool + O_KL, lane);
    flag_set(fb, FL_KA + kw, 1);
  } else {
    if (wid >= 4) {
      const int lane = fresh_lane();
      __builtin_amdgcn_s_setprio(0);
      u32x4 cv[6][6];
      {
        KPtr kp; kp.init(lds + SL2 + lw * 384, lane);
        flag_wait(fb, FL_KP, 3);
#pragma unroll
        for (int i = 0; i < 6; ++i) kp.fetch(cv[i], 64 * i);
      }
      const float sca = *(LAS float*)(lds + L_SC + 24 + lw * 4), scb = *(LAS float*)(lds + L_SC + 24 + lw * 8), scc = *(LAS float*)(lds + L_SC + 28 + lw * 4);
      SBAR();
      prep_dma(fresh_lane());
      if (lw) { issue(1, SL1, 1); waitv<20>(); } else waitv<28>();
      BAR_L();
      if (!lw) { waitv<0>(); flag_set(fb, FL_RT, 1); }
      issue(2, SL2, lw, 1);
      f32x4 ka[3][3], kc[3];
#pragma unroll
      for (int s = 0; s < 7; ++s) {
        if (s == 0) waitv<12>(); else waitv<20>();
        BAR();
        if (s == 0) issue(2, SL2, lw, 2);
        issue(s + 3, s % 3 == 0 ? SL0 : s % 3 == 1 ? SL1 : SL2, lw);
        if (s < 6) { u32x4 o[4]; kprod(cv[s], o); kmma1(o, ka[s >> 1], !(s & 1)); }
      }
      kcombine(kc, ka, sca, scb, scc);
      if (lw == 0) {
#pragma unroll
        for (int i = 0; i < 3; ++i) *(LAS f32x4*)(lds + L_STG + i * 1024 + lane * 16) = kc[i];
      }
      waitv<20>();
      BAR_L();
      tag = uni64(*(volatile LAS u64*)(lds + L_FLG + FL_TAG * 4));
      u32x4 uc[8]; unsigned uprev;
      const int n0 = lw * 128 + (lane >> 2) * 8;
      const unsigned uo = WS_XB + n0 * 10240 + (c0 + (lane & 3)) * 16;
      u32x4 sv, cp[10];
      if (lw == 0) {
        sv = LD_SC1(WS_ST + rb * 1024 + lane * 16);
#pragma unroll
        for (int j = 0; j < 10; ++j) cp[j] = LD_SC1(WS_CS + (cb * 10 + j) * 768 + (lane < 48 ? lane : 0) * 16);
      }
#pragma unroll
      for (int j = 0; j < 8; ++j) uc[j] = LD_SC1(uo + j * 10240);
      uprev = __builtin_amdgcn_raw_buffer_load_b32(rs, uo - 10240 + 12, 0, SC1);
      SBAR();
      if (lw) {
#pragma unroll
        for (int i = 0; i < 3; ++i) kc[i] += *(LAS f32x4*)(lds + L_STG + i * 1024 + lane * 16);
        kstore(lds + L_PC, lane, kc, ubf(p_dsk, chC));
        waitv<9>();
      } else waitv<20>();
      BAR();
      if (lw == 0) {
        f32x4 cs = {0, 0, 0, 0};
#pragma unroll
        for (int j = 0; j < 10; ++j) cs += __builtin_bit_cast(f32x4, cp[j]);
        st16(lds + L_STAT + lane * 16, sv);
        if (lane < 48) *(LAS f32x4*)(lds + L_CS + lane * 16) = cs;
        flag_set(fb, FL_EPI, 1);
        if (bid == 0 && lane == 0) __hip_atomic_store((GAS u64*)(ws + WS_EPOCH), tag, RLX_AGENT);
      }
      if (n0 == 0) uprev = 0;
      if ((lane & 3) < 3) {
        lptr ud = lds + L_U + (lane & 3) * UCH + 448 + n0 * 16;
#pragma unroll
        for (int j = 0; j < 8; ++j) {
          st16(ud + j * 16, uc[j]);
          const unsigned pw_ = j ? uc[j - 1][3] : uprev;
          st16(ud + UC1 + j * 16, u32x4{__builtin_amdgcn_alignbit(uc[j][0], pw_, 16), __builtin_amdgcn_alignbit(uc[j][1], uc[j][0], 16),
                                        __builtin_amdgcn_alignbit(uc[j][2], uc[j][1], 16), __builtin_amdgcn_alignbit(uc[j][3], uc[j][2], 16)});
        }
      }
      if (lane < 28) {
#pragma unroll
        for (int c = 0; c < 3; ++c) st16(lds + L_U + c * UCH + lw * UC1 + lane * 16, u32x4{0, 0, 0, 0});
      }
      flag_add(fb, FL_U, lane);
      if (lw) { kexpand(lds + L_KC, lds + L_PC, lane); flag_set(fb, FL_KC, 1); }

    } else {
      f32x4 acc[2][3][4];
      __builtin_amdgcn_s_setprio(2);
      const int tw = wid >> 1, fw = wid & 1;
      lptr pB0 = lds + (tw * 64 + r) * 128 + ((q ^ (r >> 1)) << 4);
      lptr pB1 = lds + (tw * 64 + r) * 128 + (((q ^ (r >> 1)) ^ 4) << 4);
      const int sg = (q & 1) * 32;
      lptr pAb = lds + 16384 + (8 * q + (r >> 2)) * 192 + 8 * (lane & 3);
      lptr pA[3] = {pAb + (fw ? 160 - sg : sg), pAb + (fw ? 128 + sg : 32 - sg), pAb + (fw ? 96 - sg : 64 + sg)};
      u32x4 A0[2][3], B0[4], A1[2][3], B1[4];
      auto rd = [&](u32x4 (&A)[2][3], u32x4 (&B)[4], int off, int kk) {
#pragma unroll
        for (int tb = 0; tb < 4; ++tb) B[tb] = ld16((kk ? pB1 : pB0) + off + tb * 2048);
#pragma unroll
        for (int m = 0; m < 2; ++m)
#pragma unroll
          for (int ii = 0; ii < 3; ++ii) A[m][ii] = tr2(pA[ii] + off + m * 12288 + kk * 6144, 768);
      };
      auto mm = [&](u32x4 (&A)[2][3], u32x4 (&B)[4]) {
#pragma unroll
        for (int e = 0; e < 24; ++e) { const int m = e / 12, ii = (e / 4) % 3, tb = e % 4; acc[m][ii][tb] = MFMA_BF(A[m][ii], B[tb], acc[m][ii][tb]); }
      };
      auto sched = [&]() {
#pragma unroll
        for (int k = 0; k < 16; ++k) { SGB(0x8, 1); SGB(0x100, 1); }
        SGB(0x8, 8);
      };
      BAR_L();
      rd(A0, B0, SL0, 0);
      SBAR();
      rd(A1, B1, SL0, 1);
#pragma unroll
      for (int e = 0; e < 24; ++e) { const int m = e / 12, ii = (e / 4) % 3, tb = e % 4; acc[m][ii][tb] = MFMA_BF(A0[m][ii], B0[tb], (f32x4{0, 0, 0, 0})); }
      sched();
      int s0 = SL1, s1 = SL2, s2 = SL0;
#pragma unroll 1
      for (int s = 0; s < 9; ++s) {
        BAR_L();
        rd(A0, B0, s0, 0); mm(A1, B1); sched();
        SBAR();
        rd(A1, B1, s0, 1); mm(A0, B0); sched();
        const int t = s0; s0 = s1; s1 = s2; s2 = t;
      }
      SBAR();
      asm volatile("s_waitcnt lgkmcnt(0)\n\ts_mov_b64 exec, 1\n\tds_add_u32 %0, %1\n\ts_mov_b64 exec, -1" :: "v"(fb + FL_G1 * 4), "v"(1u) : "memory");
      auto mlast = [&](int ii, int tb) { acc[0][ii][tb] = MFMA_BF(A1[0][ii], B1[tb], acc[0][ii][tb]); acc[1][ii][tb] = MFMA_BF(A1[1][ii], B1[tb], acc[1][ii][tb]); };
      mlast(0, 0); mlast(1, 0); mlast(0, 1); mlast(1, 1);
      SBAR();
      flag_wait(fb, FL_EPI, 1);
      {
        f32x2 st[4];
#pragma unroll
        for (int tb = 0; tb < 4; ++tb) st[tb] = *(LAS f32x2*)(lds + L_STAT + (tw * 64 + tb * 16 + r) * 8);
        lptr pcs = lds + L_CS + (fw * 48 + 4 * q) * 4;
        const int oa = fw ? 128 : 0, oc = fw ? 0 : 128;
        const f32x4 cga = *(LAS f32x4*)(pcs + oa), cua = *(LAS f32x4*)(pcs + 384 + oa), cgb = *(LAS f32x4*)(pcs + 64), cub = *(LAS f32x4*)(pcs + 384 + 64);
        const u64 tv = *(volatile LAS u64*)(lds + L_FLG + FL_TAG * 4);
        SBAR();
        mlast(0, 2); mlast(1, 2); mlast(0, 3); mlast(1, 3);
        SBAR();
        tag = uni64(tv);
        float nmb[4], al[4], sd[4];
#pragma unroll
        for (int tb = 0; tb < 4; ++tb) { nmb[tb] = -st[tb].x; sd[tb] = st[tb].y; al[tb] = -1.44269504f * __builtin_amdgcn_rcpf(st[tb].y); }
        const unsigned hoff = WS_H + ((rb * 2 + tw) * 16 + cb) * 12288 + lane * 16;
        auto hcalc = [&](const f32x4& g4, const f32x4& u4, const f32x4& cg, const f32x4& cu, int tb) {
          float hv[4];
#pragma unroll
          for (int j = 0; j < 4; ++j) {
            const float g = __builtin_fmaf(nmb[tb], cg[j], g4[j]) * al[tb];
            const float u = __builtin_fmaf(nmb[tb], cu[j], u4[j]);
            const float e = __builtin_amdgcn_exp2f(g);
            hv[j] = u * __builtin_amdgcn_rcpf(__builtin_fmaf(e, sd[tb], sd[tb]));
          }
          return u32x2{pkbf(hv[0], hv[1]), pkbf(hv[2], hv[3])};
        };
#pragma unroll
        for (int tb = 0; tb < 4; ++tb) {
          const u32x2 ha = hcalc(acc[0][0][tb], acc[1][0][tb], cga, cua, tb);
          const u32x2 hb = hcalc(acc[0][1][tb], acc[1][1][tb], cgb, cub, tb);
          ST_SC1((u32x4{ha[0], ha[1], hb[0], hb[1]}), hoff + fw * 4096 + tb * 1024);
          SBAR();
        }
        mlast(2, 0); mlast(2, 1); mlast(2, 2); mlast(2, 3);
        SBAR();
        {
          const f32x4 cg = *(LAS f32x4*)(pcs + oc), cu = *(LAS f32x4*)(pcs + 384 + oc);
          u32x2 hs[4];
#pragma unroll
          for (int tb = 0; tb < 4; ++tb) { hs[tb] = hcalc(acc[0][2][tb], acc[1][2][tb], cg, cu, tb); asm volatile("" : "+v"(hs[tb])); SBAR(); }
          waitv<0>();
          PUBLISH(WS_H1 + (((rb * 2 + tw) * 16 + cb) * 2 + fw) * FBS);
#pragma unroll
          for (int t2 = 0; t2 < 2; ++t2) ST_SC1((u32x4{hs[2 * t2][0], hs[2 * t2][1], hs[2 * t2 + 1][0], hs[2 * t2 + 1][1]}), hoff + 8192 + fw * 2048 + t2 * 1024);
        }
      }
    {
    const int sw = wid ^ 2, hm = sw & 1;
    lptr am = lds + SA + hm * PCH;
    f32x4 ca[8];
#pragma unroll
    for (int t = 0; t < 8; ++t) ca[t] = f32x4{0, 0, 0, 0};
    lptr pmain = am + O_KL + 1312 + lane * 16;
    flag_wait(fb, FL_KA + hm, 1);
    flag_wait(fb, FL_U, 2);
    conv_units<0xff>(am + O_KREV, lds + L_U + (hm + odd) * UCH, lane, sw < 2 ? 5 : 0, sw < 2 ? 3 : 5, ca, [&](int it) {
      if (it == 0) { waitv<0>(); PUBLISH(WS_H2 + (((rb * 2 + tw) * 16 + cb) * 2 + fw) * FBS); } });
    if (sw < 2) {
      lptr pc = lds + L_PC;
#pragma unroll
      for (int t = 0; t < 8; ++t) { *(LAS f32x4*)(pmain + t * 1024) = ca[t]; ca[t] = f32x4{0, 0, 0, 0}; }
      flag_set(fb, FL_PA + hm, 1);
      flag_wait(fb, FL_KC, 1);
      lptr uc_ = lds + L_U + (odd ? 0 : 2) * UCH;
      if (odd) conv_units<0x3a>(lds + L_KC, uc_, lane, hm * 4, 4, ca, [](int) {});
      else conv_units<0xc5>(lds + L_KC, uc_, lane, hm * 4, 4, ca, [](int) {});
      f32x4 c4[4];
      c4[0] = odd ? ca[1] : ca[0]; c4[1] = odd ? ca[3] : ca[2]; c4[2] = odd ? ca[4] : ca[6]; c4[3] = odd ? ca[5] : ca[7];
      {
        lptr po = pc + hm * 2048, pi = pc + (hm ^ 1) * 2048;
        *(LAS f32x4*)(po + lane * 16) = hm ? c4[0] : c4[2]; *(LAS f32x4*)(po + 1024 + lane * 16) = hm ? c4[1] : c4[3];
        flag_add(fb, FL_PC, lane);
        flag_wait(fb, FL_PC, 2);
        const f32x4 d0 = (hm ? c4[2] : c4[0]) + *(LAS f32x4*)(pi + lane * 16), d1 = (hm ? c4[3] : c4[1]) + *(LAS f32x4*)(pi + 1024 + lane * 16);
        *(LAS unsigned*)(pi + q * 128 + r * 4) = pkbf(d0[0], d0[1]); *(LAS unsigned*)(pi + q * 128 + r * 4 + 64) = pkbf(d0[2], d0[3]);
        *(LAS unsigned*)(pi + 512 + q * 128 + r * 4) = pkbf(d1[0], d1[1]); *(LAS unsigned*)(pi + 512 + q * 128 + r * 4 + 64) = pkbf(d1[2], d1[3]);
        const int T = ((odd ? 0x5431 : 0x7620) >> (4 * (2 * hm + (lane >> 5)))) & 7;
        ST_SC1(ld16(pi + lane * 16), WS_Y + ((T * 4 + ((lane >> 3) & 3)) * DM + chC) * 128 + (lane & 7) * 16);
      }
    } else {
      flag_wait(fb, FL_PA + hm, 1);
#pragma unroll
      for (int t = 0; t < 8; ++t) ca[t] += *(LAS f32x4*)(pmain + t * 1024);
#pragma unroll
      for (int t = 0; t < 8; ++t) { *(LAS unsigned*)(am + t * 512 + q * 128 + r * 4) = pkbf(ca[t][0], ca[t][1]); *(LAS unsigned*)(am + t * 512 + q * 128 + r * 4 + 64) = pkbf(ca[t][2], ca[t][3]); }
#pragma unroll
      for (int i = 0; i < 4; ++i) ST_SC1(ld16(am + i * 1024 + lane * 16), WS_Y + ((i * 8 + (lane >> 3)) * DM + chA + hm) * 128 + (lane & 7) * 16);
    }
    waitv<0>();
    if (flag_add(fb, FL_YC, lane) == 3u) {
      PUBLISH(WS_FY + cw * FBS);
      __builtin_amdgcn_s_sleep(16);
      _Pragma("nounroll") for (int sp = 0; sp < SPIN_GLB; ++sp) { if (wall(gflag_ok(ws, WS_FY + (nb * 32 + (lane & 31)) * FBS, tag))) break; __builtin_amdgcn_s_sleep(8); }
      flag_set(fb, FL_YOK, 1);
    }
    flag_wait(fb, FL_YOK, 1);
    u32x4 yv[3];
#pragma unroll
    for (int j = 0; j < 3; ++j) {
      const int p = umin(sw + 4 * j, 9), c = p * 64 + lane, row = (c * 6554) >> 16;
      glds1((const char*)x + (size_t)tg * 64 * 1280 + nb * 160, row * 1280 + (c - row * 10) * 16, lds0 + L_XT + p * 1024);
    }
#pragma unroll
    for (int j = 0; j < 3; ++j) { const int p = umin(sw + 4 * j, 9); yv[j] = LD_SC1(WS_Y + (tg * DM + nb * 80 + p * 8) * 128 + lane * 16); }
    lptr yb = lds + L_YB + sw * 3072;
#pragma unroll
    for (int j = 0; j < 3; ++j) st16(yb + j * 1024 + lane * 16, yv[j]);
#pragma unroll
    for (int j = 0; j < 3; ++j) {
      const u32x4 a = tr2(yb + j * 1024 + (r >> 2) * 128 + q * 32 + (lane & 3) * 8, 512);
      if (sw + 4 * j < 10) st16(lds + L_BASE + lane * 160 + (sw + 4 * j) * 16, a);
    }
    }
      __builtin_amdgcn_s_setprio(0);
    }
  }

  if (wid >= 4) {
    const int sw = wid - 4;
    const int lane = fresh_lane();
    {
      unsigned bo[4], ao[4];
#pragma unroll
      for (int c = 0; c < 4; ++c) { const int cc = sw * 4 + ((c + nb) & 3); bo[c] = WS_WD + (nb * 48 + cc * 3) * 5120; ao[c] = WS_H + (tg * 16 + cc) * 12288; }
      const unsigned l16 = lane * 16;
#define BFRAG(k, n) __builtin_amdgcn_raw_buffer_load_b128(rs, l16, bo[(k) < 8 ? (k) >> 1 : (k) - 8] + ((k) < 8 ? ((k) & 1) * 5120 : 10240) + (n) * 1024, SC1)
#define AFRAG(k, t) __builtin_amdgcn_raw_buffer_load_b128(rs, l16, ao[(k) < 8 ? (k) >> 1 : (k) - 8] + ((k) < 8 ? ((k) & 1) * 4096 : 8192) + (t) * 1024, SC1)
      u32x4 Bw[4][5];
#pragma unroll
      for (int k = 0; k < 4; ++k)
#pragma unroll
        for (int n = 0; n < 5; ++n) Bw[k][n] = BFRAG(k, n);
      wd_dma(lane, sw, 0, 2);
      flag_wait(fb, FL_G1, 4);
      wd_dma(lane, sw, 2, 4);
      lptr park = lds + SL2 + sw * 10240 + (lane >> 2) * 160 + (lane & 3) * 8;
      asm volatile("" : "+v"(park));
      const unsigned fo = WS_H1 + (tg * 32 + sw * 8 + (lane & 7)) * FBS;
      _Pragma("nounroll") for (int sp = 0; sp < SPIN_GLB; ++sp) { if (wall(gflag_ok(ws, fo, tag))) break; __builtin_amdgcn_s_sleep(2); }
      asm volatile("" ::: "memory");
      f32x4 a2[5][4];
#pragma unroll
      for (int n = 0; n < 5; ++n)
#pragma unroll
        for (int tb = 0; tb < 4; ++tb) a2[n][tb] = f32x4{0, 0, 0, 0};
      u32x4 Ah[4][4];
#pragma unroll
      for (int k = 0; k < 3; ++k)
#pragma unroll
        for (int tb = 0; tb < 4; ++tb) Ah[k][tb] = AFRAG(k, tb);
      u64 f2 = 0;
      SBAR();
#pragma unroll
      for (int k = 0; k < 12; ++k) {
        if (k == 3) { f2 = __hip_atomic_load((GAS u64*)(ws + fo + 1024 * FBS), RLX_AGENT); SBAR(); }
        if (k == 5) {
          unsigned tc;
          asm volatile("s_mov_b32 %1, %5\n\t1:\n\tv_cmp_ne_u64 vcc, %2, %0\n\ts_cbranch_vccz 2f\n\ts_sleep 2\n\t"
                       "global_load_dwordx2 %0, %3, %4 sc1\n\ts_sub_u32 %1, %1, 1\n\ts_waitcnt vmcnt(0)\n\ts_cbranch_scc0 1b\n\t2:"
                       : "+v"(f2), "=&s"(tc) : "s"(tag), "v"(fo), "s"(ws + 1024 * FBS), "i"(SPIN_GLB) : "memory", "vcc", "scc");
          SBAR();
        }
        u32x4 hf[4];
        if (k >= 8) {
          const u32x4 X0 = Ah[k & 3][0], X1 = Ah[k & 3][1], Y0 = Ah[k & 3][2], Y1 = Ah[k & 3][3];
          hf[0] = u32x4{X0[0], X0[1], Y0[0], Y0[1]}; hf[1] = u32x4{X0[2], X0[3], Y0[2], Y0[3]};
          hf[2] = u32x4{X1[0], X1[1], Y1[0], Y1[1]}; hf[3] = u32x4{X1[2], X1[3], Y1[2], Y1[3]};
        } else {
#pragma unroll
          for (int tb = 0; tb < 4; ++tb) hf[tb] = Ah[k & 3][tb];
        }
#pragma unroll
        for (int e = 0; e < 20; ++e) {
          const int n = e >> 2, tb = e & 3;
          a2[n][tb] = MFMA_BF(Bw[k & 3][n], hf[tb], a2[n][tb]);
          if (e & 1) {
            const int j = e >> 1;
            if (j < 4) { if (k + 3 < 12) Ah[(k + 3) & 3][j] = AFRAG(k + 3, j); }
            else if (j < 9 && k >= 1 && k + 3 < 12) {
              if (k + 3 < 8) Bw[(k + 3) & 3][j - 4] = BFRAG(k + 3, j - 4);
              else Bw[(k + 3) & 3][j - 4] = tr2(park + ((k - 5) >> 1) * (SL0 - SL2) + ((k - 5) & 1) * 5120 + (j - 4) * 32, 2560);
            }
            SBAR();
          }
        }
#pragma unroll
        for (int n = 0; n < 5; ++n) asm volatile("" : "+v"(a2[n][0]), "+v"(a2[n][1]), "+v"(a2[n][2]), "+v"(a2[n][3]));
      }
      {
        lptr red = lds + SL2 + sw * 10240;
        const int l2 = fresh_lane(), r = l2 & 15, q = l2 >> 4;
#pragma unroll
        for (int n = 0; n < 5; ++n)
#pragma unroll
          for (int tb = 0; tb < 4; ++tb) {
            *(LAS f32x4*)(red + (q >> 1) * (SL0 - SL2) + (2 * n + (q & 1)) * 1024 + ((tb * 16 + r + 3 * n) & 63) * 16) = a2[n][tb];
          }
      }
    }
  }
  BAR_L();
  if (wid < 5) {
    const int tid = wid * 64 + fresh_lane();
    const int tok = (tid * 13108) >> 16, g = tid - tok * 5;
    lptr rp = lds + SL2 + g * 2048 + ((tok + 3 * g) & 63) * 16;
    f32x4 s[4];
#pragma unroll
    for (int m = 0; m < 4; ++m) {
      lptr p = rp + (m >> 1) * (SL0 - SL2) + (m & 1) * 1024;
      s[m] = (*(LAS f32x4*)(p) + *(LAS f32x4*)(p + 10240)) + (*(LAS f32x4*)(p + 20480) + *(LAS f32x4*)(p + 30720));
    }
    u16* op = out + (size_t)(tg * 64 + tok) * DM + nb * 80 + g * 16;
#pragma unroll
    for (int hh = 0; hh < 2; ++hh) {
      const u32x4 bs = ld16(lds + L_BASE + tid * 32 + hh * 16), xs = ld16(lds + L_XT + tid * 32 + hh * 16);
      const f32x4 sa = s[2 * hh], sb = s[2 * hh + 1];
      *(u32x4*)(op + hh * 8) = u32x4{pkbf((bflo(xs[0]) + bflo(bs[0])) + sa[0], (bfhi(xs[0]) + bfhi(bs[0])) + sa[1]), pkbf((bflo(xs[1]) + bflo(bs[1])) + sa[2], (bfhi(xs[1]) + bfhi(bs[1])) + sa[3]),
                                     pkbf((bflo(xs[2]) + bflo(bs[2])) + sb[0], (bfhi(xs[2]) + bfhi(bs[2])) + sb[1]), pkbf((bflo(xs[3]) + bflo(bs[3])) + sb[2], (bfhi(xs[3]) + bfhi(bs[3])) + sb[3])};
    }
  }
  asm volatile(".Lfk_tail:");
}

#if defined(__HIP_DEVICE_COMPILE__)
#pragma clang attribute pop
#endif

extern "C" void kernel_launch(void* const* d_in, const int* in_sizes, int n_in, void* d_out, int out_size, void* d_ws, size_t ws_size, hipStream_t stream) {
  (void)in_sizes; (void)n_in; (void)out_size; (void)ws_size;
  (void)hipFuncSetAttribute((const void*)fused_kernel, hipFuncAttributeMaxDynamicSharedMemorySize, LDS_BYTES);
  fused_kernel<<<256, 512, LDS_BYTES, stream>>>((const u16*)d_in[0], (const u16*)d_in[1], (const u16*)d_in[2], (const u16*)d_in[3], (const u16*)d_in[4],
                                                (const u16*)d_in[5], (const u16*)d_in[6], (const u16*)d_in[8], (const u16*)d_in[9], (const u16*)d_in[10],
                                                (u16*)d_out, (char*)d_ws);
}
```

```cpp
#include <hip/hip_runtime.h>
#include <stdint.h>

#if defined(__HIP_DEVICE_COMPILE__)
#pragma clang attribute push(__attribute__((target("no-packed-fp32-ops"))), apply_to = function)
#endif
typedef unsigned short u16;
typedef unsigned long long u64;
typedef short bf16x8 __attribute__((ext_vector_type(8)));
typedef _Float16 f16x8 __attribute__((ext_vector_type(8)));
typedef short s16x4 __attribute__((ext_vector_type(4)));
typedef float f32x2 __attribute__((ext_vector_type(2)));
typedef float f32x4 __attribute__((ext_vector_type(4)));
typedef unsigned u32x2 __attribute__((ext_vector_type(2)));
typedef unsigned u32x4 __attribute__((ext_vector_type(4)));
typedef _Float16 h16x2 __attribute__((ext_vector_type(2)));
typedef __bf16 b16x2 __attribute__((ext_vector_type(2)));
#define LAS __attribute__((address_space(3)))
#define GAS __attribute__((address_space(1)))
typedef LAS char* lptr;
#define DI __device__ __forceinline__
#define SBAR() __builtin_amdgcn_sched_barrier(0)
#define SGB(m, n) __builtin_amdgcn_sched_group_barrier(m, n, 0)
#define MFMA_BF(a, b, c) __builtin_amdgcn_mfma_f32_16x16x32_bf16(__builtin_bit_cast(bf16x8, a), __builtin_bit_cast(bf16x8, b), c, 0, 0, 0)
#define MFMA_H(a, b, c) __builtin_amdgcn_mfma_f32_16x16x32_f16(__builtin_bit_cast(f16x8, a), __builtin_bit_cast(f16x8, b), c, 0, 0, 0)
#define SC1 16

constexpr int DM = 640, NM = 192;
constexpr unsigned FBS = 64, WS_EPOCH = 0, WS_F0 = 262144, WS_FY = WS_F0 + 256 * FBS, WS_H1 = WS_FY + 256 * FBS, WS_H2 = WS_H1 + 1024 * FBS;
constexpr unsigned WS_ST = 65536, WS_CS = 98304, WS_XB = 655360, WS_WD = WS_XB + 2621440 + 4096;
constexpr unsigned WS_H = WS_WD + 1966080, WS_Y = WS_H + 6291456, WS_END = WS_Y + 2621440;
constexpr int STAGE = 40960, SL1 = 0, SA = 40960, SL2 = 81920, SL0 = 122880, POOL_BIAS = SA;
constexpr int PROW = 784, PCH = 23 * PROW;
constexpr int KC1 = 4228, O_KREV = 128, O_KL = 8528;
constexpr int L_FLG = SA + 2 * PCH, L_SC = L_FLG + 128, L_STG = L_FLG + 192, STGW = 2304;
constexpr int L_STAT = L_STG, L_CS = L_STG + 1024;
constexpr int L_RTB = SL2 + 32768;
constexpr int L_LNT = L_STG;
constexpr int L_LNB = SL2 + 18432, LNR = 1344;
constexpr int UCH = 9232, UC1 = 4640;
constexpr int L_U = SL1 + 128, L_KC = L_U + 3 * UCH + 128, L_PC = L_KC + 8400;
constexpr int L_XT = SL1;
constexpr int L_BASE = SA, L_YB = SA + 10240;
constexpr int LDS_BYTES = 163840;
static_assert(O_KL + 1312 + 8192 <= PCH && L_LNB + 8 * LNR <= L_RTB && L_RTB + 8192 <= SL0 && L_PC + 4096 <= SA && L_STG + 2 * STGW <= SL0 && (L_KC & 15) == 0 && (L_PC & 15) == 0 && (L_STG & 15) == 0, "lds map");
enum { FL_EPI = 0, FL_U, FL_KA, FL_KB, FL_KC, FL_KP, FL_PA, FL_PB, FL_PC, FL_YC, FL_YOK, FL_G1, FL_S2, FL_RT, FL_TAG = 16 };
constexpr int SPIN_LDS = 1 << 20, SPIN_GLB = 1 << 17;

DI bool wall(bool p) { return __builtin_amdgcn_ballot_w64(!p) == 0; }
DI unsigned umin(unsigned a, unsigned b) { return a < b ? a : b; }
DI unsigned umax(unsigned a, unsigned b) { return a > b ? a : b; }
DI float asf(unsigned v) { return __builtin_bit_cast(float, v); }
DI unsigned asu(float v) { return __builtin_bit_cast(unsigned, v); }
DI float bflo(unsigned v) { return asf(v << 16); }
DI float bfhi(unsigned v) { return asf(v & 0xffff0000u); }
DI float bf1(u16 v) { return asf((unsigned)v << 16); }
DI unsigned pkbf(float lo, float hi) { return __builtin_bit_cast(unsigned, __builtin_convertvector(f32x2{lo, hi}, b16x2)); }
DI float rbf(float x) { return bflo(pkbf(x, x)); }
template <int CTRL> DI float dppf(float x) { return __builtin_bit_cast(float, __builtin_amdgcn_mov_dpp(__builtin_bit_cast(int, x), CTRL, 0xf, 0xf, true)); }
DI float row16_sum(float v) { v += dppf<0xB1>(v); v += dppf<0x4E>(v); v += dppf<0x124>(v); v += dppf<0x128>(v); return v; }

DI void glds4(const void* sb, unsigned v0, unsigned v1, unsigned v2, unsigned v3, unsigned lds) {
  asm volatile("s_mov_b32 m0, %5\n\ts_nop 0\n\tglobal_load_lds_dwordx4 %0, %4\n\tglobal_load_lds_dwordx4 %1, %4 offset:1024\n\t"
               "global_load_lds_dwordx4 %2, %4 offset:2048\n\tglobal_load_lds_dwordx4 %3, %4 offset:3072"
               :: "v"(v0), "v"(v1), "v"(v2), "v"(v3), "s"(sb), "s"(lds) : "memory");
}
DI void glds1(const void* sb, unsigned v0, unsigned lds) {
  asm volatile("s_mov_b32 m0, %2\n\ts_nop 0\n\tglobal_load_lds_dwordx4 %0, %1" :: "v"(v0), "s"(sb), "s"(lds) : "memory");
}
template <int N> DI void waitv() { asm volatile("s_waitcnt vmcnt(%0)" :: "i"(N) : "memory"); }
DI void waitl() { asm volatile("s_waitcnt lgkmcnt(0)" ::: "memory"); }
DI u32x2 vtr(lptr p) { return __builtin_bit_cast(u32x2, __builtin_amdgcn_ds_read_tr16_b64_v4i16((LAS s16x4*)p)); }
DI u32x4 tr2(lptr p, int d) { u32x2 lo = vtr(p), hi = vtr(p + d); return u32x4{lo[0], lo[1], hi[0], hi[1]}; }
DI u32x4 ld16(lptr p) { return *(LAS u32x4*)p; }
DI void st16(lptr p, u32x4 v) { *(LAS u32x4*)p = v; }

DI unsigned flag_ld(unsigned addr) { unsigned r; asm volatile("ds_read_b32 %0, %1\n\ts_waitcnt lgkmcnt(0)" : "=v"(r) : "v"(addr) : "memory"); return __builtin_amdgcn_readfirstlane(r); }
DI void flag_wait(unsigned fb, int idx, unsigned need) { _Pragma("nounroll") for (int sp = 0; sp < SPIN_LDS && flag_ld(fb + idx * 4) < need; ++sp) __builtin_amdgcn_s_sleep(1); }
DI void flag_set(unsigned fb, int idx, unsigned v) {
  asm volatile("s_waitcnt lgkmcnt(0)\n\ts_mov_b64 exec, 1\n\tds_write_b32 %0, %1\n\ts_mov_b64 exec, -1" :: "v"(fb + idx * 4), "v"(v) : "memory"); }
DI unsigned flag_add(unsigned fb, int idx, int lane) {
  unsigned r; (void)lane;
  asm volatile("s_waitcnt lgkmcnt(0)\n\ts_mov_b64 exec, 1\n\tds_add_rtn_u32 %0, %1, %2\n\ts_mov_b64 exec, -1\n\ts_waitcnt lgkmcnt(0)" : "=&v"(r) : "v"(fb + idx * 4), "v"(1u) : "memory");
  return __builtin_amdgcn_readfirstlane(r);
}
#define RLX_AGENT __ATOMIC_RELAXED, __HIP_MEMORY_SCOPE_AGENT
DI bool gflag_ok(const char* ws, unsigned off, u64 tag) { return __hip_atomic_load((GAS u64*)(ws + off), RLX_AGENT) == tag; }

DI f32x2 csq(f32x2 x) { const float t = x.x + x.x; return f32x2{__builtin_fmaf(x.x, x.x, -(x.y * x.y)), t * x.y}; }
DI h16x2 toh(f32x2 v) { return __builtin_convertvector(v, h16x2); }
DI h16x2 hmul(h16x2 x, h16x2 y) { return __builtin_elementwise_fma(h16x2{x.y, x.y}, h16x2{-y.y, y.x}, h16x2{x.x, x.x} * y); }
DI f32x2 cexp_p(float a2, float bh, float bl, float p) {
  const float e = __builtin_amdgcn_exp2f(a2 * p);
  const float ph = __builtin_fmaf(bl, p, __builtin_amdgcn_fractf(bh * p));
  return f32x2{e * __builtin_amdgcn_cosf(ph), e * __builtin_amdgcn_sinf(ph)};
}
template <int NS, int ROW0> DI void put4(const unsigned (&tb)[NS], const h16x2 (&a)[NS], const h16x2 (&b)[NS], const h16x2 (&c)[NS], const h16x2 (&d)[NS]) {
#pragma unroll
  for (int e = 0; e < NS; ++e)
    asm volatile("s_mov_b32 m0, %4\n\ts_nop 0\n\tds_write_addtid_b32 %0 offset:%5\n\tds_write_addtid_b32 %1 offset:%5+784\n\t"
                 "ds_write_addtid_b32 %2 offset:%5+1568\n\tds_write_addtid_b32 %3 offset:%5+2352"
                 :: "v"(a[e]), "v"(b[e]), "v"(c[e]), "v"(d[e]), "s"(tb[e]), "i"(ROW0 * PROW + POOL_BIAS) : "memory");
}
#define FORS _Pragma("unroll") for (int e = 0; e < NS; ++e)
template <int NS> DI void kpool(const unsigned (&tb)[NS], const float (&pv)[NS][4], const float (&ldt)[NS], bool join, float (&sc)[NS]) {
  float dt[NS];
  FORS dt[e] = rbf(__builtin_amdgcn_exp2f(ldt[e] * 1.44269504f));
  unsigned eb[NS]; float cs[NS];
  FORS {
    unsigned mi = umax(asu(pv[e][2]) & 0x7fffffffu, asu(pv[e][3]) & 0x7fffffffu);
    mi = umax(mi, asu(dppf<0xB1>(asf(mi)))); mi = umax(mi, asu(dppf<0x4E>(asf(mi)))); mi = umax(mi, asu(dppf<0x124>(asf(mi)))); mi = umax(mi, asu(dppf<0x128>(asf(mi))));
    eb[e] = umax(umax((unsigned)__builtin_amdgcn_readlane(mi, 0), (unsigned)__builtin_amdgcn_readlane(mi, 16)),
                 umax((unsigned)__builtin_amdgcn_readlane(mi, 32), (unsigned)__builtin_amdgcn_readlane(mi, 48))) >> 23;
  }
  if (join) eb[0] = eb[NS - 1] = umax(eb[0], eb[NS - 1]);
  FORS { eb[e] = umin(umax(eb[e], 2u), 252u); cs[e] = asf((254u - eb[e]) << 23); }
  float a2[NS], bh[NS], bl[NS];
  FORS {
    const float ea = rbf(__builtin_amdgcn_exp2f(pv[e][0] * 1.44269504f));
    a2[e] = -(ea * dt[e]) * 1.44269504f;
    const float b = pv[e][1] * dt[e];
    bh[e] = b * 0.15915494f;
    bl[e] = __builtin_fmaf(b, 6.4206382e-9f, __builtin_fmaf(b, 0.15915494f, -bh[e]));
  }
  h16x2 one[NS], w8[NS], w16[NS], w24[NS], w32[NS], w64[NS], w96[NS], w1[NS], w2[NS], w3[NS], w4[NS], C[NS], c4[NS];
  FORS {
    const f32x2 f1 = cexp_p(a2[e], bh[e], bl[e], 1.f), f2 = csq(f1), f4 = csq(f2);
    const f32x2 f8 = csq(f32x2{f4.x, -f4.y}), f16 = csq(f8), f32 = csq(f16), f64 = csq(f32);
    one[e] = h16x2{(_Float16)1.f, (_Float16)0.f};
    w8[e] = toh(f8); w16[e] = toh(f16); w32[e] = toh(f32); w64[e] = toh(f64); w24[e] = hmul(w8[e], w16[e]); w96[e] = hmul(w32[e], w64[e]);
    w1[e] = toh(f1); w2[e] = toh(f2); w4[e] = toh(f4); w3[e] = hmul(w1[e], w2[e]);
    C[e] = toh(f32x2{pv[e][2] * cs[e], pv[e][3] * cs[e]}); c4[e] = hmul(C[e], w4[e]);
  }
  put4<NS, 0>(tb, one, w8, w16, w24);
  put4<NS, 4>(tb, w32, w64, w96, w1);
  put4<NS, 8>(tb, w2, w3, C, c4);
  h16x2 c128[NS], c132[NS], w256[NS], w258[NS], w512[NS], w768[NS], w1024[NS], w1280[NS], w1536[NS], w1792[NS], w1796[NS];
  FORS {
    const f32x2 f128 = cexp_p(a2[e], bh[e], bl[e], 128.f), f256 = csq(f128), f512 = csq(f256);
    const h16x2 w128 = toh(f128);
    w256[e] = toh(f256); w512[e] = toh(f512); c128[e] = hmul(C[e], w128); c132[e] = hmul(c4[e], w128);
    w258[e] = hmul(w256[e], w2[e]); w768[e] = hmul(w512[e], w256[e]);
    w1024[e] = toh(csq(f512)); w1280[e] = hmul(w1024[e], w256[e]); w1536[e] = hmul(w1024[e], w512[e]);
    w1792[e] = hmul(w1024[e], w768[e]); w1796[e] = hmul(w1792[e], w4[e]);
  }
  put4<NS, 12>(tb, c128, c132, w256, w258);
  put4<NS, 16>(tb, w512, w768, w1024, w1280);
  put4<NS, 19>(tb, w1280, w1536, w1792, w1796);
  FORS sc[e] = 2.f * dt[e] * asf(eb[e] << 23);
}
struct KPtr {
  lptr pbx, pby, py, px0, px2;
  DI void init(lptr pool, int lane) {
    const int r = lane & 15, x = r & 3, y = r >> 2;
    lptr p = pool + (lane >> 4) * 16;
    pbx = p + x * PROW; pby = p + (y ? 3 + y : 0) * PROW; py = p + (10 + y) * PROW; px0 = p + (x ? 6 + x : 0) * PROW; px2 = p + (y == 3 ? 22 : 18 + x) * PROW;
  }
  DI void fetch(u32x4 (&v)[6], int o) const {
    v[0] = ld16(pbx + o); v[1] = ld16(pby + o); v[2] = ld16(py + o); v[3] = ld16(px0 + o); v[4] = ld16(pbx + 14 * PROW + o); v[5] = ld16(px2 + o);
  }
};
DI void kprod(const u32x4 (&v)[6], u32x4 (&o)[4]) {
  unsigned t[16];
#pragma unroll
  for (int i = 0; i < 16; ++i) { const unsigned xe = v[(i >> 2) ? 2 + (i >> 2) : 0][i & 3], ye = v[(i >> 2) ? 2 : 1][i & 3];
    asm("v_pk_mul_f16 %0, %1, %2 op_sel_hi:[0,1]" : "=v"(t[i]) : "v"(xe), "v"(ye)); }
  SBAR();
#pragma unroll
  for (int i = 0; i < 16; ++i) { const unsigned xe = v[(i >> 2) ? 2 + (i >> 2) : 0][i & 3], ye = v[(i >> 2) ? 2 : 1][i & 3]; unsigned z;
    asm("v_pk_fma_f16 %0, %1, %2, %3 op_sel:[1,1,0] op_sel_hi:[1,0,1] neg_lo:[0,1,0]" : "=v"(z) : "v"(xe), "v"(ye), "v"(t[i])); o[i >> 2][i & 3] = z; }
  SBAR();
}
template <int H> DI void kprodh(const u32x4 (&v)[6], u32x4 (&o)[4]) {
  unsigned t[8];
#pragma unroll
  for (int i = 8 * H; i < 8 * H + 8; ++i) { const unsigned xe = v[(i >> 2) ? 2 + (i >> 2) : 0][i & 3], ye = v[(i >> 2) ? 2 : 1][i & 3];
    asm("v_pk_mul_f16 %0, %1, %2 op_sel_hi:[0,1]" : "=v"(t[i & 7]) : "v"(xe), "v"(ye)); }
  SBAR();
#pragma unroll
  for (int i = 8 * H; i < 8 * H + 8; ++i) { const unsigned xe = v[(i >> 2) ? 2 + (i >> 2) : 0][i & 3], ye = v[(i >> 2) ? 2 : 1][i & 3]; unsigned z;
    asm("v_pk_fma_f16 %0, %1, %2, %3 op_sel:[1,1,0] op_sel_hi:[1,0,1] neg_lo:[0,1,0]" : "=v"(z) : "v"(xe), "v"(ye), "v"(t[i & 7])); o[i >> 2][i & 3] = z; }
  asm volatile("" : "+v"(o[2 * H]), "+v"(o[2 * H + 1]));
  SBAR();
}
DI void kmma1(const u32x4 (&o0)[4], f32x4 (&acc)[3], bool first) {
  SBAR(); asm volatile("s_setprio 3\n\ts_nop 1"); SBAR();
#pragma unroll
  for (int i = 0; i < 3; ++i) acc[i] = MFMA_H(o0[1 + i], o0[0], (first ? f32x4{0, 0, 0, 0} : acc[i]));
  SBAR(); __builtin_amdgcn_s_setprio(0); SBAR();
}
DI void kcombine(f32x4 (&ko)[3], const f32x4 (&ka)[3][3], float s0, float s1, float s2) {
#pragma unroll
  for (int i = 0; i < 3; ++i)
#pragma unroll
    for (int j = 0; j < 4; ++j) ko[i][j] = __builtin_fmaf(ka[2][i][j], s2, __builtin_fmaf(ka[1][i][j], s1, ka[0][i][j] * s0));
}
DI float ubf(const u16* p, int i) { const unsigned w = ((const __attribute__((address_space(4))) unsigned*)(uintptr_t)p)[i >> 1]; return (i & 1) ? bfhi(w) : bflo(w); }
DI void kstore(lptr kl, int lane, const f32x4 (&acc)[3], float dsk) {
  const int c = lane & 15, q = lane >> 4;
  LAS u16* t = (LAS u16*)kl;
  const float k0 = acc[0][0] + (lane == 0 ? dsk : 0.f);
  *(LAS u32x2*)(t + 128 * (q >> 1) + 8 * c + 4 * (q & 1)) = u32x2{pkbf(k0, acc[0][1]), pkbf(acc[0][2], acc[0][3])};
  *(LAS unsigned*)(t + 256 + 64 * (q >> 1) + 2 * (q & 1) + 4 * c) = pkbf(acc[1][0], acc[1][1]);
  const unsigned p1 = pkbf(acc[1][2], acc[1][3]);
  const int i1 = 384 + 32 * (q >> 1) + (q & 1) + 2 * c;
  t[i1] = (u16)p1; t[i1 + 64] = (u16)(p1 >> 16);
  const unsigned r0 = pkbf(acc[2][0], acc[2][1]), r1 = pkbf(acc[2][2], acc[2][3]);
  if (!(q & 1)) { const int i2 = 512 + 8 * q + c; t[i2] = (u16)r0; t[i2 + 32] = (u16)(r0 >> 16); t[i2 + 64] = (u16)r1; t[i2 + 96] = (u16)(r1 >> 16); }
  else if (lane == 63) t[640] = (u16)r0;
}
DI unsigned dup16(unsigned a) { return a | (a << 16); }
DI unsigned bsel(unsigned m, unsigned a, unsigned b) { asm("" : "+v"(m)); return (a & m) | (b & ~m); }
DI unsigned rot16(unsigned a) { return __builtin_amdgcn_alignbit(a, a, 16); }
DI void kexpand(lptr kr, lptr kl, int lane) {
  LAS u16* t = (LAS u16*)kl;
  auto put = [&](int ch, unsigned dp, unsigned d0, unsigned d1, unsigned d2, unsigned d3) {
    st16(kr + 16 * ch, u32x4{d0, d1, d2, d3}); st16(kr + KC1 - 4 + 16 * ch, u32x4{dp, d0, d1, d2}); };
  const int n = 63 - lane, n0 = n & 31;
  const unsigned a0 = t[576 + n], b0 = t[577 + n], a1 = t[512 + n], b1 = t[513 + n];
  const unsigned ab = *(LAS unsigned*)(t + 384 + 2 * n), c2 = t[386 + 2 * n];
  const u32x2 v1 = *(LAS u32x2*)(t + 256 + 4 * n0); const unsigned q4 = t[260 + 4 * n0];
  const u32x4 v0 = ld16((lptr)(t + 8 * n0)); const unsigned w = *(LAS unsigned*)(t + 8 * n0 + 8);
  const unsigned mo = (n & 1) ? ~0u : 0u, lo = lane >= 32 ? ~0u : 0u, m31 = n == 31 ? ~0u : 0u;
  put(lane, dup16(b0), dup16(b0), bsel(mo, dup16(b0), b0 | (a0 << 16)), dup16(a0), dup16(a0));
  put(64 + lane, dup16(b1), dup16(b1), bsel(mo, dup16(b1), b1 | (a1 << 16)), dup16(a1), dup16(a1));
  put(128 + lane, dup16(c2), dup16(c2), dup16(ab >> 16), rot16(ab), dup16(ab & 0xffffu));
  put(192 + lane, bsel(lo, bsel(m31, dup16(w & 0xffffu), rot16(w)), dup16(q4)), bsel(lo, rot16(v0[3]), q4 | (v1[1] & 0xffff0000u)),
      bsel(lo, rot16(v0[2]), dup16(v1[1] & 0xffffu)), bsel(lo, rot16(v0[1]), (v1[1] & 0xffffu) | (v1[0] & 0xffff0000u)), bsel(lo, rot16(v0[0]), dup16(v1[0] & 0xffffu)));
  if (lane >= 60) put(319 - lane, lane == 63 ? rot16(v0[0]) : 0u, 0u, 0u, 0u, 0u);
}

struct ConvPlan { int n; int item[36][3]; };
constexpr ConvPlan conv_plan(int tmask, int maxt) {
  ConvPlan p{};
  int fifo[64] = {}, head = 0, tail = 0, carry[16] = {}, nc = 0, per = (tmask == 0xff) ? 1 : 2;
  for (int pass = 0; pass < 2; ++pass) {
    head = tail = 0;
    for (int i = 0; i < nc; ++i) fifo[tail++] = carry[i] + 32;
    int e = 0;
    for (int m = maxt; m >= 0; --m) {
      for (int j = 0; j + m <= maxt; ++j) {
        const bool on = (tmask >> (m + j)) & 1;
        int k = 0;
        const bool lastu = on && ((tmask >> j) & ((1 << m) - 1)) == 0;
        if (lastu) { if (j < maxt - 1) p.item[e][k++] = j; else fifo[tail++] = j; }
        if (j + m == maxt) { fifo[tail++] = 8 + 2 * m; fifo[tail++] = 9 + 2 * m; }
        if (on) { while (k < per && head < tail) p.item[e][k++] = fifo[head++]; while (k < 3) p.item[e][k++] = -1; ++e; }
      }
    }
    p.n = e; nc = 0;
    while (head < tail) carry[nc++] = fifo[head++];
  }
  return p;
}
template <int TMASK, class F>
DI void conv_units(lptr kr, lptr ub, int lane, int rho0, int nrho, f32x4 (&acc)[8], F hook) {
  constexpr int MAXT = (TMASK & 0x80) ? 7 : 5;
  constexpr ConvPlan plan = conv_plan(TMASK, MAXT);
  const int r = lane & 15, q = lane >> 4;
  lptr pk = kr + ((r & 1) ? 0 : KC1) + 4092 - 4 * r + 16 * q - 3584 - 64 * rho0 - 64;
  lptr pu = ub + ((r & 1) ? 0 : UC1) + 64 * (r >> 1) + 16 * q + 64 * (7 - rho0) - 64;
  lptr pk2 = pk + 8;
  u32x2 Kl[8], Kh[8]; u32x4 Uf[8];
  asm volatile("" : "+v"(pk), "+v"(pk2), "+v"(pu));
#pragma unroll
  for (int j = 0; j <= MAXT; ++j) { const int m = MAXT - j; Uf[j] = ld16(pu + 64 + 512 * j); Kl[m] = *(LAS u32x2*)(pk + 64 + 512 * (7 - m)); Kh[m] = *(LAS u32x2*)(pk2 + 64 + 512 * (7 - m)); SBAR(); }
#pragma unroll 1
  for (int it = 0; it < nrho; ++it) {
    int e = 0;
#pragma unroll
    for (int m = MAXT; m >= 0; --m) {
#pragma unroll
      for (int j = 0; j + m <= MAXT; ++j) {
        if ((TMASK >> (m + j)) & 1) {
          acc[m + j] = MFMA_BF(Uf[j], (u32x4{Kl[m][0], Kl[m][1], Kh[m][0], Kh[m][1]}), acc[m + j]);
#pragma unroll
          for (int k = 0; k < 3; ++k) {
            const int c = plan.item[e][k];
            if (c >= 0) {
              const int o = (c & 32) ? 64 : 0, cc = c & 31, mm = (cc - 8) >> 1;
              if (cc < 8) Uf[cc] = ld16(pu + o + 512 * cc);
              else if (cc & 1) Kh[mm] = *(LAS u32x2*)(pk2 + o + 512 * (7 - mm));
              else Kl[mm] = *(LAS u32x2*)(pk + o + 512 * (7 - mm));
            }
          }
          SBAR();
          ++e;
        }
      }
    }
    pk -= 64; pk2 -= 64; pu -= 64;
    asm volatile("" : "+v"(pk), "+v"(pk2), "+v"(pu));
    hook(it);
  }
}

DI u64 uni64(u64 v) { return (u64)(unsigned)__builtin_amdgcn_readfirstlane((unsigned)v) | ((u64)(unsigned)__builtin_amdgcn_readfirstlane((unsigned)(v >> 32)) << 32); }
DI int fresh_lane() { int l; asm volatile("v_mbcnt_lo_u32_b32 %0, -1, 0\n\tv_mbcnt_hi_u32_b32 %0, -1, %0" : "=v"(l)); return l; }
#define BAR() do { SBAR(); asm volatile("s_barrier" ::: "memory"); SBAR(); } while (0)
#define BAR_L() do { SBAR(); asm volatile("s_waitcnt lgkmcnt(0)\n\ts_barrier" ::: "memory"); SBAR(); } while (0)
#define PUBLISH(off) do { if (lane < FBS / 16) ST_SC1((u32x4{(unsigned)tag, (unsigned)(tag >> 32), (unsigned)tag, (unsigned)(tag >> 32)}), (off) + lane * 16); } while (0)
#define LD_SC1(off) __builtin_amdgcn_raw_buffer_load_b128(rs, (off), 0, SC1)
#define ST_SC1(v, off) __builtin_amdgcn_raw_buffer_store_b128((v), rs, (off), 0, SC1)

__global__ __launch_bounds__(512, 2) void fused_kernel(const u16* __restrict__ x, const u16* __restrict__ p_lar, const u16* __restrict__ p_aim,
                                                       const u16* __restrict__ p_cr, const u16* __restrict__ p_ci, const u16* __restrict__ p_ldt,
                                                       const u16* __restrict__ p_dsk, const u16* __restrict__ wg, const u16* __restrict__ wu,
                                                       const u16* __restrict__ wd, u16* __restrict__ out, char* ws) {
  extern __shared__ __attribute__((aligned(16))) char smem[];
  const int tid = __builtin_amdgcn_workitem_id_x(), bid = __builtin_amdgcn_workgroup_id_x();
  if (__builtin_amdgcn_readfirstlane(tid >> 6) == 6) {
    u64 pc_; unsigned len_;
    asm volatile("s_getpc_b64 %0\n.Lfk_head:\n\ts_mov_b32 %1, .Lfk_tail-.Lfk_head" : "=s"(pc_), "=s"(len_));
    asm volatile("s_mov_b32 m0, %2\n\ts_mov_b32 exec_hi, 0xff\n\tglobal_load_lds_dword %0, %1\n\ts_mov_b64 exec, -1"
                 :: "v"(umin((((bid >> 3) & 7) * 40 + (tid & 63)) * 128, len_)), "s"(pc_), "s"((unsigned)(uintptr_t)smem + L_RTB + 7936) : "memory");
  }
  lptr lds = (lptr)smem;
  const unsigned lds0 = (unsigned)(uintptr_t)smem;
  const unsigned fb = lds0 + L_FLG;
  const int wraw = __builtin_amdgcn_readfirstlane(tid >> 6), lane = tid & 63;
  const int wid = wraw ^ ((wraw >> 2) << 1);
  const int xcd = bid & 7, bi = bid >> 3;
  const int rb = (xcd >> 2) * 8 + (bi >> 2), cb = (xcd & 3) * 4 + (bi & 3);
  const int tg = (xcd >> 1) * 8 + (bi >> 2), nb = (xcd & 1) * 4 + (bi & 3);
  const int cw = xcd * 32 + bi, odd = cw & 1, c0 = 5 * (cw >> 1) + 2 * odd;
  const int chA = c0 + odd, chC = odd ? c0 : c0 + 2;
  const int rm = rb & 7, rot = 2 * (rm >= 5 ? rm - 5 : rm) + (rb >> 3);
  const bool csum = rm < 5;
  const __amdgpu_buffer_rsrc_t rs = __builtin_amdgcn_make_buffer_rsrc(ws, 0, (int)WS_END, 0x00020000);
  const int r = lane & 15, q = lane >> 4;

  u64 tag = 0;
  const int lw = wid - 4, kw = wid - 6;
  unsigned vx[4], vw[12];
  auto issue = [&](int s, int sbase, int h, int part = 3) {
    int ks = s + rot; if (ks >= 10) ks -= 10;
    const unsigned base = lds0 + sbase;
    const char* sx = (const char*)x + (size_t)(rb * 128 + h * 64) * 1280 - 4096 + ks * 128;
    const char* sw = (const char*)(h ? wu : wg) + cb * 192 - 4096 + (size_t)ks * 64 * 3072;
    if (part & 1) {
      glds4(sx, vx[0], vx[1], vx[2], vx[3], base + h * 8192);
      glds4(sw, vw[0], vw[1], vw[2], vw[3], base + 16384 + h * 12288);
      glds4(sx + 32 * 1280, vx[0], vx[1], vx[2], vx[3], base + h * 8192 + 4096);
    }
    if (part & 2) {
      glds4(sw, vw[4], vw[5], vw[6], vw[7], base + 16384 + h * 12288 + 4096);
      glds4(sw, vw[8], vw[9], vw[10], vw[11], base + 16384 + h * 12288 + 8192);
    }
  };
  auto prep_dma = [&](int ln) {
#pragma unroll
    for (int j = 0; j < 4; ++j) { const int row = 8 * j + (ln >> 3); vx[j] = row * 1280 + (((ln & 7) ^ ((row >> 1) & 7)) << 4) + 4096 - 1024 * j; }
#pragma unroll
    for (int j = 0; j < 3; ++j) { const int c = j * 64 + ln, row = (c * 43691) >> 19, pos = c - row * 12;
      const unsigned v = row * 3072 + ((pos ^ (((row >> 3) & 1) << 1)) << 4) + 4096;
#pragma unroll
      for (int g = 0; g < 4; ++g) vw[3 * g + j] = v + g * 16 * 3072 - 1024 * ((3 * g + j) & 3); }
  };
  auto wd_dma = [&](int ln, int w, int j0, int j1) {
    unsigned v[5];
#pragma unroll
    for (int i = 0; i < 5; ++i) { const int c = i * 64 + ln, row = (c * 6554) >> 16; v[i] = row * 1280 + (c - row * 10) * 16 + 4096 - (i & 3) * 1024; }
#pragma unroll
    for (int j = j0; j < j1; ++j) {
      const char* sp = (const char*)wd + (size_t)((w * 4 + ((j + nb) & 3)) * 96 + 32) * 1280 + nb * 160 - 4096;
      const unsigned d = lds0 + ((j >> 1) ? SL0 : SL2) + w * 10240 + (j & 1) * 5120;
      glds4(sp, v[0], v[1], v[2], v[3], d); glds1(sp, v[4], d + 4096);
    }
  };
  const int lnb = cw;
  const int ro0 = (0x100222 >> (4 * wid)) & 3, th0 = (0x000210 >> (4 * wid)) & 3, ro1 = (wid >> 1) & 1, th1 = 1 + (wid & 1);
  const int ch0 = c0 + ((0x0210 >> (4 * (ro0 + odd))) & 3), ch1 = chA + ro1;
  const int pt0 = ro0 * PCH + (ro0 >> 1) * (SL2 - SA - 2 * PCH) + th0 * 256, pt1 = ro1 * PCH + th1 * 256;
  unsigned o = (lane < 32 ? ch0 * 384 + th0 * 128 : ch1 * 384 + th1 * 128) + (lane & 31) * 4;
  unsigned xo0 = lane * 16, xo1 = q * 1280 + r * 16 + 1024;
  asm volatile("" : "+v"(o), "+v"(xo0), "+v"(xo1));
  asm volatile("" :: "s"(x), "s"(p_lar), "s"(p_aim), "s"(p_cr), "s"(p_ci), "s"(p_ldt), "s"(p_dsk), "s"(wg), "s"(wu), "s"(wd), "s"(out), "s"(ws));
  const int ptp = wid == 5 ? pt0 : pt1;
  if ((0x2f >> wid) & 1) {
    asm volatile("s_mov_b32 m0, %5\n\ts_nop 0\n\tglobal_load_lds_dword %0, %1\n\tglobal_load_lds_dword %0, %2 offset:784\n\t"
                 "global_load_lds_dword %0, %3 offset:1568\n\tglobal_load_lds_dword %0, %4 offset:2352"
                 :: "v"(o), "s"(p_lar), "s"((const char*)p_aim - 784), "s"((const char*)p_cr - 1568), "s"((const char*)p_ci - 2352), "s"(lds0 + SA + ptp) : "memory");
  }
  if (wid >= 6) {
    const char* xr = (const char*)x + (size_t)(lnb * 8 + kw * 4) * 1280;
#pragma unroll
    for (int j = 0; j < 4; ++j) glds1(xr + j * 1280, xo0, lds0 + L_LNB + (kw * 4 + j) * LNR);
    glds1(xr, xo1, lds0 + L_LNT + kw * 1024);
  } else if (wid >= 4) { if (lane < 16) *(LAS unsigned*)(lds + L_FLG + lane * 4) = 0; prep_dma(lane); }
  SBAR(); asm volatile("s_waitcnt lgkmcnt(0)\n\ts_barrier" ::: "memory"); SBAR();
  const float ldt0 = ubf(p_ldt, ch0), ldt1 = ubf(p_ldt, ch1);
  if (wid == 4 || wid == 5) {
    issue(0, SL0, lw);
  }
  auto lds_params = [&](float (&pv)[4], int m) {
#pragma unroll
    for (int e = 0; e < 4; ++e) pv[e] = bf1(*(LAS u16*)(lds + SA + ptp + e * PROW + m * 2));
  };
  if (wid >= 6) {
    waitv<0>();
    lptr xp = lds + L_LNB + (kw * 4 + q) * LNR + r * 16;
    u32x4 xv[5];
#pragma unroll
    for (int j = 0; j < 4; ++j) xv[j] = ld16(xp + 256 * j);
    xv[4] = ld16(lds + L_LNT + kw * 1024 + lane * 16);
    float v[40]; float s0 = 0.f, s1 = 0.f;
#pragma unroll
    for (int j = 0; j < 20; ++j) { const unsigned w = xv[j >> 2][j & 3]; v[2 * j] = bflo(w); v[2 * j + 1] = bfhi(w); s0 += v[2 * j]; s1 += v[2 * j + 1]; }
    const float mean = row16_sum(s0 + s1) * (1.f / DM);
    float q0 = 0.f, q1 = 0.f;
#pragma unroll
    for (int j = 0; j < 20; ++j) { v[2 * j] -= mean; v[2 * j + 1] -= mean; q0 = __builtin_fmaf(v[2 * j], v[2 * j], q0); q1 = __builtin_fmaf(v[2 * j + 1], v[2 * j + 1], q1); }
    const float var = rbf(row16_sum(q0 + q1) * (1.f / DM));
    const float mb = rbf(mean), sd = rbf(__builtin_amdgcn_sqrtf(rbf(var + 1e-5f)));
    const float rsd = __builtin_amdgcn_rcpf(sd), off = (mean - mb) * rsd;
#pragma unroll
    for (int j = 0; j < 20; ++j) xv[j >> 2][j & 3] = pkbf(__builtin_fmaf(v[2 * j], rsd, off), __builtin_fmaf(v[2 * j + 1], rsd, off));
#pragma unroll
    for (int j = 0; j < 5; ++j) st16(xp + 256 * j, xv[j]);
    if (r == 0) __builtin_amdgcn_raw_buffer_store_b64(u32x2{asu(mb), asu(sd)}, rs, WS_ST + (lnb * 8 + kw * 4 + q) * 8, 0, SC1);
  } else if (wid == 4) {
    issue(1, SL1, 0);
    const int c3 = cw >> 4, kt = (cw >> 3) & 1, unb = cw & 7;
#pragma unroll
    for (int j = 0; j < 5; ++j) { const int c = j * 64 + lane, row = (c * 6554) >> 16, pos = c - row * 10;
      glds1(wd, (c3 * 96 + row + kt * (row < 16 ? 80 : 48)) * 1280 + unb * 160 + pos * 16, lds0 + L_RTB + j * 1024); }
    glds1(wd, ((cw >> 4) * 96 + 32 + (cw & 15) * 2) * 1280 + lane * 16, lds0 + L_RTB + 5120); glds1(wd, ((cw >> 4) * 96 + 32 + (cw & 15) * 2) * 1280 + 1024 + lane * 16, lds0 + L_RTB + 6144);
    glds1(wd, ((cw >> 4) * 96 + 32 + (cw & 15) * 2) * 1280 + 2048 + (lane & 31) * 16, lds0 + L_RTB + 6144);
  } else {
    if (wid == 5) { __builtin_amdgcn_s_setprio(3); waitv<20>(); } else waitv<0>();
    float pv[1][4], pn[4];
    lds_params(pv[0], lane); lds_params(pn, 64 + lane);
    unsigned tb[1] = {lds0 + pt0}, sa_ = lds0 + L_SC + (ro0 * 3 + th0) * 4, inc = wid < 3;
    float ldt[1] = {ldt0};
#pragma unroll 1
    for (int rnd = wid == 5; rnd < 2; ++rnd) {
      float sc[1];
      kpool<1>(tb, pv, ldt, false, sc);
      asm volatile("s_mov_b64 exec, 1\n\tds_write_b32 %0, %1\n\tds_add_u32 %2, %3\n\ts_mov_b64 exec, -1" :: "v"(sa_), "v"(sc[0]), "v"(fb + FL_KP * 4), "v"(inc) : "memory");
#pragma unroll
      for (int e = 0; e < 4; ++e) pv[0][e] = pn[e];
      tb[0] = lds0 + pt1; sa_ = lds0 + L_SC + (ro1 * 3 + th1) * 4; inc = 0; ldt[0] = ldt1;
    }
  }

  if (wid >= 6) {
    const int lane = fresh_lane(), r = lane & 15, q = lane >> 4;
    const float dsk = ubf(p_dsk, chA + kw);
    u32x4 xv[5];
    u64 ep = __hip_atomic_load((GAS u64*)(ws + WS_EPOCH), RLX_AGENT);
    u32x4 wt[3], wf[12];
    flag_add(fb, FL_S2, lane);
    flag_wait(fb, FL_S2, 2);
#pragma unroll
    for (int j = 0; j < 5; ++j) xv[j] = tr2(lds + L_LNB + (r >> 2) * LNR + (kw * 5 + j) * 128 + q * 32 + (lane & 3) * 8, 4 * LNR);
#pragma unroll
    for (int j = 0; j < 5; ++j) ST_SC1(xv[j], WS_XB + lnb * 10240 + (kw * 5 + j) * 1024 + lane * 16);
    BAR_L();
    {
      const int sg = (q & 1) * 32;
      lptr pa = lds + SL0 + 16384 + kw * 12288 + (8 * q + (r >> 2)) * 192 + 8 * (lane & 3);
#pragma unroll
      for (int t = 0; t < 12; ++t) wf[t] = tr2(pa + (t / 6) * 6144 + (t % 6) * 32 + ((t & 1) ? -sg : sg), 768);
    }
    lptr pool = lds + SA + kw * PCH;
    KPtr kp; kp.init(pool, lane);
    u32x4 kv[2][6], kq[4];
    kp.fetch(kv[0], 0); kp.fetch(kv[1], 64);
    flag_wait(fb, FL_RT, 1);
#pragma unroll
    for (int j = 0; j < 3; ++j) wt[j] = tr2(lds + L_RTB + (4 * q + (r >> 2)) * 160 + (kw * 2 + j) * 32 + (lane & 3) * 8, 2560);
    BAR_L();
    tag = uni64(ep) + 1;
    *(volatile LAS u64*)(lds + L_FLG + FL_TAG * 4) = tag;
    f32x4 ko[3], ka[3][3];
    u32x4 f0 = {0, 0, 0, 0}, f1 = {0, 0, 0, 0};
    constexpr int HS0[9] = {0, 1, 4, 7, 10, 14, 17, 20, 24};
#pragma unroll
    for (int sl = 0; sl < 8; ++sl) {
      if (sl == 6) { f0 = LD_SC1(WS_F0 + (kw * 128 + lane) * FBS); f1 = LD_SC1(WS_F0 + (kw * 128 + 64 + lane) * FBS); SBAR(); }
      if (sl == 3 && kw == 0) PUBLISH(WS_F0 + cw * FBS);
#pragma unroll
      for (int hs = HS0[sl]; hs < HS0[sl + 1]; ++hs) {
        const int k = hs >> 1;
        if (!(hs & 1)) kprodh<0>(kv[k & 1], kq);
        else { kprodh<1>(kv[k & 1], kq); if (k + 2 < 12) kp.fetch(kv[k & 1], 64 * (k + 2)); kmma1(kq, ka[k >> 2], (k & 3) == 0); }
      }
      if (sl == 0) {
        if (csum) {
          const u32x4 ones = {0x3f803f80u, 0x3f803f80u, 0x3f803f80u, 0x3f803f80u};
          f32x4 cs[6];
          SBAR(); __builtin_amdgcn_s_setprio(3); SBAR();
#pragma unroll
          for (int t = 0; t < 6; ++t) cs[t] = MFMA_BF(ones, wf[t], (f32x4{0, 0, 0, 0}));
#pragma unroll
          for (int t = 0; t < 6; ++t) cs[t] = MFMA_BF(ones, wf[6 + t], cs[t]);
          SBAR(); __builtin_amdgcn_s_setprio(0); SBAR();
          const float cs0 = q == 0 ? cs[0][0] : q == 1 ? cs[1][0] : q == 2 ? cs[2][0] : cs[3][0], cs1 = q == 0 ? cs[4][0] : cs[5][0];
          const unsigned co = WS_CS + (cb * 10 + rot) * 768 + kw * 384 + lane * 4;
          __builtin_amdgcn_raw_buffer_store_b32(asu(cs0), rs, co, 0, SC1);
          if (lane < 32) __builtin_amdgcn_raw_buffer_store_b32(asu(cs1), rs, co + 256, 0, SC1);
        }
#pragma unroll
        for (int f = 0; f < 3; ++f) if (f || !kw) ST_SC1(wt[f], WS_WD + ((cw & 7) * 48 + (cw >> 4) * 3 + ((cw >> 3) & 1)) * 5120 + (kw * 2 + f) * 1024 + lane * 16);
      }
      if (sl == 2) waitv<0>();
      if (sl == 6) {
        const unsigned tl = (unsigned)tag, th = (unsigned)(tag >> 32);
        _Pragma("nounroll") for (int sp = 0; sp < SPIN_GLB; ++sp) {
          if (wall(((f0[0] ^ tl) | (f0[1] ^ th) | (f0[2] ^ tl) | (f0[3] ^ th) | (f1[0] ^ tl) | (f1[1] ^ th) | (f1[2] ^ tl) | (f1[3] ^ th)) == 0u)) break;
          asm volatile("" ::: "memory");
          f0 = LD_SC1(WS_F0 + (kw * 128 + lane) * FBS); f1 = LD_SC1(WS_F0 + (kw * 128 + 64 + lane) * FBS);
          __builtin_amdgcn_s_sleep(2);
        }
      }
      BAR();
    }
    kcombine(ko, ka, *(LAS float*)(lds + L_SC + kw * 12), *(LAS float*)(lds + L_SC + kw * 12 + 4), *(LAS float*)(lds + L_SC + kw * 12 + 8));
    kstore(pool + O_KL, lane, ko, dsk);
    kexpand(pool + O_KREV, pool + O_KL, lane);
    flag_set(fb, FL_KA + kw, 1);
  } else {
    if (wid >= 4) {
      const int lane = fresh_lane();
      __builtin_amdgcn_s_setprio(0);
      u32x4 cv[6][6];
      {
        KPtr kp; kp.init(lds + SL2 + lw * 384, lane);
        flag_wait(fb, FL_KP, 3);
#pragma unroll
        for (int i = 0; i < 6; ++i) kp.fetch(cv[i], 64 * i);
      }
      const float sca = *(LAS float*)(lds + L_SC + 24 + lw * 4), scb = *(LAS float*)(lds + L_SC + 24 + lw * 8), scc = *(LAS float*)(lds + L_SC + 28 + lw * 4);
      SBAR();
      prep_dma(fresh_lane());
      if (lw) { issue(1, SL1, 1); waitv<20>(); } else waitv<28>();
      BAR_L();
      if (!lw) { waitv<0>(); flag_set(fb, FL_RT, 1); }
      issue(2, SL2, lw, 1);
      f32x4 ka[3][3], kc[3];
#pragma unroll
      for (int s = 0; s < 7; ++s) {
        if (s == 0) waitv<12>(); else waitv<20>();
        BAR();
        if (s == 0) issue(2, SL2, lw, 2);
        issue(s + 3, s % 3 == 0 ? SL0 : s % 3 == 1 ? SL1 : SL2, lw);
        if (s < 6) { u32x4 o[4]; kprod(cv[s], o); kmma1(o, ka[s >> 1], !(s & 1)); }
      }
      kcombine(kc, ka, sca, scb, scc);
      if (lw == 0) {
#pragma unroll
        for (int i = 0; i < 3; ++i) *(LAS f32x4*)(lds + L_STG + i * 1024 + lane * 16) = kc[i];
      }
      waitv<20>();
      BAR_L();
      tag = uni64(*(volatile LAS u64*)(lds + L_FLG + FL_TAG * 4));
      u32x4 uc[8]; unsigned uprev;
      const int n0 = lw * 128 + (lane >> 2) * 8;
      const unsigned uo = WS_XB + n0 * 10240 + (c0 + (lane & 3)) * 16;
      u32x4 sv, cp[10];
      if (lw == 0) {
        sv = LD_SC1(WS_ST + rb * 1024 + lane * 16);
#pragma unroll
        for (int j = 0; j < 10; ++j) cp[j] = LD_SC1(WS_CS + (cb * 10 + j) * 768 + (lane < 48 ? lane : 0) * 16);
      }
#pragma unroll
      for (int j = 0; j < 8; ++j) uc[j] = LD_SC1(uo + j * 10240);
      uprev = __builtin_amdgcn_raw_buffer_load_b32(rs, uo - 10240 + 12, 0, SC1);
      SBAR();
      if (lw) {
#pragma unroll
        for (int i = 0; i < 3; ++i) kc[i] += *(LAS f32x4*)(lds + L_STG + i * 1024 + lane * 16);
        kstore(lds + L_PC, lane, kc, ubf(p_dsk, chC));
        waitv<9>();
      } else waitv<20>();
      BAR();
      if (lw == 0) {
        f32x4 cs = {0, 0, 0, 0};
#pragma unroll
        for (int j = 0; j < 10; ++j) cs += __builtin_bit_cast(f32x4, cp[j]);
        st16(lds + L_STAT + lane * 16, sv);
        if (lane < 48) *(LAS f32x4*)(lds + L_CS + lane * 16) = cs;
        flag_set(fb, FL_EPI, 1);
        if (bid == 0 && lane == 0) __hip_atomic_store((GAS u64*)(ws + WS_EPOCH), tag, RLX_AGENT);
      }
      if (n0 == 0) uprev = 0;
      if ((lane & 3) < 3) {
        lptr ud = lds + L_U + (lane & 3) * UCH + 448 + n0 * 16;
#pragma unroll
        for (int j = 0; j < 8; ++j) {
          st16(ud + j * 16, uc[j]);
          const unsigned pw_ = j ? uc[j - 1][3] : uprev;
          st16(ud + UC1 + j * 16, u32x4{__builtin_amdgcn_alignbit(uc[j][0], pw_, 16), __builtin_amdgcn_alignbit(uc[j][1], uc[j][0], 16),
                                        __builtin_amdgcn_alignbit(uc[j][2], uc[j][1], 16), __builtin_amdgcn_alignbit(uc[j][3], uc[j][2], 16)});
        }
      }
      if (lane < 28) {
#pragma unroll
        for (int c = 0; c < 3; ++c) st16(lds + L_U + c * UCH + lw * UC1 + lane * 16, u32x4{0, 0, 0, 0});
      }
      flag_add(fb, FL_U, lane);
      if (lw) { kexpand(lds + L_KC, lds + L_PC, lane); flag_set(fb, FL_KC, 1); }

    } else {
      f32x4 acc[2][3][4];
      __builtin_amdgcn_s_setprio(2);
      const int tw = wid >> 1, fw = wid & 1;
      lptr pB0 = lds + (tw * 64 + r) * 128 + ((q ^ (r >> 1)) << 4);
      lptr pB1 = lds + (tw * 64 + r) * 128 + (((q ^ (r >> 1)) ^ 4) << 4);
      const int sg = (q & 1) * 32;
      lptr pAb = lds + 16384 + (8 * q + (r >> 2)) * 192 + 8 * (lane & 3);
      lptr pA[3] = {pAb + (fw ? 160 - sg : sg), pAb + (fw ? 128 + sg : 32 - sg), pAb + (fw ? 96 - sg : 64 + sg)};
      u32x4 A0[2][3], B0[4], A1[2][3], B1[4];
      auto rd = [&](u32x4 (&A)[2][3], u32x4 (&B)[4], int off, int kk) {
#pragma unroll
        for (int tb = 0; tb < 4; ++tb) B[tb] = ld16((kk ? pB1 : pB0) + off + tb * 2048);
#pragma unroll
        for (int m = 0; m < 2; ++m)
#pragma unroll
          for (int ii = 0; ii < 3; ++ii) A[m][ii] = tr2(pA[ii] + off + m * 12288 + kk * 6144, 768);
      };
      auto mm = [&](u32x4 (&A)[2][3], u32x4 (&B)[4]) {
#pragma unroll
        for (int e = 0; e < 24; ++e) { const int m = e / 12, ii = (e / 4) % 3, tb = e % 4; acc[m][ii][tb] = MFMA_BF(A[m][ii], B[tb], acc[m][ii][tb]); }
      };
      auto sched = [&]() {
#pragma unroll
        for (int k = 0; k < 16; ++k) { SGB(0x8, 1); SGB(0x100, 1); }
        SGB(0x8, 8);
      };
      BAR_L();
      rd(A0, B0, SL0, 0);
      SBAR();
      rd(A1, B1, SL0, 1);
#pragma unroll
      for (int e = 0; e < 24; ++e) { const int m = e / 12, ii = (e / 4) % 3, tb = e % 4; acc[m][ii][tb] = MFMA_BF(A0[m][ii], B0[tb], (f32x4{0, 0, 0, 0})); }
      sched();
      int s0 = SL1, s1 = SL2, s2 = SL0;
#pragma unroll 1
      for (int s = 0; s < 9; ++s) {
        BAR_L();
        rd(A0, B0, s0, 0); mm(A1, B1); sched();
        SBAR();
        rd(A1, B1, s0, 1); mm(A0, B0); sched();
        const int t = s0; s0 = s1; s1 = s2; s2 = t;
      }
      SBAR();
      asm volatile("s_waitcnt lgkmcnt(0)\n\ts_mov_b64 exec, 1\n\tds_add_u32 %0, %1\n\ts_mov_b64 exec, -1" :: "v"(fb + FL_G1 * 4), "v"(1u) : "memory");
      auto mlast = [&](int ii, int tb) { acc[0][ii][tb] = MFMA_BF(A1[0][ii], B1[tb], acc[0][ii][tb]); acc[1][ii][tb] = MFMA_BF(A1[1][ii], B1[tb], acc[1][ii][tb]); };
      mlast(0, 0); mlast(1, 0); mlast(0, 1); mlast(1, 1);
      SBAR();
      flag_wait(fb, FL_EPI, 1);
      {
        f32x2 st[4];
#pragma unroll
        for (int tb = 0; tb < 4; ++tb) st[tb] = *(LAS f32x2*)(lds + L_STAT + (tw * 64 + tb * 16 + r) * 8);
        lptr pcs = lds + L_CS + (fw * 48 + 4 * q) * 4;
        const int oa = fw ? 128 : 0, oc = fw ? 0 : 128;
        const f32x4 cga = *(LAS f32x4*)(pcs + oa), cua = *(LAS f32x4*)(pcs + 384 + oa), cgb = *(LAS f32x4*)(pcs + 64), cub = *(LAS f32x4*)(pcs + 384 + 64);
        const u64 tv = *(volatile LAS u64*)(lds + L_FLG + FL_TAG * 4);
        SBAR();
        mlast(0, 2); mlast(1, 2); mlast(0, 3); mlast(1, 3);
        SBAR();
        tag = uni64(tv);
        float nmb[4], al[4], sd[4];
#pragma unroll
        for (int tb = 0; tb < 4; ++tb) { nmb[tb] = -st[tb].x; sd[tb] = st[tb].y; al[tb] = -1.44269504f * __builtin_amdgcn_rcpf(st[tb].y); }
        const unsigned hoff = WS_H + ((rb * 2 + tw) * 16 + cb) * 12288 + lane * 16;
        auto hcalc = [&](const f32x4& g4, const f32x4& u4, const f32x4& cg, const f32x4& cu, int tb) {
          float hv[4];
#pragma unroll
          for (int j = 0; j < 4; ++j) {
            const float g = __builtin_fmaf(nmb[tb], cg[j], g4[j]) * al[tb];
            const float u = __builtin_fmaf(nmb[tb], cu[j], u4[j]);
            const float e = __builtin_amdgcn_exp2f(g);
            hv[j] = u * __builtin_amdgcn_rcpf(__builtin_fmaf(e, sd[tb], sd[tb]));
          }
          return u32x2{pkbf(hv[0], hv[1]), pkbf(hv[2], hv[3])};
        };
#pragma unroll
        for (int tb = 0; tb < 4; ++tb) {
          const u32x2 ha = hcalc(acc[0][0][tb], acc[1][0][tb], cga, cua, tb);
          const u32x2 hb = hcalc(acc[0][1][tb], acc[1][1][tb], cgb, cub, tb);
          ST_SC1((u32x4{ha[0], ha[1], hb[0], hb[1]}), hoff + fw * 4096 + tb * 1024);
          SBAR();
        }
        mlast(2, 0); mlast(2, 1); mlast(2, 2); mlast(2, 3);
        SBAR();
        {
          const f32x4 cg = *(LAS f32x4*)(pcs + oc), cu = *(LAS f32x4*)(pcs + 384 + oc);
          u32x2 hs[4];
#pragma unroll
          for (int tb = 0; tb < 4; ++tb) { hs[tb] = hcalc(acc[0][2][tb], acc[1][2][tb], cg, cu, tb); asm volatile("" : "+v"(hs[tb])); SBAR(); }
          waitv<0>();
          PUBLISH(WS_H1 + (((rb * 2 + tw) * 16 + cb) * 2 + fw) * FBS);
#pragma unroll
          for (int t2 = 0; t2 < 2; ++t2) ST_SC1((u32x4{hs[2 * t2][0], hs[2 * t2][1], hs[2 * t2 + 1][0], hs[2 * t2 + 1][1]}), hoff + 8192 + fw * 2048 + t2 * 1024);
        }
      }
    {
    const int sw = wid ^ 2, hm = sw & 1;
    lptr am = lds + SA + hm * PCH;
    f32x4 ca[8];
#pragma unroll
    for (int t = 0; t < 8; ++t) ca[t] = f32x4{0, 0, 0, 0};
    lptr pmain = am + O_KL + 1312 + lane * 16;
    flag_wait(fb, FL_KA + hm, 1);
    flag_wait(fb, FL_U, 2);
    conv_units<0xff>(am + O_KREV, lds + L_U + (hm + odd) * UCH, lane, sw < 2 ? 5 : 0, sw < 2 ? 3 : 5, ca, [&](int it) {
      if (it == 0) { waitv<0>(); PUBLISH(WS_H2 + (((rb * 2 + tw) * 16 + cb) * 2 + fw) * FBS); } });
    if (sw < 2) {
      lptr pc = lds + L_PC;
#pragma unroll
      for (int t = 0; t < 8; ++t) { *(LAS f32x4*)(pmain + t * 1024) = ca[t]; ca[t] = f32x4{0, 0, 0, 0}; }
      flag_set(fb, FL_PA + hm, 1);
      flag_wait(fb, FL_KC, 1);
      lptr uc_ = lds + L_U + (odd ? 0 : 2) * UCH;
      if (odd) conv_units<0x3a>(lds + L_KC, uc_, lane, hm * 4, 4, ca, [](int) {});
      else conv_units<0xc5>(lds + L_KC, uc_, lane, hm * 4, 4, ca, [](int) {});
      f32x4 c4[4];
      c4[0] = odd ? ca[1] : ca[0]; c4[1] = odd ? ca[3] : ca[2]; c4[2] = odd ? ca[4] : ca[6]; c4[3] = odd ? ca[5] : ca[7];
      {
        lptr po = pc + hm * 2048, pi = pc + (hm ^ 1) * 2048;
        *(LAS f32x4*)(po + lane * 16) = hm ? c4[0] : c4[2]; *(LAS f32x4*)(po + 1024 + lane * 16) = hm ? c4[1] : c4[3];
        flag_add(fb, FL_PC, lane);
        flag_wait(fb, FL_PC, 2);
        const f32x4 d0 = (hm ? c4[2] : c4[0]) + *(LAS f32x4*)(pi + lane * 16), d1 = (hm ? c4[3] : c4[1]) + *(LAS f32x4*)(pi + 1024 + lane * 16);
        *(LAS unsigned*)(pi + q * 128 + r * 4) = pkbf(d0[0], d0[1]); *(LAS unsigned*)(pi + q * 128 + r * 4 + 64) = pkbf(d0[2], d0[3]);
        *(LAS unsigned*)(pi + 512 + q * 128 + r * 4) = pkbf(d1[0], d1[1]); *(LAS unsigned*)(pi + 512 + q * 128 + r * 4 + 64) = pkbf(d1[2], d1[3]);
        const int T = ((odd ? 0x5431 : 0x7620) >> (4 * (2 * hm + (lane >> 5)))) & 7;
        ST_SC1(ld16(pi + lane * 16), WS_Y + ((T * 4 + ((lane >> 3) & 3)) * DM + chC) * 128 + (lane & 7) * 16);
      }
    } else {
      flag_wait(fb, FL_PA + hm, 1);
#pragma unroll
      for (int t = 0; t < 8; ++t) ca[t] += *(LAS f32x4*)(pmain + t * 1024);
#pragma unroll
      for (int t = 0; t < 8; ++t) { *(LAS unsigned*)(am + t * 512 + q * 128 + r * 4) = pkbf(ca[t][0], ca[t][1]); *(LAS unsigned*)(am + t * 512 + q * 128 + r * 4 + 64) = pkbf(ca[t][2], ca[t][3]); }
#pragma unroll
      for (int i = 0; i < 4; ++i) ST_SC1(ld16(am + i * 1024 + lane * 16), WS_Y + ((i * 8 + (lane >> 3)) * DM + chA + hm) * 128 + (lane & 7) * 16);
    }
    waitv<0>();
    if (flag_add(fb, FL_YC, lane) == 3u) {
      PUBLISH(WS_FY + cw * FBS);
      __builtin_amdgcn_s_sleep(24);
      _Pragma("nounroll") for (int sp = 0; sp < SPIN_GLB; ++sp) { if (wall(gflag_ok(ws, WS_FY + (nb * 32 + (lane & 31)) * FBS, tag))) break; __builtin_amdgcn_s_sleep(8); }
      flag_set(fb, FL_YOK, 1);
    }
    flag_wait(fb, FL_YOK, 1);
    u32x4 yv[3];
#pragma unroll
    for (int j = 0; j < 3; ++j) {
      const int p = umin(sw + 4 * j, 9), c = p * 64 + lane, row = (c * 6554) >> 16;
      glds1((const char*)x + (size_t)tg * 64 * 1280 + nb * 160, row * 1280 + (c - row * 10) * 16, lds0 + L_XT + p * 1024);
    }
#pragma unroll
    for (int j = 0; j < 3; ++j) { const int p = umin(sw + 4 * j, 9); yv[j] = LD_SC1(WS_Y + (tg * DM + nb * 80 + p * 8) * 128 + lane * 16); }
    lptr yb = lds + L_YB + sw * 3072;
#pragma unroll
    for (int j = 0; j < 3; ++j) st16(yb + j * 1024 + lane * 16, yv[j]);
#pragma unroll
    for (int j = 0; j < 3; ++j) {
      const u32x4 a = tr2(yb + j * 1024 + (r >> 2) * 128 + q * 32 + (lane & 3) * 8, 512);
      if (sw + 4 * j < 10) st16(lds + L_BASE + lane * 160 + (sw + 4 * j) * 16, a);
    }
    }
      __builtin_amdgcn_s_setprio(0);
    }
  }

  if (wid >= 4) {
    const int sw = wid - 4;
    const int lane = fresh_lane();
    {
      unsigned bo[4], ao[4];
#pragma unroll
      for (int c = 0; c < 4; ++c) { const int cc = sw * 4 + ((c + nb) & 3); bo[c] = WS_WD + (nb * 48 + cc * 3) * 5120; ao[c] = WS_H + (tg * 16 + cc) * 12288; }
      const unsigned l16 = lane * 16;
#define BFRAG(k, n) __builtin_amdgcn_raw_buffer_load_b128(rs, l16, bo[(k) < 8 ? (k) >> 1 : (k) - 8] + ((k) < 8 ? ((k) & 1) * 5120 : 10240) + (n) * 1024, SC1)
#define AFRAG(k, t) __builtin_amdgcn_raw_buffer_load_b128(rs, l16, ao[(k) < 8 ? (k) >> 1 : (k) - 8] + ((k) < 8 ? ((k) & 1) * 4096 : 8192) + (t) * 1024, SC1)
      u32x4 Bw[4][5];
#pragma unroll
      for (int k = 0; k < 4; ++k)
#pragma unroll
        for (int n = 0; n < 5; ++n) Bw[k][n] = BFRAG(k, n);
      wd_dma(lane, sw, 0, 2);
      flag_wait(fb, FL_G1, 4);
      wd_dma(lane, sw, 2, 4);
      lptr park = lds + SL2 + sw * 10240 + (lane >> 2) * 160 + (lane & 3) * 8;
      asm volatile("" : "+v"(park));
      const unsigned fo = WS_H1 + (tg * 32 + sw * 8 + (lane & 7)) * FBS;
      _Pragma("nounroll") for (int sp = 0; sp < SPIN_GLB; ++sp) { if (wall(gflag_ok(ws, fo, tag))) break; __builtin_amdgcn_s_sleep(2); }
      asm volatile("" ::: "memory");
      f32x4 a2[5][4];
#pragma unroll
      for (int n = 0; n < 5; ++n)
#pragma unroll
        for (int tb = 0; tb < 4; ++tb) a2[n][tb] = f32x4{0, 0, 0, 0};
      u32x4 Ah[4][4];
#pragma unroll
      for (int k = 0; k < 3; ++k)
#pragma unroll
        for (int tb = 0; tb < 4; ++tb) Ah[k][tb] = AFRAG(k, tb);
      u64 f2 = 0;
      SBAR();
#pragma unroll
      for (int k = 0; k < 12; ++k) {
        if (k == 3) { f2 = __hip_atomic_load((GAS u64*)(ws + fo + 1024 * FBS), RLX_AGENT); SBAR(); }
        if (k == 5) {
          unsigned tc;
          asm volatile("s_mov_b32 %1, %5\n\t1:\n\tv_cmp_ne_u64 vcc, %2, %0\n\ts_cbranch_vccz 2f\n\ts_sleep 2\n\t"
                       "global_load_dwordx2 %0, %3, %4 sc1\n\ts_sub_u32 %1, %1, 1\n\ts_waitcnt vmcnt(0)\n\ts_cbranch_scc0 1b\n\t2:"
                       : "+v"(f2), "=&s"(tc) : "s"(tag), "v"(fo), "s"(ws + 1024 * FBS), "i"(SPIN_GLB) : "memory", "vcc", "scc");
          SBAR();
        }
        u32x4 hf[4];
        if (k >= 8) {
          const u32x4 X0 = Ah[k & 3][0], X1 = Ah[k & 3][1], Y0 = Ah[k & 3][2], Y1 = Ah[k & 3][3];
          hf[0] = u32x4{X0[0], X0[1], Y0[0], Y0[1]}; hf[1] = u32x4{X0[2], X0[3], Y0[2], Y0[3]};
          hf[2] = u32x4{X1[0], X1[1], Y1[0], Y1[1]}; hf[3] = u32x4{X1[2], X1[3], Y1[2], Y1[3]};
        } else {
#pragma unroll
          for (int tb = 0; tb < 4; ++tb) hf[tb] = Ah[k & 3][tb];
        }
#pragma unroll
        for (int e = 0; e < 20; ++e) {
          const int n = e >> 2, tb = e & 3;
          a2[n][tb] = MFMA_BF(Bw[k & 3][n], hf[tb], a2[n][tb]);
          if (e & 1) {
            const int j = e >> 1;
            if (j < 4) { if (k + 3 < 12) Ah[(k + 3) & 3][j] = AFRAG(k + 3, j); }
            else if (j < 9 && k >= 1 && k + 3 < 12) {
              if (k + 3 < 8) Bw[(k + 3) & 3][j - 4] = BFRAG(k + 3, j - 4);
              else Bw[(k + 3) & 3][j - 4] = tr2(park + ((k - 5) >> 1) * (SL0 - SL2) + ((k - 5) & 1) * 5120 + (j - 4) * 32, 2560);
            }
            SBAR();
          }
        }
#pragma unroll
        for (int n = 0; n < 5; ++n) asm volatile("" : "+v"(a2[n][0]), "+v"(a2[n][1]), "+v"(a2[n][2]), "+v"(a2[n][3]));
      }
      {
        lptr red = lds + SL2 + sw * 10240;
        const int l2 = fresh_lane(), r = l2 & 15, q = l2 >> 4;
#pragma unroll
        for (int n = 0; n < 5; ++n)
#pragma unroll
          for (int tb = 0; tb < 4; ++tb) {
            *(LAS f32x4*)(red + (q >> 1) * (SL0 - SL2) + (2 * n + (q & 1)) * 1024 + ((tb * 16 + r + 3 * n) & 63) * 16) = a2[n][tb];
          }
      }
    }
  }
  BAR_L();
  if (wid < 5) {
    const int tid = wid * 64 + fresh_lane();
    const int tok = (tid * 13108) >> 16, g = tid - tok * 5;
    lptr rp = lds + SL2 + g * 2048 + ((tok + 3 * g) & 63) * 16;
    f32x4 s[4];
#pragma unroll
    for (int m = 0; m < 4; ++m) {
      lptr p = rp + (m >> 1) * (SL0 - SL2) + (m & 1) * 1024;
      s[m] = (*(LAS f32x4*)(p) + *(LAS f32x4*)(p + 10240)) + (*(LAS f32x4*)(p + 20480) + *(LAS f32x4*)(p + 30720));
    }
    u16* op = out + (size_t)(tg * 64 + tok) * DM + nb * 80 + g * 16;
#pragma unroll
    for (int hh = 0; hh < 2; ++hh) {
      const u32x4 bs = ld16(lds + L_BASE + tid * 32 + hh * 16), xs = ld16(lds + L_XT + tid * 32 + hh * 16);
      const f32x4 sa = s[2 * hh], sb = s[2 * hh + 1];
      *(u32x4*)(op + hh * 8) = u32x4{pkbf((bflo(xs[0]) + bflo(bs[0])) + sa[0], (bfhi(xs[0]) + bfhi(bs[0])) + sa[1]), pkbf((bflo(xs[1]) + bflo(bs[1])) + sa[2], (bfhi(xs[1]) + bfhi(bs[1])) + sa[3]),
                                     pkbf((bflo(xs[2]) + bflo(bs[2])) + sb[0], (bfhi(xs[2]) + bfhi(bs[2])) + sb[1]), pkbf((bflo(xs[3]) + bflo(bs[3])) + sb[2], (bfhi(xs[3]) + bfhi(bs[3])) + sb[3])};
    }
  }
  asm volatile(".Lfk_tail:");
}

#if defined(__HIP_DEVICE_COMPILE__)
#pragma clang attribute pop
#endif

extern "C" void kernel_launch(void* const* d_in, const int* in_sizes, int n_in, void* d_out, int out_size, void* d_ws, size_t ws_size, hipStream_t stream) {
  (void)in_sizes; (void)n_in; (void)out_size; (void)ws_size;
  (void)hipFuncSetAttribute((const void*)fused_kernel, hipFuncAttributeMaxDynamicSharedMemorySize, LDS_BYTES);
  fused_kernel<<<256, 512, LDS_BYTES, stream>>>((const u16*)d_in[0], (const u16*)d_in[1], (const u16*)d_in[2], (const u16*)d_in[3], (const u16*)d_in[4],
                                                (const u16*)d_in[5], (const u16*)d_in[6], (const u16*)d_in[8], (const u16*)d_in[9], (const u16*)d_in[10],
                                                (u16*)d_out, (char*)d_ws);
}
```
